```python
import torch

def _softmax(x, axis=-1):
    return torch.softmax(x, dim=axis)

def _gelu(x):
    return torch.nn.functional.gelu(x, approximate='tanh')

def _silu(x):
    return torch.nn.functional.silu(x)

def _sigmoid(x):
    return torch.sigmoid(x)

def _layernorm(x, axis=-1, eps=1e-05):
    m = x.mean(dim=axis, keepdim=True)
    v = x.var(dim=axis, keepdim=True, unbiased=False)
    return (x - m) / torch.sqrt(v + eps)

def _scatter_add(tgt, idx, src, axis=0):
    return tgt.clone().index_add_(axis, idx, src)

def _segment_sum(src, seg, n):
    out = torch.zeros((n,) + src.shape[1:], dtype=src.dtype)
    return out.index_add_(0, seg, src)

def _pad(x, pw):
    flat = tuple((v for pair in reversed(pw) for v in pair))
    return torch.nn.functional.pad(x, flat)
kernel_name = 'interleave_layers__memory_aug_recurrent_mem_tr__swiglu_ffn__prompt'

def setup_inputs():

    _w = lambda a: (lambda t: t.to(torch.bfloat16) if t.is_floating_point() else t)(torch.as_tensor(a))

    def _A_setup():
        B, S, Dm = (1, 2048, 512)
        H, Dh = (32, 128)
        M, seg = (8, 32)
        seg = min(seg, S)
        S = S // seg * seg
        return {'x': _w((lambda *s: torch.randn(*s, dtype=torch.bfloat16) * 0.05)(B, S, Dm)), 'mem0': _w((lambda *s: torch.randn(*s, dtype=torch.bfloat16) * 0.8)(B, M, Dm)), 'Wq': _w((lambda *s: torch.randn(*s, dtype=torch.bfloat16) * 0.8)(Dm, H, Dh) * Dm ** (-0.5)), 'Wk': _w((lambda *s: torch.randn(*s, dtype=torch.bfloat16) * 0.8)(Dm, H, Dh) * Dm ** (-0.5)), 'Wv': _w((lambda *s: torch.randn(*s, dtype=torch.bfloat16) * 0.8)(Dm, H, Dh) * Dm ** (-0.5)), 'Wo': _w((lambda *s: torch.randn(*s, dtype=torch.bfloat16) * 0.8)(H, Dh, Dm) * (H * Dh) ** (-0.5)), 'seg_len': seg}
    _A_in = _A_setup()

    def _B_setup():
        B, S, D, F = (1, 2048, 512, 1376)
        return {'x': _w((lambda *s: torch.randn(*s, dtype=torch.bfloat16) * 0.05)(B, S, D)), 'w_gate': _w((lambda *s: torch.randn(*s, dtype=torch.bfloat16) * 0.05)(D, F)), 'w_up': _w((lambda *s: torch.randn(*s, dtype=torch.bfloat16) * 0.05)(D, F)), 'w_down': _w((lambda *s: torch.randn(*s, dtype=torch.bfloat16) * 0.05)(F, D))}
    _B_in = _B_setup()
    return {'A_x': _A_in['x'], 'A_mem0': _A_in['mem0'], 'A_Wq': _A_in['Wq'], 'A_Wk': _A_in['Wk'], 'A_Wv': _A_in['Wv'], 'A_Wo': _A_in['Wo'], 'A_seg_len': _A_in['seg_len'], 'B_x': _B_in['x'], 'B_w_gate': _B_in['w_gate'], 'B_w_up': _B_in['w_up'], 'B_w_down': _B_in['w_down']}

def reference(A_x, A_mem0, A_Wq, A_Wk, A_Wv, A_Wo, A_seg_len, B_x, B_w_gate, B_w_up, B_w_down):

    def _A_ref(x, mem0, Wq, Wk, Wv, Wo, seg_len):
        B, S, Dm = x.shape
        H, Dh = (Wq.shape[1], Wq.shape[2])
        M = mem0.shape[1]
        scale = Dh ** (-0.5)
        n_seg = S // seg_len
        L = M + seg_len
        idx = torch.arange(L)
        mask = (idx[:, None] < M) | (idx[:, None] >= idx[None, :])
        mem = mem0
        outs = []
        for s in range(n_seg):
            xs = x[:, s * seg_len:(s + 1) * seg_len, :].clone()
            h = torch.concatenate([mem, xs], axis=1)
            q = torch.einsum('btd,dhe->bhte', h, Wq)
            k = torch.einsum('btd,dhe->bhte', h, Wk)
            v = torch.einsum('btd,dhe->bhte', h, Wv)
            scores = torch.einsum('bhqe,bhke->bhqk', q, k) * scale
            scores = torch.where(mask[None, None], scores, -1000000000.0)
            p = _softmax(scores, axis=-1)
            o = torch.einsum('bhqk,bhke->bhqe', p, v)
            o = torch.einsum('bhte,hed->btd', o, Wo)
            mem = o[:, :M, :].clone()
            outs.append(o[:, M:, :])
        return torch.concatenate(outs, axis=1)

    def _B_ref(x, w_gate, w_up, w_down):
        g = _silu(x @ w_gate)
        u = x @ w_up
        return g * u @ w_down
    x = A_x
    x = x + _A_ref(_layernorm(x, axis=-1), A_mem0, A_Wq, A_Wk, A_Wv, A_Wo, A_seg_len)
    x = x + _B_ref(_layernorm(x, axis=-1), B_w_gate, B_w_up, B_w_down)
    x = x + _A_ref(_layernorm(x, axis=-1), A_mem0, A_Wq, A_Wk, A_Wv, A_Wo, A_seg_len)
    x = x + _B_ref(_layernorm(x, axis=-1), B_w_gate, B_w_up, B_w_down)
    return x
```

```cpp
#include <hip/hip_runtime.h>
#include <hip/hip_bf16.h>
typedef unsigned short u16;
__device__ __forceinline__ float b2f(u16 x) { return __builtin_bit_cast(float, (unsigned)x << 16); }
__device__ __forceinline__ u16 f2b(float f) { __hip_bfloat16 h = __float2bfloat16(f); return __builtin_bit_cast(u16, h); }
__device__ __forceinline__ float rb(float f) { return b2f(f2b(f)); }

__global__ void ln_k(const u16* x, u16* y) {
  int row = blockIdx.x, l = threadIdx.x; float v[8]; float s = 0;
  for (int j = 0; j < 8; ++j) { v[j] = b2f(x[row * 512 + l * 8 + j]); s += v[j]; }
  for (int o = 32; o; o >>= 1) s += __shfl_xor(s, o);
  float m = s / 512; float q = 0;
  for (int j = 0; j < 8; ++j) q += (v[j] - m) * (v[j] - m);
  for (int o = 32; o; o >>= 1) q += __shfl_xor(q, o);
  float r = rsqrtf(q / 512 + 1e-5f);
  for (int j = 0; j < 8; ++j) y[row * 512 + l * 8 + j] = f2b((v[j] - m) * r);
}
__global__ void gemm_k(const u16* A, int lda, const u16* B, int ldb, u16* C, int ldc, const u16* res, int M, int N, int K) {
  __shared__ float sa[16][33], sb[32][17];
  int tx = threadIdx.x & 15, ty = threadIdx.x >> 4; int row = blockIdx.y * 16 + ty, col = blockIdx.x * 16 + tx; float acc = 0;
  for (int k0 = 0; k0 < K; k0 += 32) {
    for (int e = threadIdx.x; e < 512; e += 256) { int r = e >> 5, c = e & 31; int gr = blockIdx.y * 16 + r; sa[r][c] = gr < M ? b2f(A[(size_t)gr * lda + k0 + c]) : 0.f;
      int r2 = e >> 4, c2 = e & 15; sb[r2][c2] = b2f(B[(size_t)(k0 + r2) * ldb + blockIdx.x * 16 + c2]); }
    __syncthreads();
    for (int k = 0; k < 32; ++k) acc += sa[ty][k] * sb[k][tx];
    __syncthreads();
  }
  if (row < M) { float o = rb(acc); if (res) o = b2f(res[(size_t)row * ldc + col]) + o; C[(size_t)row * ldc + col] = f2b(o); }
}
__global__ void attn_k(const u16* q, const u16* km, const u16* vm, const u16* kx, const u16* vx, u16* o, int nq, int ismem, int segstride_q, int segstride_m) {
  int h = blockIdx.x, s = blockIdx.y; int t = threadIdx.x;
  __shared__ float p[40];
  for (int i = 0; i < nq; ++i) {
    const u16* qi = q + ((size_t)s * segstride_q + i) * 4096 + h * 128;
    if (t < 40) {
      const u16* kj = t < 8 ? km + ((size_t)s * segstride_m + t) * 4096 + h * 128 : kx + ((size_t)s * 32 + t - 8) * 4096 + h * 128;
      float a = 0; for (int e = 0; e < 128; ++e) a += b2f(qi[e]) * b2f(kj[e]);
      a = rb(rb(a) * 0.08838834764831845f);
      if (!ismem && t >= 8 && t - 8 > i) a = -1e9f;
      p[t] = a;
    }
    __syncthreads();
    float m = -3e38f; for (int j = 0; j < 40; ++j) m = fmaxf(m, p[j]);
    float l = 0; for (int j = 0; j < 40; ++j) l += __expf(p[j] - m);
    float acc = 0;
    for (int j = 0; j < 40; ++j) { const u16* vj = j < 8 ? vm + ((size_t)s * segstride_m + j) * 4096 + h * 128 : vx + ((size_t)s * 32 + j - 8) * 4096 + h * 128;
      acc += rb(__expf(p[j] - m) / l) * b2f(vj[t]); }
    o[((size_t)s * segstride_q + i) * 4096 + h * 128 + t] = f2b(acc);
    __syncthreads();
  }
}
__global__ void swiglu_k(const u16* g, const u16* u, u16* h, int n) { int i = blockIdx.x * 256 + threadIdx.x; if (i < n) { float a = b2f(g[i]); float s = rb(a / (1.f + __expf(-a))); h[i] = f2b(s * b2f(u[i])); } }

extern "C" void kernel_launch(void* const* d_in, const int* in_sizes, int n_in, void* d_out, int out_size, void* d_ws, size_t ws_size, hipStream_t stream) {
  const u16 *x = (const u16*)d_in[0], *mem0 = (const u16*)d_in[1], *Wq = (const u16*)d_in[2], *Wk = (const u16*)d_in[3], *Wv = (const u16*)d_in[4], *Wo = (const u16*)d_in[5];
  const u16 *wg = (const u16*)d_in[8], *wu = (const u16*)d_in[9], *wd = (const u16*)d_in[10];
  u16* w = (u16*)d_ws; size_t off = 0; auto al = [&](size_t n) { u16* p = w + off; off += n; return p; };
  u16 *xn = al(2048 * 512), *q = al(2048 * 4096), *k = al(2048 * 4096), *v = al(2048 * 4096), *o = al(2048 * 4096);
  u16 *mem = al(65 * 8 * 512), *qm = al(8 * 4096), *km = al(512 * 4096), *vm = al(512 * 4096), *om = al(8 * 4096);
  u16 *x1 = al(2048 * 512), *x2 = al(2048 * 512), *g = al(2048 * 1376), *u = al(2048 * 1376), *hh = al(2048 * 1376);
  const u16* xin = x;
  for (int layer = 0; layer < 2; ++layer) {
    ln_k<<<2048, 64, 0, stream>>>(xin, xn);
    gemm_k<<<dim3(256, 128), 256, 0, stream>>>(xn, 512, Wq, 4096, q, 4096, nullptr, 2048, 4096, 512);
    gemm_k<<<dim3(256, 128), 256, 0, stream>>>(xn, 512, Wk, 4096, k, 4096, nullptr, 2048, 4096, 512);
    gemm_k<<<dim3(256, 128), 256, 0, stream>>>(xn, 512, Wv, 4096, v, 4096, nullptr, 2048, 4096, 512);
    hipMemcpyAsync(mem, mem0, 8 * 512 * 2, hipMemcpyDeviceToDevice, stream);
    for (int s = 0; s < 64; ++s) {
      u16* m = mem + s * 4096;
      gemm_k<<<dim3(256, 1), 256, 0, stream>>>(m, 512, Wq, 4096, qm, 4096, nullptr, 8, 4096, 512);
      gemm_k<<<dim3(256, 1), 256, 0, stream>>>(m, 512, Wk, 4096, km + (size_t)s * 8 * 4096, 4096, nullptr, 8, 4096, 512);
      gemm_k<<<dim3(256, 1), 256, 0, stream>>>(m, 512, Wv, 4096, vm + (size_t)s * 8 * 4096, 4096, nullptr, 8, 4096, 512);
      attn_k<<<dim3(32, 1), 128, 0, stream>>>(qm, km + (size_t)s * 8 * 4096, vm + (size_t)s * 8 * 4096, k + (size_t)s * 32 * 4096, v + (size_t)s * 32 * 4096, om, 8, 1, 0, 0);
      gemm_k<<<dim3(32, 1), 256, 0, stream>>>(om, 4096, Wo, 512, m + 4096, 512, nullptr, 8, 512, 4096);
    }
    attn_k<<<dim3(32, 64), 128, 0, stream>>>(q, km, vm, k, v, o, 32, 0, 32, 8);
    gemm_k<<<dim3(32, 128), 256, 0, stream>>>(o, 4096, Wo, 512, x1, 512, xin, 2048, 512, 4096);
    ln_k<<<2048, 64, 0, stream>>>(x1, xn);
    gemm_k<<<dim3(86, 128), 256, 0, stream>>>(xn, 512, wg, 1376, g, 1376, nullptr, 2048, 1376, 512);
    gemm_k<<<dim3(86, 128), 256, 0, stream>>>(xn, 512, wu, 1376, u, 1376, nullptr, 2048, 1376, 512);
    swiglu_k<<<(2048 * 1376 + 255) / 256, 256, 0, stream>>>(g, u, hh, 2048 * 1376);
    u16* xo = layer == 0 ? x2 : (u16*)d_out;
    gemm_k<<<dim3(32, 128), 256, 0, stream>>>(hh, 1376, wd, 512, xo, 512, x1, 2048, 512, 1376);
    xin = x2;
  }
}
```

```cpp
#define PROBES 3
#define DOP 0
#define DFF 0
#define HW 3
#define ORD 3
#define HD 40
#define OUTM 2
#define DL1 900
#define DS1 100
#define HSM 7
#define OH3 20
#define FPW 512
#define LNF 2
#include <hip/hip_runtime.h>
typedef unsigned short u16;
typedef unsigned u32;
typedef unsigned long long u64;
typedef __bf16 bf2 __attribute__((ext_vector_type(2)));
typedef __bf16 bf8 __attribute__((ext_vector_type(8)));
typedef __bf16 bf16x __attribute__((ext_vector_type(16)));
typedef short s4 __attribute__((ext_vector_type(4)));
typedef float f2 __attribute__((ext_vector_type(2)));
typedef float f4 __attribute__((ext_vector_type(4)));
typedef u32 u2v __attribute__((ext_vector_type(2)));
typedef u32 u4v __attribute__((ext_vector_type(4)));
typedef u32 u8v __attribute__((ext_vector_type(8)));
#define LDS __attribute__((address_space(3)))
#define DEV __device__ __forceinline__
typedef __amdgpu_buffer_rsrc_t rsrc_t;

#define SWP 1
#define MIX 0
#define BID (blockIdx.x ^ SWP)
#define PRD 40
#define SPREAD 0
#define DRF 500
#define DCH 0
#define PRB 5
#define E20 40
#define E21 8
#define XNC 1
#define HD1 HD
#define DQK 0
#define HLP 1
#define PFC 1
#define SLP 1
#define T1 3000
#define T2 8500
constexpr int RING = 31;
constexpr u32 A21_0 = 148, PM_0 = 130;
DEV u32 sp(u32 lin) { return ((lin >> 13) << 14) | (lin & 8191); }
DEV u32 ph(u32 lin, u32 cls) { return sp(lin) | (((cls ^ (lin >> 20)) & 1) << 13); }
DEV u32 aQKV(int s, u32 cls) { return ph(s << 20, cls); }
DEV u32 aO(int s, u32 cls) { return ph((s << 20) + 786432, cls); }
DEV u32 aFP(int s, u32 cls) { return ph((64u << 20) + s * 524288, cls); }
DEV u32 aOPP(int s, u32 cls) { return ph((64u << 20) + s * 524288 + 360448, cls); }
DEV u32 aXN(int s, u32 cls) { return ph((64u << 20) + s * 524288 + 491520, cls); }
DEV u32 aXF(int s, u32 cls) { return ph((96u << 20) + s * 131072, cls); }
DEV u32 aY(int s, u32 cls) { return ph((96u << 20) + s * 131072 + 32768, cls); }
DEV u32 aX2(int s, u32 cls) { return ph((96u << 20) + s * 131072 + 65536, cls); }
constexpr u32 OFF_HOP = 208u << 20;
constexpr u32 OFF_FL = 210u << 20;
constexpr u32 OFF_DBG = 216u << 20;
constexpr u32 WS_USED = 224u << 20;
constexpr u32 OOR = 0x80000000u;
constexpr u32 FL_XN = 0, FL_XF = 32, FL_CTL = 40, FL_CLEAN = 64, FL_OP = 96, FL_FP = 224, FL_O = 320, FL_KV = 384, FL_BYTES = 163840;
DEV u32 fl(u32 cls, u32 n) { return OFF_FL + ((~cls & 1) << 13) + sp(n * 128); }
#define TCH 0
#define TM 63
#define RN 1
#define RX 1
#define RP 1
constexpr u32 F2_XN = 704, F2_XF = F2_XN + 32 * RN, F2_FP = F2_XF + 8 * RX, F2_END = F2_FP + 96 * RP;
constexpr u32 HS = OFF_FL + FL_BYTES, FL_X0 = HS + 4096, FL_C0 = HS + 8192, OFF_X0 = 64u << 20, MB = 5, BUDGET = 200000;
DEV u32 aX0(int s) { return OFF_X0 + (s << 20) + ((s & 2) << 12); }

#define TS(k) do {} while (0)
#define TF(seg, o) do {} while (0)

struct Ctx { rsrc_t ws; u32 budget; u32 tv; u64 top; };

DEV u4v ld128(rsrc_t r, u32 off) { return __builtin_amdgcn_raw_buffer_load_b128(r, off, 0, 16); }
DEV void st128(rsrc_t r, u32 off, u4v v) { __builtin_amdgcn_raw_buffer_store_b128(v, r, off, 0, 16); }
DEV void st32(rsrc_t r, u32 off, u32 v) { __builtin_amdgcn_raw_buffer_store_b32(v, r, off, 0, 16); }
#define VM0() asm volatile("s_waitcnt vmcnt(0)" ::: "memory")
#define VMN(n) asm volatile("s_waitcnt vmcnt(%0)" :: "i"(n) : "memory")
#define LG0() asm volatile("s_waitcnt lgkmcnt(0)" ::: "memory")
#define BAR() asm volatile("s_waitcnt lgkmcnt(0)\n\ts_barrier" ::: "memory")
#define SB() __builtin_amdgcn_sched_barrier(0)
#define MEMB() do { asm volatile("" ::: "memory"); __builtin_amdgcn_sched_barrier(0); } while (0)

DEV bool expired(Ctx& C) { return --C.budget == 0; }
DEV bool wait_ge(Ctx& C, u32 off, u32 need) {
  for (;;) {
    u32 v = __builtin_amdgcn_raw_buffer_load_b32(C.ws, off, 0, 16);
    MEMB();
    if (__builtin_amdgcn_ballot_w64(v < need) == 0) return true;
    if (expired(C)) return false;
    __builtin_amdgcn_s_sleep(SLP);
  }
}
DEV u32 ld32(rsrc_t r, u32 off) { return __builtin_amdgcn_raw_buffer_load_b32(r, off, 0, 16); }
DEV bool allge(u32 v, u32 need) { return __builtin_amdgcn_ballot_w64(v < need) == 0; }
DEV u32 now() { return (u32)__builtin_amdgcn_s_memtime(); }
template <int T> DEV bool wait_p(Ctx& C, u32 off, u32 need, u32& pv, u32& tn) {
  if (allge(pv, need)) return true;
  while ((int)(now() - tn) < 0) __builtin_amdgcn_s_sleep(1);
  bool miss = false;
  for (;;) {
    pv = ld32(C.ws, off);
    MEMB();
    if (allge(pv, need)) break;
    miss = true;
    if (expired(C)) return false;
    __builtin_amdgcn_s_sleep(SLP);
  }
  tn = (miss ? now() : tn) + T;
  return true;
}
#define RNP 0
#define RPD 3
template <int T> DEV bool wait_q(Ctx& C, char* ws, u32 off, u32 need, u32& pv, u32& tn) {
  if (allge(pv, need)) return true;
  while ((int)(now() - tn) < 0) __builtin_amdgcn_s_sleep(1);
  typedef int i4w __attribute__((ext_vector_type(4)));
  const i4w rs = {(int)(u64)ws, (int)((u64)ws >> 32), (int)WS_USED, 0x00020000};
  u32 bud = __builtin_amdgcn_readfirstlane(C.budget);
#define LK(i) "buffer_load_dword v25" #i ", %2, %3, 0 offen sc1\n\t"
#define CK(i) "s_waitcnt vmcnt(3)\n\tv_mov_b32 %0, v25" #i "\n\tv_cmp_gt_u32 vcc, %4, v25" #i "\n\ts_cbranch_vccz 2f\n\t"
  asm volatile("s_nop 4\n\t" LK(0) "s_sleep %5\n\t" LK(1) "s_sleep %5\n\t" LK(2) "s_sleep %5\n"
               "1:\n\t" LK(3) CK(0) LK(0) CK(1) LK(1) CK(2) LK(2) CK(3)
               "s_sub_u32 %1, %1, 1\n\ts_cbranch_scc0 1b\n"
               "2:"
               : "=&v"(pv), "+s"(bud) : "v"(off), "s"(rs), "s"(__builtin_amdgcn_readfirstlane(need)), "i"(RPD) : "memory", "vcc", "scc", "v250", "v251", "v252", "v253");
  C.budget = bud;
  tn = now() + T;
  return bud != ~0u;
}
DEV u32 rt() { return (u32)__builtin_amdgcn_s_memrealtime(); }
DEV void pause(u32 t0, u32 d) { while (rt() - t0 < d) __builtin_amdgcn_s_sleep(8); }
DEV u32 hs_wait(Ctx& C, u32 v0, u32 any, u32 bump = OOR) {
  for (;;) {
    const u2v r = __builtin_amdgcn_raw_buffer_load_b64(C.ws, HS, 0, 16);
    MEMB();
    const u32 p = __builtin_amdgcn_readfirstlane(r.x != v0 ? r.y | any : 0);
    if (p || expired(C)) return p;
    st32(C.ws, bump, v0 + 1 + BUDGET - C.budget);
    __builtin_amdgcn_s_sleep(4);
  }
}
DEV u4v lr128(LDS char* p) { return *(LDS u4v*)p; }
DEV void lw128(LDS char* p, u4v v) { *(LDS u4v*)p = v; }
DEV void lw64(LDS char* p, u2v v) { *(LDS u2v*)p = v; }
DEV u2v ltr(LDS char* p) { return __builtin_bit_cast(u2v, __builtin_amdgcn_ds_read_tr16_b64_v4i16((LDS s4*)p)); }
DEV u4v ltr2(LDS char* p, int d) { u2v a = ltr(p), b = ltr(p + d); return u4v{a.x, a.y, b.x, b.y}; }
DEV void glds16(const void* g, u32 dst) {
  u32 keep;
  asm volatile("s_mov_b32 %0, m0\n\ts_mov_b32 m0, %2\n\ts_nop 0\n\tglobal_load_lds_dwordx4 %1, off\n\ts_mov_b32 m0, %0" : "=&s"(keep) : "v"(g), "s"(dst) : "memory");
}
DEV f4 mf(u4v a, u4v b, f4 c) { return __builtin_amdgcn_mfma_f32_16x16x32_bf16(__builtin_bit_cast(bf8, a), __builtin_bit_cast(bf8, b), c, 0, 0, 0); }
DEV f4 mf16(u2v a, u2v b, f4 c) { return __builtin_amdgcn_mfma_f32_16x16x16bf16_1k(__builtin_bit_cast(s4, a), __builtin_bit_cast(s4, b), c, 0, 0, 0); }
DEV f4 smf(u4v a, u8v b, f4 c, int idx) { return __builtin_amdgcn_smfmac_f32_16x16x64_bf16(__builtin_bit_cast(bf8, a), __builtin_bit_cast(bf16x, b), c, idx, 0, 0); }
DEV u32 pk(float a, float b) { return __builtin_bit_cast(u32, __builtin_convertvector(f2{a, b}, bf2)); }
DEV u2v pk4(f4 a) { return u2v{pk(a.x, a.y), pk(a.z, a.w)}; }
DEV u4v pk8(f4 a, f4 b) { return u4v{pk(a.x, a.y), pk(a.z, a.w), pk(b.x, b.y), pk(b.z, b.w)}; }
DEV float asf(u32 x) { return __builtin_bit_cast(float, x); }
DEV u32 asu(float x) { return __builtin_bit_cast(u32, x); }
DEV float blo(u32 x) { return asf(x << 16); }
DEV float bhi(u32 x) { return asf(x & 0xffff0000u); }
DEV float swadd(float a, float b) { auto r = __builtin_amdgcn_permlane32_swap(asu(a), asu(b), false, false); return asf(r[0]) + asf(r[1]); }
DEV f4 swadd4(f4 a, f4 b) { return f4{swadd(a.x, b.x), swadd(a.y, b.y), swadd(a.z, b.z), swadd(a.w, b.w)}; }
DEV float max_g(float x) {
  auto r = __builtin_amdgcn_permlane16_swap(asu(x), asu(x), false, false); x = fmaxf(asf(r[0]), asf(r[1]));
  auto s = __builtin_amdgcn_permlane32_swap(asu(x), asu(x), false, false); return fmaxf(asf(s[0]), asf(s[1]));
}
DEV float sum_g(float x) {
  auto r = __builtin_amdgcn_permlane16_swap(asu(x), asu(x), false, false); x = asf(r[0]) + asf(r[1]);
  auto s = __builtin_amdgcn_permlane32_swap(asu(x), asu(x), false, false); return asf(s[0]) + asf(s[1]);
}
template <int CTRL> DEV float dpp(float x) { return asf(__builtin_amdgcn_update_dpp(0, (int)asu(x), CTRL, 0xf, 0xf, true)); }
DEV float sum_wave(float x) {
  x += dpp<0xB1>(x); x += dpp<0x4E>(x); x += dpp<0x141>(x); x += dpp<0x140>(x);
  return sum_g(x);
}
DEV f4 vzero() { f4 z = {0.f, 0.f, 0.f, 0.f}; asm volatile("" : "+v"(z)); return z; }
#define PINA(a) asm volatile("" : "+a"(a))
#define PIN2(a, b) asm volatile("" : "+v"(a), "+v"(b))
DEV float max3f(float a, float b, float c) { return fmaxf(fmaxf(a, b), c); }
DEV u32 max3u(u32 a, u32 b, u32 c) { return max(max(a, b), c); }
constexpr float SC = 0.08838834764831845f * 1.4426950408889634f;

template <int ROWB> DEV void stage8(const char* g, u32 rowstride, u32 dst, int lane) {
  constexpr int PPR = ROWB / 16, RPC = 64 / PPR;
  const char* p = g + (size_t)(lane / PPR) * rowstride + (lane % PPR) * 16;
#pragma unroll
  for (int c = 0; c < 8; ++c) glds16(p + (size_t)c * RPC * rowstride, dst + c * 1024);
}

DEV void stage_x(Ctx& C, LDS char* X, u32 src, int w, int lane) {
  u4v xl[8];
#pragma unroll
  for (int c = 0; c < 8; ++c) xl[c] = ld128(C.ws, src + w * 16384 + c * 1024 + lane * 16);
  MEMB();
#pragma unroll
  for (int c = 0; c < 8; ++c) lw128(X + (w * 8 + c) * 1024 + ((lane ^ (8 * (w & 1) + c)) << 4), xl[c]);
}
#define XFRAG(tt, ks) lr128(X + xa[(ks) & 3] + ((ks) >> 2) * 256 + (tt) * 16384)
#define SGB(m, n) __builtin_amdgcn_sched_group_barrier(m, n, 0)
DEV void sethalf(u4v& d, int u, u2v v) { if (u == 0) { d.x = v.x; d.y = v.y; } else { d.z = v.x; d.w = v.y; } }
DEV u4v pkr(const f4* a, int r) { return u4v{pk(a[0][r], a[1][r]), pk(a[2][r], a[3][r]), pk(a[4][r], a[5][r]), pk(a[6][r], a[7][r])}; }

typedef int i4v __attribute__((ext_vector_type(4)));
DEV u32 RFL(u32 x) { u32 r; asm volatile("s_nop 0\n\tv_readfirstlane_b32 %0, %1" : "=s"(r) : "v"(x)); return r; }
DEV u64 PTR(const u16* p, u32 e) { return (u64)p + e; }
DEV void role_qkv(Ctx& C, char* ws, u32 l, int h, int w, int lane, const u16* Wq, const u16* Wk, const u16* Wv) {
  const int g = lane >> 4, li = lane & 15;
  const u64 wsa = (u64)ws;
  const i4v rs = {(int)wsa, (int)(wsa >> 32), (int)WS_USED, 0x00020000};
  const u32 wo = RFL(h * 256 + 64 * w);
  u64 pk_ = PTR(Wk, wo), pv_ = PTR(Wv, wo), pq_ = PTR(Wq, wo);
  const bool sink = l == 0 && lane >= 32;
  u4v xa;
#pragma unroll
  for (int k = 0; k < 4; ++k) xa[k] = li * 1024 + (((4 * k + g) ^ li) << 4);
  const u32 reg = 65536 + w * 24576;
  u32 sq = RFL(aQKV(0, l) + h * 49152), st = 0, bud = C.budget, dbg = RFL(OFF_DBG + (BID * 4 + w) * 64 * 64);
  asm volatile(
  "s_nop 4\n"
  "buffer_load_dword v114, off, s[36:39], s64 sc1\n"
  "s_cmp_eq_u32 s65, 0\n"
  "s_cbranch_scc1 .Lq_d1\n"
  "s_memrealtime s[70:71]\n"
  "s_waitcnt lgkmcnt(0)\n"
  "s_add_u32 s65, s65, s70\n"
  ".Lq_d0:\n"
  "s_sleep 8\n"
  "s_memrealtime s[70:71]\n"
  "s_waitcnt lgkmcnt(0)\n"
  "s_sub_u32 s70, s70, s65\n"
  "s_cmp_lt_i32 s70, 0\n"
  "s_cbranch_scc1 .Lq_d0\n"
  ".Lq_d1:\n"
  "s_add_u32 m0, s50, 0\n"
  "s_nop 0\n"
  "global_load_lds_dwordx4 v118, s[40:41]\n"
  "s_add_u32 s40, s40, 0x20000\n"
  "s_addc_u32 s41, s41, 0\n"
  "s_add_u32 m0, m0, 1024\n"
  "s_nop 0\n"
  "global_load_lds_dwordx4 v118, s[40:41]\n"
  "s_add_u32 s40, s40, 0x20000\n"
  "s_addc_u32 s41, s41, 0\n"
  "s_add_u32 m0, m0, 1024\n"
  "s_nop 0\n"
  "global_load_lds_dwordx4 v118, s[40:41]\n"
  "s_add_u32 s40, s40, 0x20000\n"
  "s_addc_u32 s41, s41, 0\n"
  "s_add_u32 m0, m0, 1024\n"
  "s_nop 0\n"
  "global_load_lds_dwordx4 v118, s[40:41]\n"
  "s_add_u32 s40, s40, 0x20000\n"
  "s_addc_u32 s41, s41, 0\n"
  "s_add_u32 m0, m0, 1024\n"
  "s_nop 0\n"
  "global_load_lds_dwordx4 v118, s[40:41]\n"
  "s_add_u32 s40, s40, 0x20000\n"
  "s_addc_u32 s41, s41, 0\n"
  "s_add_u32 m0, m0, 1024\n"
  "s_nop 0\n"
  "global_load_lds_dwordx4 v118, s[40:41]\n"
  "s_add_u32 s40, s40, 0x20000\n"
  "s_addc_u32 s41, s41, 0\n"
  "s_add_u32 m0, m0, 1024\n"
  "s_nop 0\n"
  "global_load_lds_dwordx4 v118, s[40:41]\n"
  "s_add_u32 s40, s40, 0x20000\n"
  "s_addc_u32 s41, s41, 0\n"
  "s_add_u32 m0, m0, 1024\n"
  "s_nop 0\n"
  "global_load_lds_dwordx4 v118, s[40:41]\n"
  "s_add_u32 s40, s40, 0x20000\n"
  "s_addc_u32 s41, s41, 0\n"
  "s_add_u32 m0, s50, 8192\n"
  "s_nop 0\n"
  "global_load_lds_dwordx4 v118, s[40:41]\n"
  "s_add_u32 s40, s40, 0x20000\n"
  "s_addc_u32 s41, s41, 0\n"
  "s_add_u32 m0, m0, 1024\n"
  "s_nop 0\n"
  "global_load_lds_dwordx4 v118, s[40:41]\n"
  "s_add_u32 s40, s40, 0x20000\n"
  "s_addc_u32 s41, s41, 0\n"
  "s_add_u32 m0, m0, 1024\n"
  "s_nop 0\n"
  "global_load_lds_dwordx4 v118, s[40:41]\n"
  "s_add_u32 s40, s40, 0x20000\n"
  "s_addc_u32 s41, s41, 0\n"
  "s_add_u32 m0, m0, 1024\n"
  "s_nop 0\n"
  "global_load_lds_dwordx4 v118, s[40:41]\n"
  "s_add_u32 s40, s40, 0x20000\n"
  "s_addc_u32 s41, s41, 0\n"
  "s_add_u32 m0, m0, 1024\n"
  "s_nop 0\n"
  "global_load_lds_dwordx4 v118, s[40:41]\n"
  "s_add_u32 s40, s40, 0x20000\n"
  "s_addc_u32 s41, s41, 0\n"
  "s_add_u32 m0, m0, 1024\n"
  "s_nop 0\n"
  "global_load_lds_dwordx4 v118, s[40:41]\n"
  "s_add_u32 s40, s40, 0x20000\n"
  "s_addc_u32 s41, s41, 0\n"
  "s_add_u32 m0, m0, 1024\n"
  "s_nop 0\n"
  "global_load_lds_dwordx4 v118, s[40:41]\n"
  "s_add_u32 s40, s40, 0x20000\n"
  "s_addc_u32 s41, s41, 0\n"
  "s_add_u32 m0, m0, 1024\n"
  "s_nop 0\n"
  "global_load_lds_dwordx4 v118, s[40:41]\n"
  "s_add_u32 s40, s40, 0x20000\n"
  "s_addc_u32 s41, s41, 0\n"
  "s_add_u32 m0, s50, 16384\n"
  "s_nop 0\n"
  "global_load_lds_dwordx4 v118, s[40:41]\n"
  "s_add_u32 s40, s40, 0x20000\n"
  "s_addc_u32 s41, s41, 0\n"
  "s_add_u32 m0, m0, 1024\n"
  "s_nop 0\n"
  "global_load_lds_dwordx4 v118, s[40:41]\n"
  "s_add_u32 s40, s40, 0x20000\n"
  "s_addc_u32 s41, s41, 0\n"
  "s_add_u32 m0, m0, 1024\n"
  "s_nop 0\n"
  "global_load_lds_dwordx4 v118, s[40:41]\n"
  "s_add_u32 s40, s40, 0x20000\n"
  "s_addc_u32 s41, s41, 0\n"
  "s_add_u32 m0, m0, 1024\n"
  "s_nop 0\n"
  "global_load_lds_dwordx4 v118, s[40:41]\n"
  "s_add_u32 s40, s40, 0x20000\n"
  "s_addc_u32 s41, s41, 0\n"
  "s_add_u32 m0, m0, 1024\n"
  "s_nop 0\n"
  "global_load_lds_dwordx4 v118, s[40:41]\n"
  "s_add_u32 s40, s40, 0x20000\n"
  "s_addc_u32 s41, s41, 0\n"
  "s_add_u32 m0, m0, 1024\n"
  "s_nop 0\n"
  "global_load_lds_dwordx4 v118, s[40:41]\n"
  "s_add_u32 s40, s40, 0x20000\n"
  "s_addc_u32 s41, s41, 0\n"
  "s_add_u32 m0, m0, 1024\n"
  "s_nop 0\n"
  "global_load_lds_dwordx4 v118, s[40:41]\n"
  "s_add_u32 s40, s40, 0x20000\n"
  "s_addc_u32 s41, s41, 0\n"
  "s_add_u32 m0, m0, 1024\n"
  "s_nop 0\n"
  "global_load_lds_dwordx4 v118, s[40:41]\n"
  "s_add_u32 s40, s40, 0x20000\n"
  "s_addc_u32 s41, s41, 0\n"
  "v_xor_b32 v80, 0, v111\n"
  "v_xor_b32 v81, 16, v111\n"
  "v_xor_b32 v82, 32, v111\n"
  "v_xor_b32 v83, 48, v111\n"
  "v_xor_b32 v84, 64, v111\n"
  "v_xor_b32 v85, 80, v111\n"
  "v_xor_b32 v86, 96, v111\n"
  "v_xor_b32 v87, 112, v111\n"
  "s_mov_b32 s63, 1\n"
  "s_mov_b32 s58, s52\n"
  "s_add_u32 s59, s52, 4096\n"
  "s_cmp_eq_u32 s66, 0\n"
  "s_cselect_b32 s68, 1, 0\n"
  "s_waitcnt vmcnt(16)\n"
  "v_readfirstlane_b32 s46, v114\n"
  "ds_read_b64_tr_b16 a[0:1], v119 offset:0\n"
  "ds_read_b64_tr_b16 a[64:65], v119 offset:8\n"
  "ds_read_b64_tr_b16 a[4:5], v119 offset:1024\n"
  "ds_read_b64_tr_b16 a[68:69], v119 offset:1032\n"
  "ds_read_b64_tr_b16 a[16:17], v119 offset:2048\n"
  "ds_read_b64_tr_b16 a[80:81], v119 offset:2056\n"
  "ds_read_b64_tr_b16 a[20:21], v119 offset:3072\n"
  "ds_read_b64_tr_b16 a[84:85], v119 offset:3080\n"
  "ds_read_b64_tr_b16 a[32:33], v119 offset:4096\n"
  "ds_read_b64_tr_b16 a[96:97], v119 offset:4104\n"
  "ds_read_b64_tr_b16 a[36:37], v119 offset:5120\n"
  "ds_read_b64_tr_b16 a[100:101], v119 offset:5128\n"
  "ds_read_b64_tr_b16 a[48:49], v119 offset:6144\n"
  "ds_read_b64_tr_b16 a[112:113], v119 offset:6152\n"
  "ds_read_b64_tr_b16 a[52:53], v119 offset:7168\n"
  "ds_read_b64_tr_b16 a[116:117], v119 offset:7176\n"
  "s_waitcnt lgkmcnt(0)\n"
  "s_add_u32 m0, s50, 0\n"
  "s_nop 0\n"
  "global_load_lds_dwordx4 v118, s[40:41]\n"
  "s_add_u32 s40, s40, 0x20000\n"
  "s_addc_u32 s41, s41, 0\n"
  "s_add_u32 m0, m0, 1024\n"
  "s_nop 0\n"
  "global_load_lds_dwordx4 v118, s[40:41]\n"
  "s_add_u32 s40, s40, 0x20000\n"
  "s_addc_u32 s41, s41, 0\n"
  "s_add_u32 m0, m0, 1024\n"
  "s_nop 0\n"
  "global_load_lds_dwordx4 v118, s[40:41]\n"
  "s_add_u32 s40, s40, 0x20000\n"
  "s_addc_u32 s41, s41, 0\n"
  "s_add_u32 m0, m0, 1024\n"
  "s_nop 0\n"
  "global_load_lds_dwordx4 v118, s[40:41]\n"
  "s_add_u32 s40, s40, 0x20000\n"
  "s_addc_u32 s41, s41, 0\n"
  "s_add_u32 m0, m0, 1024\n"
  "s_nop 0\n"
  "global_load_lds_dwordx4 v118, s[40:41]\n"
  "s_add_u32 s40, s40, 0x20000\n"
  "s_addc_u32 s41, s41, 0\n"
  "s_add_u32 m0, m0, 1024\n"
  "s_nop 0\n"
  "global_load_lds_dwordx4 v118, s[40:41]\n"
  "s_add_u32 s40, s40, 0x20000\n"
  "s_addc_u32 s41, s41, 0\n"
  "s_add_u32 m0, m0, 1024\n"
  "s_nop 0\n"
  "global_load_lds_dwordx4 v118, s[40:41]\n"
  "s_add_u32 s40, s40, 0x20000\n"
  "s_addc_u32 s41, s41, 0\n"
  "s_add_u32 m0, m0, 1024\n"
  "s_nop 0\n"
  "global_load_lds_dwordx4 v118, s[40:41]\n"
  "s_add_u32 s40, s40, 0x20000\n"
  "s_addc_u32 s41, s41, 0\n"
  "s_waitcnt vmcnt(16)\n"
  "ds_read_b64_tr_b16 a[8:9], v119 offset:8192\n"
  "ds_read_b64_tr_b16 a[72:73], v119 offset:8200\n"
  "ds_read_b64_tr_b16 a[12:13], v119 offset:9216\n"
  "ds_read_b64_tr_b16 a[76:77], v119 offset:9224\n"
  "ds_read_b64_tr_b16 a[24:25], v119 offset:10240\n"
  "ds_read_b64_tr_b16 a[88:89], v119 offset:10248\n"
  "ds_read_b64_tr_b16 a[28:29], v119 offset:11264\n"
  "ds_read_b64_tr_b16 a[92:93], v119 offset:11272\n"
  "ds_read_b64_tr_b16 a[40:41], v119 offset:12288\n"
  "ds_read_b64_tr_b16 a[104:105], v119 offset:12296\n"
  "ds_read_b64_tr_b16 a[44:45], v119 offset:13312\n"
  "ds_read_b64_tr_b16 a[108:109], v119 offset:13320\n"
  "ds_read_b64_tr_b16 a[56:57], v119 offset:14336\n"
  "ds_read_b64_tr_b16 a[120:121], v119 offset:14344\n"
  "ds_read_b64_tr_b16 a[60:61], v119 offset:15360\n"
  "ds_read_b64_tr_b16 a[124:125], v119 offset:15368\n"
  "s_waitcnt lgkmcnt(0)\n"
  "buffer_load_dwordx2 v[116:117], off, s[36:39], s64 sc1\n"
  "s_add_u32 m0, s50, 8192\n"
  "s_nop 0\n"
  "global_load_lds_dwordx4 v118, s[42:43]\n"
  "s_add_u32 s42, s42, 0x20000\n"
  "s_addc_u32 s43, s43, 0\n"
  "s_add_u32 m0, m0, 1024\n"
  "s_nop 0\n"
  "global_load_lds_dwordx4 v118, s[42:43]\n"
  "s_add_u32 s42, s42, 0x20000\n"
  "s_addc_u32 s43, s43, 0\n"
  "s_add_u32 m0, m0, 1024\n"
  "s_nop 0\n"
  "global_load_lds_dwordx4 v118, s[42:43]\n"
  "s_add_u32 s42, s42, 0x20000\n"
  "s_addc_u32 s43, s43, 0\n"
  "s_add_u32 m0, m0, 1024\n"
  "s_nop 0\n"
  "global_load_lds_dwordx4 v118, s[42:43]\n"
  "s_add_u32 s42, s42, 0x20000\n"
  "s_addc_u32 s43, s43, 0\n"
  "s_add_u32 m0, m0, 1024\n"
  "s_nop 0\n"
  "global_load_lds_dwordx4 v118, s[42:43]\n"
  "s_add_u32 s42, s42, 0x20000\n"
  "s_addc_u32 s43, s43, 0\n"
  "s_add_u32 m0, m0, 1024\n"
  "s_nop 0\n"
  "global_load_lds_dwordx4 v118, s[42:43]\n"
  "s_add_u32 s42, s42, 0x20000\n"
  "s_addc_u32 s43, s43, 0\n"
  "s_add_u32 m0, m0, 1024\n"
  "s_nop 0\n"
  "global_load_lds_dwordx4 v118, s[42:43]\n"
  "s_add_u32 s42, s42, 0x20000\n"
  "s_addc_u32 s43, s43, 0\n"
  "s_add_u32 m0, m0, 1024\n"
  "s_nop 0\n"
  "global_load_lds_dwordx4 v118, s[42:43]\n"
  "s_add_u32 s42, s42, 0x20000\n"
  "s_addc_u32 s43, s43, 0\n"
  "s_waitcnt vmcnt(17)\n"
  "ds_read_b64_tr_b16 a[2:3], v119 offset:16384\n"
  "ds_read_b64_tr_b16 a[66:67], v119 offset:16392\n"
  "ds_read_b64_tr_b16 a[6:7], v119 offset:17408\n"
  "ds_read_b64_tr_b16 a[70:71], v119 offset:17416\n"
  "ds_read_b64_tr_b16 a[18:19], v119 offset:18432\n"
  "ds_read_b64_tr_b16 a[82:83], v119 offset:18440\n"
  "ds_read_b64_tr_b16 a[22:23], v119 offset:19456\n"
  "ds_read_b64_tr_b16 a[86:87], v119 offset:19464\n"
  "ds_read_b64_tr_b16 a[34:35], v119 offset:20480\n"
  "ds_read_b64_tr_b16 a[98:99], v119 offset:20488\n"
  "ds_read_b64_tr_b16 a[38:39], v119 offset:21504\n"
  "ds_read_b64_tr_b16 a[102:103], v119 offset:21512\n"
  "ds_read_b64_tr_b16 a[50:51], v119 offset:22528\n"
  "ds_read_b64_tr_b16 a[114:115], v119 offset:22536\n"
  "ds_read_b64_tr_b16 a[54:55], v119 offset:23552\n"
  "ds_read_b64_tr_b16 a[118:119], v119 offset:23560\n"
  "s_waitcnt lgkmcnt(0)\n"
  "s_add_u32 m0, s50, 16384\n"
  "s_nop 0\n"
  "global_load_lds_dwordx4 v118, s[42:43]\n"
  "s_add_u32 s42, s42, 0x20000\n"
  "s_addc_u32 s43, s43, 0\n"
  "s_add_u32 m0, m0, 1024\n"
  "s_nop 0\n"
  "global_load_lds_dwordx4 v118, s[42:43]\n"
  "s_add_u32 s42, s42, 0x20000\n"
  "s_addc_u32 s43, s43, 0\n"
  "s_add_u32 m0, m0, 1024\n"
  "s_nop 0\n"
  "global_load_lds_dwordx4 v118, s[42:43]\n"
  "s_add_u32 s42, s42, 0x20000\n"
  "s_addc_u32 s43, s43, 0\n"
  "s_add_u32 m0, m0, 1024\n"
  "s_nop 0\n"
  "global_load_lds_dwordx4 v118, s[42:43]\n"
  "s_add_u32 s42, s42, 0x20000\n"
  "s_addc_u32 s43, s43, 0\n"
  "s_add_u32 m0, m0, 1024\n"
  "s_nop 0\n"
  "global_load_lds_dwordx4 v118, s[42:43]\n"
  "s_add_u32 s42, s42, 0x20000\n"
  "s_addc_u32 s43, s43, 0\n"
  "s_add_u32 m0, m0, 1024\n"
  "s_nop 0\n"
  "global_load_lds_dwordx4 v118, s[42:43]\n"
  "s_add_u32 s42, s42, 0x20000\n"
  "s_addc_u32 s43, s43, 0\n"
  "s_add_u32 m0, m0, 1024\n"
  "s_nop 0\n"
  "global_load_lds_dwordx4 v118, s[42:43]\n"
  "s_add_u32 s42, s42, 0x20000\n"
  "s_addc_u32 s43, s43, 0\n"
  "s_add_u32 m0, m0, 1024\n"
  "s_nop 0\n"
  "global_load_lds_dwordx4 v118, s[42:43]\n"
  "s_add_u32 s42, s42, 0x20000\n"
  "s_addc_u32 s43, s43, 0\n"
  "s_waitcnt vmcnt(16)\n"
  "ds_read_b64_tr_b16 a[10:11], v119 offset:0\n"
  "ds_read_b64_tr_b16 a[74:75], v119 offset:8\n"
  "ds_read_b64_tr_b16 a[14:15], v119 offset:1024\n"
  "ds_read_b64_tr_b16 a[78:79], v119 offset:1032\n"
  "ds_read_b64_tr_b16 a[26:27], v119 offset:2048\n"
  "ds_read_b64_tr_b16 a[90:91], v119 offset:2056\n"
  "ds_read_b64_tr_b16 a[30:31], v119 offset:3072\n"
  "ds_read_b64_tr_b16 a[94:95], v119 offset:3080\n"
  "ds_read_b64_tr_b16 a[42:43], v119 offset:4096\n"
  "ds_read_b64_tr_b16 a[106:107], v119 offset:4104\n"
  "ds_read_b64_tr_b16 a[46:47], v119 offset:5120\n"
  "ds_read_b64_tr_b16 a[110:111], v119 offset:5128\n"
  "ds_read_b64_tr_b16 a[58:59], v119 offset:6144\n"
  "ds_read_b64_tr_b16 a[122:123], v119 offset:6152\n"
  "ds_read_b64_tr_b16 a[62:63], v119 offset:7168\n"
  "ds_read_b64_tr_b16 a[126:127], v119 offset:7176\n"
  "s_waitcnt lgkmcnt(0)\n"
  "s_cmp_eq_u32 s68, 0\n"
  "s_cbranch_scc1 .Lqa_h1\n"
  ".Lqa_h0:\n"
  "v_readfirstlane_b32 s56, v116\n"
  "v_readfirstlane_b32 s57, v117\n"
  "s_cmp_lg_u32 s56, s46\n"
  "s_cbranch_scc1 .Lqa_h1\n"
  "s_sub_u32 s55, s55, 1\n"
  "s_cbranch_scc1 .Lq_end\n"
  "s_sleep 4\n"
  "buffer_load_dwordx2 v[116:117], off, s[36:39], s64 sc1\n"
  "s_waitcnt vmcnt(0)\n"
  "s_branch .Lqa_h0\n"
  ".Lqa_h1:\n"
  "buffer_load_dword v115, v112, s[36:39], 0 offen sc1\n"
  "s_add_u32 m0, s50, 0\n"
  "s_nop 0\n"
  "global_load_lds_dwordx4 v118, s[42:43]\n"
  "s_add_u32 s42, s42, 0x20000\n"
  "s_addc_u32 s43, s43, 0\n"
  "s_add_u32 m0, m0, 1024\n"
  "s_nop 0\n"
  "global_load_lds_dwordx4 v118, s[42:43]\n"
  "s_add_u32 s42, s42, 0x20000\n"
  "s_addc_u32 s43, s43, 0\n"
  "s_add_u32 m0, m0, 1024\n"
  "s_nop 0\n"
  "global_load_lds_dwordx4 v118, s[42:43]\n"
  "s_add_u32 s42, s42, 0x20000\n"
  "s_addc_u32 s43, s43, 0\n"
  "s_add_u32 m0, m0, 1024\n"
  "s_nop 0\n"
  "global_load_lds_dwordx4 v118, s[42:43]\n"
  "s_add_u32 s42, s42, 0x20000\n"
  "s_addc_u32 s43, s43, 0\n"
  "s_add_u32 m0, m0, 1024\n"
  "s_nop 0\n"
  "global_load_lds_dwordx4 v118, s[42:43]\n"
  "s_add_u32 s42, s42, 0x20000\n"
  "s_addc_u32 s43, s43, 0\n"
  "s_add_u32 m0, m0, 1024\n"
  "s_nop 0\n"
  "global_load_lds_dwordx4 v118, s[42:43]\n"
  "s_add_u32 s42, s42, 0x20000\n"
  "s_addc_u32 s43, s43, 0\n"
  "s_add_u32 m0, m0, 1024\n"
  "s_nop 0\n"
  "global_load_lds_dwordx4 v118, s[42:43]\n"
  "s_add_u32 s42, s42, 0x20000\n"
  "s_addc_u32 s43, s43, 0\n"
  "s_add_u32 m0, m0, 1024\n"
  "s_nop 0\n"
  "global_load_lds_dwordx4 v118, s[42:43]\n"
  "s_add_u32 s42, s42, 0x20000\n"
  "s_addc_u32 s43, s43, 0\n"
  "s_waitcnt vmcnt(17)\n"
  "ds_read_b64_tr_b16 a[128:129], v119 offset:8192\n"
  "ds_read_b64_tr_b16 a[192:193], v119 offset:8200\n"
  "ds_read_b64_tr_b16 a[132:133], v119 offset:9216\n"
  "ds_read_b64_tr_b16 a[196:197], v119 offset:9224\n"
  "ds_read_b64_tr_b16 a[144:145], v119 offset:10240\n"
  "ds_read_b64_tr_b16 a[208:209], v119 offset:10248\n"
  "ds_read_b64_tr_b16 a[148:149], v119 offset:11264\n"
  "ds_read_b64_tr_b16 a[212:213], v119 offset:11272\n"
  "ds_read_b64_tr_b16 a[160:161], v119 offset:12288\n"
  "ds_read_b64_tr_b16 a[224:225], v119 offset:12296\n"
  "ds_read_b64_tr_b16 a[164:165], v119 offset:13312\n"
  "ds_read_b64_tr_b16 a[228:229], v119 offset:13320\n"
  "ds_read_b64_tr_b16 a[176:177], v119 offset:14336\n"
  "ds_read_b64_tr_b16 a[240:241], v119 offset:14344\n"
  "ds_read_b64_tr_b16 a[180:181], v119 offset:15360\n"
  "ds_read_b64_tr_b16 a[244:245], v119 offset:15368\n"
  "s_waitcnt lgkmcnt(0)\n"
  "s_add_u32 m0, s50, 8192\n"
  "s_nop 0\n"
  "global_load_lds_dwordx4 v118, s[42:43]\n"
  "s_add_u32 s42, s42, 0x20000\n"
  "s_addc_u32 s43, s43, 0\n"
  "s_add_u32 m0, m0, 1024\n"
  "s_nop 0\n"
  "global_load_lds_dwordx4 v118, s[42:43]\n"
  "s_add_u32 s42, s42, 0x20000\n"
  "s_addc_u32 s43, s43, 0\n"
  "s_add_u32 m0, m0, 1024\n"
  "s_nop 0\n"
  "global_load_lds_dwordx4 v118, s[42:43]\n"
  "s_add_u32 s42, s42, 0x20000\n"
  "s_addc_u32 s43, s43, 0\n"
  "s_add_u32 m0, m0, 1024\n"
  "s_nop 0\n"
  "global_load_lds_dwordx4 v118, s[42:43]\n"
  "s_add_u32 s42, s42, 0x20000\n"
  "s_addc_u32 s43, s43, 0\n"
  "s_add_u32 m0, m0, 1024\n"
  "s_nop 0\n"
  "global_load_lds_dwordx4 v118, s[42:43]\n"
  "s_add_u32 s42, s42, 0x20000\n"
  "s_addc_u32 s43, s43, 0\n"
  "s_add_u32 m0, m0, 1024\n"
  "s_nop 0\n"
  "global_load_lds_dwordx4 v118, s[42:43]\n"
  "s_add_u32 s42, s42, 0x20000\n"
  "s_addc_u32 s43, s43, 0\n"
  "s_add_u32 m0, m0, 1024\n"
  "s_nop 0\n"
  "global_load_lds_dwordx4 v118, s[42:43]\n"
  "s_add_u32 s42, s42, 0x20000\n"
  "s_addc_u32 s43, s43, 0\n"
  "s_add_u32 m0, m0, 1024\n"
  "s_nop 0\n"
  "global_load_lds_dwordx4 v118, s[42:43]\n"
  "s_add_u32 s42, s42, 0x20000\n"
  "s_addc_u32 s43, s43, 0\n"
  "s_waitcnt vmcnt(16)\n"
  "ds_read_b64_tr_b16 a[136:137], v119 offset:16384\n"
  "ds_read_b64_tr_b16 a[200:201], v119 offset:16392\n"
  "ds_read_b64_tr_b16 a[140:141], v119 offset:17408\n"
  "ds_read_b64_tr_b16 a[204:205], v119 offset:17416\n"
  "ds_read_b64_tr_b16 a[152:153], v119 offset:18432\n"
  "ds_read_b64_tr_b16 a[216:217], v119 offset:18440\n"
  "ds_read_b64_tr_b16 a[156:157], v119 offset:19456\n"
  "ds_read_b64_tr_b16 a[220:221], v119 offset:19464\n"
  "ds_read_b64_tr_b16 a[168:169], v119 offset:20480\n"
  "ds_read_b64_tr_b16 a[232:233], v119 offset:20488\n"
  "ds_read_b64_tr_b16 a[172:173], v119 offset:21504\n"
  "ds_read_b64_tr_b16 a[236:237], v119 offset:21512\n"
  "ds_read_b64_tr_b16 a[184:185], v119 offset:22528\n"
  "ds_read_b64_tr_b16 a[248:249], v119 offset:22536\n"
  "ds_read_b64_tr_b16 a[188:189], v119 offset:23552\n"
  "ds_read_b64_tr_b16 a[252:253], v119 offset:23560\n"
  "s_waitcnt lgkmcnt(0)\n"
  "s_cmp_eq_u32 s68, 0\n"
  "s_cbranch_scc1 .Lq_p1\n"
  "s_branch .Lq_p0a\n"
  ".Lq_p0:\n"
  "buffer_load_dword v115, v112, s[36:39], 0 offen sc1\n"
  "s_waitcnt vmcnt(0)\n"
  ".Lq_p0a:\n"
  "v_add_u32 v114, v115, v122\n"
  "v_cmp_gt_u32 vcc, s63, v114\n"
  "s_cbranch_vccz .Lq_p0b\n"
  "s_sub_u32 s55, s55, 1\n"
  "s_cbranch_scc1 .Lq_end\n"
  "s_sleep 1\n"
  "s_branch .Lq_p0\n"
  ".Lq_p0b:\n"
  "s_mov_b32 m0, s61\n"
  "s_nop 0\n"
  "buffer_load_dwordx4 v80, s[36:39], s58 offen offset:0 sc1 lds\n"
  "buffer_load_dwordx4 v81, s[36:39], s58 offen offset:1024 sc1 lds\n"
  "buffer_load_dwordx4 v82, s[36:39], s58 offen offset:2048 sc1 lds\n"
  "buffer_load_dwordx4 v83, s[36:39], s58 offen offset:3072 sc1 lds\n"
  "s_add_u32 m0, s61, 4096\n"
  "s_nop 0\n"
  "buffer_load_dwordx4 v84, s[36:39], s59 offen offset:0 sc1 lds\n"
  "buffer_load_dwordx4 v85, s[36:39], s59 offen offset:1024 sc1 lds\n"
  "buffer_load_dwordx4 v86, s[36:39], s59 offen offset:2048 sc1 lds\n"
  "buffer_load_dwordx4 v87, s[36:39], s59 offen offset:3072 sc1 lds\n"
  ".Lq_p1:\n"
  "buffer_load_dwordx2 v[116:117], off, s[36:39], s64 sc1\n"
  "s_add_u32 m0, s50, 16384\n"
  "s_nop 0\n"
  "global_load_lds_dwordx4 v118, s[44:45]\n"
  "s_add_u32 s44, s44, 0x20000\n"
  "s_addc_u32 s45, s45, 0\n"
  "s_add_u32 m0, m0, 1024\n"
  "s_nop 0\n"
  "global_load_lds_dwordx4 v118, s[44:45]\n"
  "s_add_u32 s44, s44, 0x20000\n"
  "s_addc_u32 s45, s45, 0\n"
  "s_add_u32 m0, m0, 1024\n"
  "s_nop 0\n"
  "global_load_lds_dwordx4 v118, s[44:45]\n"
  "s_add_u32 s44, s44, 0x20000\n"
  "s_addc_u32 s45, s45, 0\n"
  "s_add_u32 m0, m0, 1024\n"
  "s_nop 0\n"
  "global_load_lds_dwordx4 v118, s[44:45]\n"
  "s_add_u32 s44, s44, 0x20000\n"
  "s_addc_u32 s45, s45, 0\n"
  "s_add_u32 m0, m0, 1024\n"
  "s_nop 0\n"
  "global_load_lds_dwordx4 v118, s[44:45]\n"
  "s_add_u32 s44, s44, 0x20000\n"
  "s_addc_u32 s45, s45, 0\n"
  "s_add_u32 m0, m0, 1024\n"
  "s_nop 0\n"
  "global_load_lds_dwordx4 v118, s[44:45]\n"
  "s_add_u32 s44, s44, 0x20000\n"
  "s_addc_u32 s45, s45, 0\n"
  "s_add_u32 m0, m0, 1024\n"
  "s_nop 0\n"
  "global_load_lds_dwordx4 v118, s[44:45]\n"
  "s_add_u32 s44, s44, 0x20000\n"
  "s_addc_u32 s45, s45, 0\n"
  "s_add_u32 m0, m0, 1024\n"
  "s_nop 0\n"
  "global_load_lds_dwordx4 v118, s[44:45]\n"
  "s_add_u32 s44, s44, 0x20000\n"
  "s_addc_u32 s45, s45, 0\n"
  "s_waitcnt vmcnt(17)\n"
  "ds_read_b64_tr_b16 a[130:131], v119 offset:0\n"
  "ds_read_b64_tr_b16 a[194:195], v119 offset:8\n"
  "ds_read_b64_tr_b16 a[134:135], v119 offset:1024\n"
  "ds_read_b64_tr_b16 a[198:199], v119 offset:1032\n"
  "ds_read_b64_tr_b16 a[146:147], v119 offset:2048\n"
  "ds_read_b64_tr_b16 a[210:211], v119 offset:2056\n"
  "ds_read_b64_tr_b16 a[150:151], v119 offset:3072\n"
  "ds_read_b64_tr_b16 a[214:215], v119 offset:3080\n"
  "ds_read_b64_tr_b16 a[162:163], v119 offset:4096\n"
  "ds_read_b64_tr_b16 a[226:227], v119 offset:4104\n"
  "ds_read_b64_tr_b16 a[166:167], v119 offset:5120\n"
  "ds_read_b64_tr_b16 a[230:231], v119 offset:5128\n"
  "ds_read_b64_tr_b16 a[178:179], v119 offset:6144\n"
  "ds_read_b64_tr_b16 a[242:243], v119 offset:6152\n"
  "ds_read_b64_tr_b16 a[182:183], v119 offset:7168\n"
  "ds_read_b64_tr_b16 a[246:247], v119 offset:7176\n"
  "s_waitcnt lgkmcnt(0)\n"
  "s_add_u32 m0, s50, 0\n"
  "s_nop 0\n"
  "global_load_lds_dwordx4 v118, s[44:45]\n"
  "s_add_u32 s44, s44, 0x20000\n"
  "s_addc_u32 s45, s45, 0\n"
  "s_add_u32 m0, m0, 1024\n"
  "s_nop 0\n"
  "global_load_lds_dwordx4 v118, s[44:45]\n"
  "s_add_u32 s44, s44, 0x20000\n"
  "s_addc_u32 s45, s45, 0\n"
  "s_add_u32 m0, m0, 1024\n"
  "s_nop 0\n"
  "global_load_lds_dwordx4 v118, s[44:45]\n"
  "s_add_u32 s44, s44, 0x20000\n"
  "s_addc_u32 s45, s45, 0\n"
  "s_add_u32 m0, m0, 1024\n"
  "s_nop 0\n"
  "global_load_lds_dwordx4 v118, s[44:45]\n"
  "s_add_u32 s44, s44, 0x20000\n"
  "s_addc_u32 s45, s45, 0\n"
  "s_add_u32 m0, m0, 1024\n"
  "s_nop 0\n"
  "global_load_lds_dwordx4 v118, s[44:45]\n"
  "s_add_u32 s44, s44, 0x20000\n"
  "s_addc_u32 s45, s45, 0\n"
  "s_add_u32 m0, m0, 1024\n"
  "s_nop 0\n"
  "global_load_lds_dwordx4 v118, s[44:45]\n"
  "s_add_u32 s44, s44, 0x20000\n"
  "s_addc_u32 s45, s45, 0\n"
  "s_add_u32 m0, m0, 1024\n"
  "s_nop 0\n"
  "global_load_lds_dwordx4 v118, s[44:45]\n"
  "s_add_u32 s44, s44, 0x20000\n"
  "s_addc_u32 s45, s45, 0\n"
  "s_add_u32 m0, m0, 1024\n"
  "s_nop 0\n"
  "global_load_lds_dwordx4 v118, s[44:45]\n"
  "s_add_u32 s44, s44, 0x20000\n"
  "s_addc_u32 s45, s45, 0\n"
  "s_waitcnt vmcnt(16)\n"
  "ds_read_b64_tr_b16 a[138:139], v119 offset:8192\n"
  "ds_read_b64_tr_b16 a[202:203], v119 offset:8200\n"
  "ds_read_b64_tr_b16 a[142:143], v119 offset:9216\n"
  "ds_read_b64_tr_b16 a[206:207], v119 offset:9224\n"
  "ds_read_b64_tr_b16 a[154:155], v119 offset:10240\n"
  "ds_read_b64_tr_b16 a[218:219], v119 offset:10248\n"
  "ds_read_b64_tr_b16 a[158:159], v119 offset:11264\n"
  "ds_read_b64_tr_b16 a[222:223], v119 offset:11272\n"
  "ds_read_b64_tr_b16 a[170:171], v119 offset:12288\n"
  "ds_read_b64_tr_b16 a[234:235], v119 offset:12296\n"
  "ds_read_b64_tr_b16 a[174:175], v119 offset:13312\n"
  "ds_read_b64_tr_b16 a[238:239], v119 offset:13320\n"
  "ds_read_b64_tr_b16 a[186:187], v119 offset:14336\n"
  "ds_read_b64_tr_b16 a[250:251], v119 offset:14344\n"
  "ds_read_b64_tr_b16 a[190:191], v119 offset:15360\n"
  "ds_read_b64_tr_b16 a[254:255], v119 offset:15368\n"
  "s_waitcnt lgkmcnt(0)\n"
  "s_add_u32 m0, s50, 8192\n"
  "s_nop 0\n"
  "global_load_lds_dwordx4 v118, s[44:45]\n"
  "s_add_u32 s44, s44, 0x20000\n"
  "s_addc_u32 s45, s45, 0\n"
  "s_add_u32 m0, m0, 1024\n"
  "s_nop 0\n"
  "global_load_lds_dwordx4 v118, s[44:45]\n"
  "s_add_u32 s44, s44, 0x20000\n"
  "s_addc_u32 s45, s45, 0\n"
  "s_add_u32 m0, m0, 1024\n"
  "s_nop 0\n"
  "global_load_lds_dwordx4 v118, s[44:45]\n"
  "s_add_u32 s44, s44, 0x20000\n"
  "s_addc_u32 s45, s45, 0\n"
  "s_add_u32 m0, m0, 1024\n"
  "s_nop 0\n"
  "global_load_lds_dwordx4 v118, s[44:45]\n"
  "s_add_u32 s44, s44, 0x20000\n"
  "s_addc_u32 s45, s45, 0\n"
  "s_add_u32 m0, m0, 1024\n"
  "s_nop 0\n"
  "global_load_lds_dwordx4 v118, s[44:45]\n"
  "s_add_u32 s44, s44, 0x20000\n"
  "s_addc_u32 s45, s45, 0\n"
  "s_add_u32 m0, m0, 1024\n"
  "s_nop 0\n"
  "global_load_lds_dwordx4 v118, s[44:45]\n"
  "s_add_u32 s44, s44, 0x20000\n"
  "s_addc_u32 s45, s45, 0\n"
  "s_add_u32 m0, m0, 1024\n"
  "s_nop 0\n"
  "global_load_lds_dwordx4 v118, s[44:45]\n"
  "s_add_u32 s44, s44, 0x20000\n"
  "s_addc_u32 s45, s45, 0\n"
  "s_add_u32 m0, m0, 1024\n"
  "s_nop 0\n"
  "global_load_lds_dwordx4 v118, s[44:45]\n"
  "s_add_u32 s44, s44, 0x20000\n"
  "s_addc_u32 s45, s45, 0\n"
  ".Lq_h0:\n"
  "v_readfirstlane_b32 s56, v116\n"
  "v_readfirstlane_b32 s57, v117\n"
  "s_cmp_lg_u32 s56, s46\n"
  "s_cselect_b32 s57, s57, 0\n"
  "s_cmp_lg_u32 s57, 0\n"
  "s_cbranch_scc1 .Lq_h1\n"
  "s_sub_u32 s55, s55, 1\n"
  "s_cbranch_scc1 .Lq_end\n"
  "s_sleep 4\n"
  "buffer_load_dwordx2 v[116:117], off, s[36:39], s64 sc1\n"
  "s_waitcnt vmcnt(0)\n"
  "s_branch .Lq_h0\n"
  ".Lq_h1:\n"
  "s_lshl_b32 s56, s57, 13\n"
  "s_and_b32 s57, s56, s66\n"
  "s_and_b32 s56, s56, 0x2000\n"
  "s_xor_b32 s57, s57, s66\n"
  "s_xor_b32 s51, s51, s56\n"
  "s_lshr_b32 s53, s57, 13\n"
  "s_or_b32 s58, s58, s57\n"
  "s_or_b32 s59, s59, s57\n"
  "v_xor_b32 v113, s56, v113\n"
  "v_xor_b32 v112, s57, v112\n"
  "s_cmp_lg_u32 s68, 0\n"
  "s_cbranch_scc1 .Lq_go\n"
  "s_waitcnt vmcnt(16)\n"
  "ds_read_b64_tr_b16 v[128:129], v119 offset:16384\n"
  "ds_read_b64_tr_b16 v[192:193], v119 offset:16392\n"
  "ds_read_b64_tr_b16 v[132:133], v119 offset:17408\n"
  "ds_read_b64_tr_b16 v[196:197], v119 offset:17416\n"
  "ds_read_b64_tr_b16 v[144:145], v119 offset:18432\n"
  "ds_read_b64_tr_b16 v[208:209], v119 offset:18440\n"
  "ds_read_b64_tr_b16 v[148:149], v119 offset:19456\n"
  "ds_read_b64_tr_b16 v[212:213], v119 offset:19464\n"
  "ds_read_b64_tr_b16 v[160:161], v119 offset:20480\n"
  "ds_read_b64_tr_b16 v[224:225], v119 offset:20488\n"
  "ds_read_b64_tr_b16 v[164:165], v119 offset:21504\n"
  "ds_read_b64_tr_b16 v[228:229], v119 offset:21512\n"
  "ds_read_b64_tr_b16 v[176:177], v119 offset:22528\n"
  "ds_read_b64_tr_b16 v[240:241], v119 offset:22536\n"
  "ds_read_b64_tr_b16 v[180:181], v119 offset:23552\n"
  "ds_read_b64_tr_b16 v[244:245], v119 offset:23560\n"
  "s_waitcnt lgkmcnt(0)\n"
  "s_add_u32 m0, s50, 16384\n"
  "s_nop 0\n"
  "global_load_lds_dwordx4 v118, s[44:45]\n"
  "s_add_u32 s44, s44, 0x20000\n"
  "s_addc_u32 s45, s45, 0\n"
  "s_add_u32 m0, m0, 1024\n"
  "s_nop 0\n"
  "global_load_lds_dwordx4 v118, s[44:45]\n"
  "s_add_u32 s44, s44, 0x20000\n"
  "s_addc_u32 s45, s45, 0\n"
  "s_add_u32 m0, m0, 1024\n"
  "s_nop 0\n"
  "global_load_lds_dwordx4 v118, s[44:45]\n"
  "s_add_u32 s44, s44, 0x20000\n"
  "s_addc_u32 s45, s45, 0\n"
  "s_add_u32 m0, m0, 1024\n"
  "s_nop 0\n"
  "global_load_lds_dwordx4 v118, s[44:45]\n"
  "s_add_u32 s44, s44, 0x20000\n"
  "s_addc_u32 s45, s45, 0\n"
  "s_add_u32 m0, m0, 1024\n"
  "s_nop 0\n"
  "global_load_lds_dwordx4 v118, s[44:45]\n"
  "s_add_u32 s44, s44, 0x20000\n"
  "s_addc_u32 s45, s45, 0\n"
  "s_add_u32 m0, m0, 1024\n"
  "s_nop 0\n"
  "global_load_lds_dwordx4 v118, s[44:45]\n"
  "s_add_u32 s44, s44, 0x20000\n"
  "s_addc_u32 s45, s45, 0\n"
  "s_add_u32 m0, m0, 1024\n"
  "s_nop 0\n"
  "global_load_lds_dwordx4 v118, s[44:45]\n"
  "s_add_u32 s44, s44, 0x20000\n"
  "s_addc_u32 s45, s45, 0\n"
  "s_add_u32 m0, m0, 1024\n"
  "s_nop 0\n"
  "global_load_lds_dwordx4 v118, s[44:45]\n"
  "s_add_u32 s44, s44, 0x20000\n"
  "s_addc_u32 s45, s45, 0\n"
  "s_waitcnt vmcnt(16)\n"
  "ds_read_b64_tr_b16 v[136:137], v119 offset:0\n"
  "ds_read_b64_tr_b16 v[200:201], v119 offset:8\n"
  "ds_read_b64_tr_b16 v[140:141], v119 offset:1024\n"
  "ds_read_b64_tr_b16 v[204:205], v119 offset:1032\n"
  "ds_read_b64_tr_b16 v[152:153], v119 offset:2048\n"
  "ds_read_b64_tr_b16 v[216:217], v119 offset:2056\n"
  "ds_read_b64_tr_b16 v[156:157], v119 offset:3072\n"
  "ds_read_b64_tr_b16 v[220:221], v119 offset:3080\n"
  "ds_read_b64_tr_b16 v[168:169], v119 offset:4096\n"
  "ds_read_b64_tr_b16 v[232:233], v119 offset:4104\n"
  "ds_read_b64_tr_b16 v[172:173], v119 offset:5120\n"
  "ds_read_b64_tr_b16 v[236:237], v119 offset:5128\n"
  "ds_read_b64_tr_b16 v[184:185], v119 offset:6144\n"
  "ds_read_b64_tr_b16 v[248:249], v119 offset:6152\n"
  "ds_read_b64_tr_b16 v[188:189], v119 offset:7168\n"
  "ds_read_b64_tr_b16 v[252:253], v119 offset:7176\n"
  "s_waitcnt lgkmcnt(0)\n"
  "s_waitcnt vmcnt(8)\n"
  "ds_read_b64_tr_b16 v[130:131], v119 offset:8192\n"
  "ds_read_b64_tr_b16 v[194:195], v119 offset:8200\n"
  "ds_read_b64_tr_b16 v[134:135], v119 offset:9216\n"
  "ds_read_b64_tr_b16 v[198:199], v119 offset:9224\n"
  "ds_read_b64_tr_b16 v[146:147], v119 offset:10240\n"
  "ds_read_b64_tr_b16 v[210:211], v119 offset:10248\n"
  "ds_read_b64_tr_b16 v[150:151], v119 offset:11264\n"
  "ds_read_b64_tr_b16 v[214:215], v119 offset:11272\n"
  "ds_read_b64_tr_b16 v[162:163], v119 offset:12288\n"
  "ds_read_b64_tr_b16 v[226:227], v119 offset:12296\n"
  "ds_read_b64_tr_b16 v[166:167], v119 offset:13312\n"
  "ds_read_b64_tr_b16 v[230:231], v119 offset:13320\n"
  "ds_read_b64_tr_b16 v[178:179], v119 offset:14336\n"
  "ds_read_b64_tr_b16 v[242:243], v119 offset:14344\n"
  "ds_read_b64_tr_b16 v[182:183], v119 offset:15360\n"
  "ds_read_b64_tr_b16 v[246:247], v119 offset:15368\n"
  "s_waitcnt lgkmcnt(0)\n"
  "s_waitcnt vmcnt(0)\n"
  "ds_read_b64_tr_b16 v[138:139], v119 offset:16384\n"
  "ds_read_b64_tr_b16 v[202:203], v119 offset:16392\n"
  "ds_read_b64_tr_b16 v[142:143], v119 offset:17408\n"
  "ds_read_b64_tr_b16 v[206:207], v119 offset:17416\n"
  "ds_read_b64_tr_b16 v[154:155], v119 offset:18432\n"
  "ds_read_b64_tr_b16 v[218:219], v119 offset:18440\n"
  "ds_read_b64_tr_b16 v[158:159], v119 offset:19456\n"
  "ds_read_b64_tr_b16 v[222:223], v119 offset:19464\n"
  "ds_read_b64_tr_b16 v[170:171], v119 offset:20480\n"
  "ds_read_b64_tr_b16 v[234:235], v119 offset:20488\n"
  "ds_read_b64_tr_b16 v[174:175], v119 offset:21504\n"
  "ds_read_b64_tr_b16 v[238:239], v119 offset:21512\n"
  "ds_read_b64_tr_b16 v[186:187], v119 offset:22528\n"
  "ds_read_b64_tr_b16 v[250:251], v119 offset:22536\n"
  "ds_read_b64_tr_b16 v[190:191], v119 offset:23552\n"
  "ds_read_b64_tr_b16 v[254:255], v119 offset:23560\n"
  "s_waitcnt lgkmcnt(0)\n"
  ".Lq_b1:\n"
  "buffer_load_dword v115, v112, s[36:39], 0 offen sc1\n"
  "s_waitcnt vmcnt(0)\n"
  ".Lq_b1a:\n"
  "v_add_u32 v114, v115, v122\n"
  "v_cmp_gt_u32 vcc, s63, v114\n"
  "s_cbranch_vccz .Lq_b1b\n"
  "s_sub_u32 s55, s55, 1\n"
  "s_cbranch_scc1 .Lq_end\n"
  "s_sleep 1\n"
  "s_branch .Lq_b1\n"
  ".Lq_b1b:\n"
  "s_mov_b32 m0, s61\n"
  "s_nop 0\n"
  "buffer_load_dwordx4 v80, s[36:39], s58 offen offset:0 sc1 lds\n"
  "buffer_load_dwordx4 v81, s[36:39], s58 offen offset:1024 sc1 lds\n"
  "buffer_load_dwordx4 v82, s[36:39], s58 offen offset:2048 sc1 lds\n"
  "buffer_load_dwordx4 v83, s[36:39], s58 offen offset:3072 sc1 lds\n"
  "s_add_u32 m0, s61, 4096\n"
  "s_nop 0\n"
  "buffer_load_dwordx4 v84, s[36:39], s59 offen offset:0 sc1 lds\n"
  "buffer_load_dwordx4 v85, s[36:39], s59 offen offset:1024 sc1 lds\n"
  "buffer_load_dwordx4 v86, s[36:39], s59 offen offset:2048 sc1 lds\n"
  "buffer_load_dwordx4 v87, s[36:39], s59 offen offset:3072 sc1 lds\n"
  "s_waitcnt vmcnt(0)\n"
  ".Lq_go:\n"
  "s_waitcnt lgkmcnt(0)\n"
  "s_barrier\n"
  "ds_read_b128 v[48:51], v104 offset:0\n"
  "ds_read_b128 v[52:55], v104 offset:16384\n"
  "ds_read_b128 v[56:59], v105 offset:0\n"
  "ds_read_b128 v[60:63], v105 offset:16384\n"
  "ds_read_b128 v[64:67], v106 offset:0\n"
  "ds_read_b128 v[68:71], v106 offset:16384\n"
  "ds_read_b128 v[72:75], v107 offset:0\n"
  "ds_read_b128 v[76:79], v107 offset:16384\n"
  "s_mov_b32 s78, 0x80000000\n"
  "s_mov_b32 s79, 0x80000000\n"
  ".Lq_seg:\n"
  "s_waitcnt lgkmcnt(7)\n"
  "v_mfma_f32_16x16x32_bf16 v[0:3], a[0:3], v[48:51], 0\n"
  "s_add_u32 s60, s54, 1\n"
  "s_add_u32 s63, s54, 2\n"
  "s_bfe_u32 s56, s60, 0x10001\n"
  "v_mfma_f32_16x16x32_bf16 v[16:19], v[48:51], a[128:131], 0\n"
  "s_xor_b32 s56, s56, s53\n"
  "s_lshl_b32 s56, s56, 13\n"
  "s_lshl_b32 s58, s60, 20\n"
  "v_mfma_f32_16x16x32_bf16 v[4:7], a[64:67], v[48:51], 0\n"
  "s_add_u32 s58, s58, s52\n"
  "s_or_b32 s58, s58, s56\n"
  "v_cmp_gt_u32 vcc, s63, v114\n"
  "v_mfma_f32_16x16x32_bf16 v[24:27], v[48:51], a[192:195], 0\n"
  "ds_read_b128 v[48:51], v104 offset:256\n"
  "s_waitcnt lgkmcnt(7)\n"
  "v_mfma_f32_16x16x32_bf16 v[8:11], a[0:3], v[52:55], 0\n"
  "v_cvt_pk_bf16_f32 v32, v32, v33\n"
  "v_cvt_pk_bf16_f32 v33, v34, v35\n"
  "v_mfma_f32_16x16x32_bf16 v[20:23], v[52:55], a[128:131], 0\n"
  "v_cvt_pk_bf16_f32 v34, v36, v37\n"
  "v_mfma_f32_16x16x32_bf16 v[12:15], a[64:67], v[52:55], 0\n"
  "v_cvt_pk_bf16_f32 v35, v38, v39\n"
  "v_mfma_f32_16x16x32_bf16 v[28:31], v[52:55], a[192:195], 0\n"
  "ds_read_b128 v[52:55], v104 offset:16640\n"
  "s_waitcnt lgkmcnt(7)\n"
  "v_mfma_f32_16x16x32_bf16 v[0:3], a[4:7], v[56:59], v[0:3]\n"
  "v_cvt_pk_bf16_f32 v40, v40, v41\n"
  "v_cvt_pk_bf16_f32 v41, v42, v43\n"
  "v_mfma_f32_16x16x32_bf16 v[16:19], v[56:59], a[132:135], v[16:19]\n"
  "buffer_store_dwordx4 v[32:35], v110, s[36:39], s78 offen offset:0 sc1\n"
  "v_cvt_pk_bf16_f32 v42, v44, v45\n"
  "v_mfma_f32_16x16x32_bf16 v[4:7], a[68:71], v[56:59], v[4:7]\n"
  "v_cvt_pk_bf16_f32 v43, v46, v47\n"
  "v_mfma_f32_16x16x32_bf16 v[24:27], v[56:59], a[196:199], v[24:27]\n"
  "ds_read_b128 v[56:59], v105 offset:256\n"
  "s_waitcnt lgkmcnt(7)\n"
  "v_mfma_f32_16x16x32_bf16 v[8:11], a[4:7], v[60:63], v[8:11]\n"
  "s_add_u32 s59, s58, 4096\n"
  "s_xor_b32 s61, s61, 0x8000\n"
  "v_mfma_f32_16x16x32_bf16 v[20:23], v[60:63], a[132:135], v[20:23]\n"
  "buffer_store_dwordx4 v[40:43], v110, s[36:39], s79 offen offset:0 sc1\n"
  "v_mfma_f32_16x16x32_bf16 v[12:15], a[68:71], v[60:63], v[12:15]\n"
  "s_cbranch_vccz .Lq_w0\n"
  "v_mfma_f32_16x16x32_bf16 v[28:31], v[60:63], a[196:199], v[28:31]\n"
  "ds_read_b128 v[60:63], v105 offset:16640\n"
  "s_waitcnt lgkmcnt(7)\n"
  "v_mfma_f32_16x16x32_bf16 v[0:3], a[8:11], v[64:67], v[0:3]\n"
  "v_mfma_f32_16x16x32_bf16 v[16:19], v[64:67], a[136:139], v[16:19]\n"
  "v_mfma_f32_16x16x32_bf16 v[4:7], a[72:75], v[64:67], v[4:7]\n"
  "v_mfma_f32_16x16x32_bf16 v[24:27], v[64:67], a[200:203], v[24:27]\n"
  "ds_read_b128 v[64:67], v106 offset:256\n"
  "s_waitcnt lgkmcnt(7)\n"
  "v_mfma_f32_16x16x32_bf16 v[8:11], a[8:11], v[68:71], v[8:11]\n"
  "v_mfma_f32_16x16x32_bf16 v[20:23], v[68:71], a[136:139], v[20:23]\n"
  "v_mfma_f32_16x16x32_bf16 v[12:15], a[72:75], v[68:71], v[12:15]\n"
  "v_mfma_f32_16x16x32_bf16 v[28:31], v[68:71], a[200:203], v[28:31]\n"
  "ds_read_b128 v[68:71], v106 offset:16640\n"
  "s_waitcnt lgkmcnt(7)\n"
  "v_mfma_f32_16x16x32_bf16 v[0:3], a[12:15], v[72:75], v[0:3]\n"
  "v_mfma_f32_16x16x32_bf16 v[16:19], v[72:75], a[140:143], v[16:19]\n"
  "v_mfma_f32_16x16x32_bf16 v[4:7], a[76:79], v[72:75], v[4:7]\n"
  "v_mfma_f32_16x16x32_bf16 v[24:27], v[72:75], a[204:207], v[24:27]\n"
  "ds_read_b128 v[72:75], v107 offset:256\n"
  "s_waitcnt lgkmcnt(7)\n"
  "v_mfma_f32_16x16x32_bf16 v[8:11], a[12:15], v[76:79], v[8:11]\n"
  "v_mfma_f32_16x16x32_bf16 v[20:23], v[76:79], a[140:143], v[20:23]\n"
  "v_mfma_f32_16x16x32_bf16 v[12:15], a[76:79], v[76:79], v[12:15]\n"
  "v_mfma_f32_16x16x32_bf16 v[28:31], v[76:79], a[204:207], v[28:31]\n"
  "ds_read_b128 v[76:79], v107 offset:16640\n"
  "s_waitcnt lgkmcnt(7)\n"
  "v_mfma_f32_16x16x32_bf16 v[0:3], a[16:19], v[48:51], v[0:3]\n"
  "v_mfma_f32_16x16x32_bf16 v[16:19], v[48:51], a[144:147], v[16:19]\n"
  "v_mfma_f32_16x16x32_bf16 v[4:7], a[80:83], v[48:51], v[4:7]\n"
  "v_mfma_f32_16x16x32_bf16 v[24:27], v[48:51], a[208:211], v[24:27]\n"
  "ds_read_b128 v[48:51], v104 offset:512\n"
  "s_waitcnt lgkmcnt(7)\n"
  "v_mfma_f32_16x16x32_bf16 v[8:11], a[16:19], v[52:55], v[8:11]\n"
  "v_mfma_f32_16x16x32_bf16 v[20:23], v[52:55], a[144:147], v[20:23]\n"
  "v_mfma_f32_16x16x32_bf16 v[12:15], a[80:83], v[52:55], v[12:15]\n"
  "v_mfma_f32_16x16x32_bf16 v[28:31], v[52:55], a[208:211], v[28:31]\n"
  "ds_read_b128 v[52:55], v104 offset:16896\n"
  "s_waitcnt lgkmcnt(7)\n"
  "v_mfma_f32_16x16x32_bf16 v[0:3], a[20:23], v[56:59], v[0:3]\n"
  "v_mfma_f32_16x16x32_bf16 v[16:19], v[56:59], a[148:151], v[16:19]\n"
  "v_mfma_f32_16x16x32_bf16 v[4:7], a[84:87], v[56:59], v[4:7]\n"
  "v_mfma_f32_16x16x32_bf16 v[24:27], v[56:59], a[212:215], v[24:27]\n"
  "ds_read_b128 v[56:59], v105 offset:512\n"
  "s_waitcnt lgkmcnt(7)\n"
  "v_mfma_f32_16x16x32_bf16 v[8:11], a[20:23], v[60:63], v[8:11]\n"
  "v_mfma_f32_16x16x32_bf16 v[20:23], v[60:63], a[148:151], v[20:23]\n"
  "v_mfma_f32_16x16x32_bf16 v[12:15], a[84:87], v[60:63], v[12:15]\n"
  "v_mfma_f32_16x16x32_bf16 v[28:31], v[60:63], a[212:215], v[28:31]\n"
  "ds_read_b128 v[60:63], v105 offset:16896\n"
  "s_waitcnt lgkmcnt(7)\n"
  "v_mfma_f32_16x16x32_bf16 v[0:3], a[24:27], v[64:67], v[0:3]\n"
  "v_mfma_f32_16x16x32_bf16 v[16:19], v[64:67], a[152:155], v[16:19]\n"
  "v_mfma_f32_16x16x32_bf16 v[4:7], a[88:91], v[64:67], v[4:7]\n"
  "v_mfma_f32_16x16x32_bf16 v[24:27], v[64:67], a[216:219], v[24:27]\n"
  "ds_read_b128 v[64:67], v106 offset:512\n"
  "s_waitcnt lgkmcnt(7)\n"
  "v_mfma_f32_16x16x32_bf16 v[8:11], a[24:27], v[68:71], v[8:11]\n"
  "v_mfma_f32_16x16x32_bf16 v[20:23], v[68:71], a[152:155], v[20:23]\n"
  "v_mfma_f32_16x16x32_bf16 v[12:15], a[88:91], v[68:71], v[12:15]\n"
  "v_mov_b32 v116, s54\n"
  "v_mfma_f32_16x16x32_bf16 v[28:31], v[68:71], a[216:219], v[28:31]\n"
  "ds_read_b128 v[68:71], v106 offset:16896\n"
  "s_waitcnt lgkmcnt(7)\n"
  "v_mfma_f32_16x16x32_bf16 v[0:3], a[28:31], v[72:75], v[0:3]\n"
  "s_waitcnt vmcnt(0)\n"
  "buffer_store_dword v116, v113, s[36:39], 0 offen offset:512 sc1\n"
  "v_mfma_f32_16x16x32_bf16 v[16:19], v[72:75], a[156:159], v[16:19]\n"
  "v_mfma_f32_16x16x32_bf16 v[4:7], a[92:95], v[72:75], v[4:7]\n"
  "buffer_load_dword v89, v112, s[36:39], 0 offen sc1\n"
  "v_mfma_f32_16x16x32_bf16 v[24:27], v[72:75], a[220:223], v[24:27]\n"
  "ds_read_b128 v[72:75], v107 offset:512\n"
  "s_waitcnt lgkmcnt(7)\n"
  "v_mfma_f32_16x16x32_bf16 v[8:11], a[28:31], v[76:79], v[8:11]\n"
  "s_add_u32 s56, s51, 16384\n"
  "s_add_u32 s57, s51, 32768\n"
  "v_mfma_f32_16x16x32_bf16 v[20:23], v[76:79], a[156:159], v[20:23]\n"
  "v_mfma_f32_16x16x32_bf16 v[12:15], a[92:95], v[76:79], v[12:15]\n"
  "v_mfma_f32_16x16x32_bf16 v[28:31], v[76:79], a[220:223], v[28:31]\n"
  "ds_read_b128 v[76:79], v107 offset:16896\n"
  "s_waitcnt lgkmcnt(7)\n"
  "v_mfma_f32_16x16x32_bf16 v[0:3], a[32:35], v[48:51], v[0:3]\n"
  "v_mfma_f32_16x16x32_bf16 v[16:19], v[48:51], a[160:163], v[16:19]\n"
  "v_mfma_f32_16x16x32_bf16 v[4:7], a[96:99], v[48:51], v[4:7]\n"
  "v_mfma_f32_16x16x32_bf16 v[24:27], v[48:51], a[224:227], v[24:27]\n"
  "ds_read_b128 v[48:51], v104 offset:768\n"
  "s_waitcnt lgkmcnt(7)\n"
  "v_mfma_f32_16x16x32_bf16 v[8:11], a[32:35], v[52:55], v[8:11]\n"
  "v_mfma_f32_16x16x32_bf16 v[20:23], v[52:55], a[160:163], v[20:23]\n"
  "v_mfma_f32_16x16x32_bf16 v[12:15], a[96:99], v[52:55], v[12:15]\n"
  "v_mfma_f32_16x16x32_bf16 v[28:31], v[52:55], a[224:227], v[28:31]\n"
  "ds_read_b128 v[52:55], v104 offset:17152\n"
  "s_waitcnt lgkmcnt(7)\n"
  "v_mfma_f32_16x16x32_bf16 v[0:3], a[36:39], v[56:59], v[0:3]\n"
  "v_mfma_f32_16x16x32_bf16 v[16:19], v[56:59], a[164:167], v[16:19]\n"
  "v_mfma_f32_16x16x32_bf16 v[4:7], a[100:103], v[56:59], v[4:7]\n"
  "v_mfma_f32_16x16x32_bf16 v[24:27], v[56:59], a[228:231], v[24:27]\n"
  "ds_read_b128 v[56:59], v105 offset:768\n"
  "s_waitcnt lgkmcnt(7)\n"
  "v_mfma_f32_16x16x32_bf16 v[8:11], a[36:39], v[60:63], v[8:11]\n"
  "v_mfma_f32_16x16x32_bf16 v[20:23], v[60:63], a[164:167], v[20:23]\n"
  "v_mfma_f32_16x16x32_bf16 v[12:15], a[100:103], v[60:63], v[12:15]\n"
  "v_mfma_f32_16x16x32_bf16 v[28:31], v[60:63], a[228:231], v[28:31]\n"
  "ds_read_b128 v[60:63], v105 offset:17152\n"
  "s_waitcnt lgkmcnt(7)\n"
  "v_mfma_f32_16x16x32_bf16 v[0:3], a[40:43], v[64:67], v[0:3]\n"
  "v_mfma_f32_16x16x32_bf16 v[16:19], v[64:67], a[168:171], v[16:19]\n"
  "v_mfma_f32_16x16x32_bf16 v[4:7], a[104:107], v[64:67], v[4:7]\n"
  "v_mfma_f32_16x16x32_bf16 v[24:27], v[64:67], a[232:235], v[24:27]\n"
  "ds_read_b128 v[64:67], v106 offset:768\n"
  "s_waitcnt lgkmcnt(7)\n"
  "v_mfma_f32_16x16x32_bf16 v[8:11], a[40:43], v[68:71], v[8:11]\n"
  "v_mfma_f32_16x16x32_bf16 v[20:23], v[68:71], a[168:171], v[20:23]\n"
  "v_mfma_f32_16x16x32_bf16 v[12:15], a[104:107], v[68:71], v[12:15]\n"
  "v_mfma_f32_16x16x32_bf16 v[28:31], v[68:71], a[232:235], v[28:31]\n"
  "ds_read_b128 v[68:71], v106 offset:17152\n"
  "s_waitcnt lgkmcnt(7)\n"
  "v_mfma_f32_16x16x32_bf16 v[0:3], a[44:47], v[72:75], v[0:3]\n"
  "v_mfma_f32_16x16x32_bf16 v[16:19], v[72:75], a[172:175], v[16:19]\n"
  "v_mfma_f32_16x16x32_bf16 v[4:7], a[108:111], v[72:75], v[4:7]\n"
  "v_mfma_f32_16x16x32_bf16 v[24:27], v[72:75], a[236:239], v[24:27]\n"
  "ds_read_b128 v[72:75], v107 offset:768\n"
  "s_waitcnt lgkmcnt(7)\n"
  "v_mfma_f32_16x16x32_bf16 v[8:11], a[44:47], v[76:79], v[8:11]\n"
  "v_mfma_f32_16x16x32_bf16 v[20:23], v[76:79], a[172:175], v[20:23]\n"
  "v_mfma_f32_16x16x32_bf16 v[12:15], a[108:111], v[76:79], v[12:15]\n"
  "v_mfma_f32_16x16x32_bf16 v[28:31], v[76:79], a[236:239], v[28:31]\n"
  "ds_read_b128 v[76:79], v107 offset:17152\n"
  "s_waitcnt lgkmcnt(7)\n"
  "v_mfma_f32_16x16x32_bf16 v[0:3], a[48:51], v[48:51], v[0:3]\n"
  "v_mfma_f32_16x16x32_bf16 v[16:19], v[48:51], a[176:179], v[16:19]\n"
  "v_mfma_f32_16x16x32_bf16 v[4:7], a[112:115], v[48:51], v[4:7]\n"
  "v_mfma_f32_16x16x32_bf16 v[24:27], v[48:51], a[240:243], v[24:27]\n"
  "ds_read_b128 v[48:51], v104 offset:0\n"
  "s_waitcnt lgkmcnt(7)\n"
  "v_mfma_f32_16x16x32_bf16 v[8:11], a[48:51], v[52:55], v[8:11]\n"
  "s_waitcnt vmcnt(0)\n"
  "v_add_u32 v114, v89, v122\n"
  "v_cmp_gt_u32 vcc, s63, v114\n"
  "v_mfma_f32_16x16x32_bf16 v[20:23], v[52:55], a[176:179], v[20:23]\n"
  "s_cbranch_vccz .Lq_w1\n"
  "v_mfma_f32_16x16x32_bf16 v[12:15], a[112:115], v[52:55], v[12:15]\n"
  "v_mfma_f32_16x16x32_bf16 v[28:31], v[52:55], a[240:243], v[28:31]\n"
  "ds_read_b128 v[52:55], v104 offset:16384\n"
  "s_waitcnt lgkmcnt(7)\n"
  "v_mfma_f32_16x16x32_bf16 v[0:3], a[52:55], v[56:59], v[0:3]\n"
  "buffer_load_dword v90, v112, s[36:39], 0 offen sc1\n"
  "v_mfma_f32_16x16x32_bf16 v[16:19], v[56:59], a[180:183], v[16:19]\n"
  "v_mfma_f32_16x16x32_bf16 v[4:7], a[116:119], v[56:59], v[4:7]\n"
  "v_mfma_f32_16x16x32_bf16 v[24:27], v[56:59], a[244:247], v[24:27]\n"
  "ds_read_b128 v[56:59], v105 offset:0\n"
  "s_waitcnt lgkmcnt(7)\n"
  "v_mfma_f32_16x16x32_bf16 v[8:11], a[52:55], v[60:63], v[8:11]\n"
  "v_mfma_f32_16x16x32_bf16 v[20:23], v[60:63], a[180:183], v[20:23]\n"
  "v_mfma_f32_16x16x32_bf16 v[12:15], a[116:119], v[60:63], v[12:15]\n"
  "v_mfma_f32_16x16x32_bf16 v[28:31], v[60:63], a[244:247], v[28:31]\n"
  "ds_read_b128 v[60:63], v105 offset:16384\n"
  "s_waitcnt lgkmcnt(7)\n"
  "v_mfma_f32_16x16x32_bf16 v[0:3], a[56:59], v[64:67], v[0:3]\n"
  "v_mfma_f32_16x16x32_bf16 v[16:19], v[64:67], a[184:187], v[16:19]\n"
  "v_mfma_f32_16x16x32_bf16 v[4:7], a[120:123], v[64:67], v[4:7]\n"
  "v_mfma_f32_16x16x32_bf16 v[24:27], v[64:67], a[248:251], v[24:27]\n"
  "ds_read_b128 v[64:67], v106 offset:0\n"
  "s_waitcnt lgkmcnt(7)\n"
  "v_mfma_f32_16x16x32_bf16 v[8:11], a[56:59], v[68:71], v[8:11]\n"
  "v_mfma_f32_16x16x32_bf16 v[20:23], v[68:71], a[184:187], v[20:23]\n"
  "v_mfma_f32_16x16x32_bf16 v[12:15], a[120:123], v[68:71], v[12:15]\n"
  "v_cmp_ge_u32 s[48:49], s63, v114\n"
  "v_mfma_f32_16x16x32_bf16 v[28:31], v[68:71], a[248:251], v[28:31]\n"
  "ds_read_b128 v[68:71], v106 offset:16384\n"
  "s_waitcnt lgkmcnt(7)\n"
  "v_mfma_f32_16x16x32_bf16 v[0:3], a[60:63], v[72:75], v[0:3]\n"
  "buffer_load_dword v91, v112, s[36:39], 0 offen sc1\n"
  "v_mfma_f32_16x16x32_bf16 v[16:19], v[72:75], a[188:191], v[16:19]\n"
  "v_mfma_f32_16x16x32_bf16 v[4:7], a[124:127], v[72:75], v[4:7]\n"
  "s_cmp_eq_u64 s[48:49], 0\n"
  "s_cselect_b32 s60, 0x80000000, 0\n"
  "v_mfma_f32_16x16x32_bf16 v[24:27], v[72:75], a[252:255], v[24:27]\n"
  "ds_read_b128 v[72:75], v107 offset:0\n"
  "s_waitcnt lgkmcnt(7)\n"
  "v_mfma_f32_16x16x32_bf16 v[8:11], a[60:63], v[76:79], v[8:11]\n"
  "v_mfma_f32_16x16x32_bf16 v[20:23], v[76:79], a[188:191], v[20:23]\n"
  "v_cvt_pk_bf16_f32 v0, v0, v1\n"
  "v_cvt_pk_bf16_f32 v1, v2, v3\n"
  "v_mfma_f32_16x16x32_bf16 v[12:15], a[124:127], v[76:79], v[12:15]\n"
  "v_cvt_pk_bf16_f32 v2, v4, v5\n"
  "v_cvt_pk_bf16_f32 v3, v6, v7\n"
  "s_cmp_lg_u32 s68, 0\n"
  "v_mfma_f32_16x16x32_bf16 v[28:31], v[76:79], a[252:255], v[28:31]\n"
  "ds_read_b128 v[76:79], v107 offset:16384\n"
  "buffer_store_dwordx4 v[0:3], v109, s[36:39], s56 offen offset:0 sc1\n"
  "s_cbranch_scc1 .Lq_first\n"
  "s_waitcnt lgkmcnt(7)\n"
  "v_mfma_f32_16x16x32_bf16 v[32:35], v[128:131], v[48:51], 0\n"
  "v_cvt_pk_bf16_f32 v16, v16, v17\n"
  "v_cvt_pk_bf16_f32 v17, v18, v19\n"
  "v_mfma_f32_16x16x32_bf16 v[36:39], v[192:195], v[48:51], 0\n"
  "ds_read_b128 v[48:51], v104 offset:256\n"
  "v_cvt_pk_bf16_f32 v18, v20, v21\n"
  "v_cvt_pk_bf16_f32 v19, v22, v23\n"
  "s_waitcnt lgkmcnt(7)\n"
  "v_mfma_f32_16x16x32_bf16 v[40:43], v[128:131], v[52:55], 0\n"
  "buffer_store_dwordx4 v[16:19], v109, s[36:39], s57 offen offset:0 sc1\n"
  "v_cvt_pk_bf16_f32 v8, v8, v9\n"
  "v_cvt_pk_bf16_f32 v9, v10, v11\n"
  "v_mfma_f32_16x16x32_bf16 v[44:47], v[192:195], v[52:55], 0\n"
  "ds_read_b128 v[52:55], v104 offset:16640\n"
  "v_cvt_pk_bf16_f32 v10, v12, v13\n"
  "v_cvt_pk_bf16_f32 v11, v14, v15\n"
  "s_waitcnt lgkmcnt(7)\n"
  "v_mfma_f32_16x16x32_bf16 v[32:35], v[132:135], v[56:59], v[32:35]\n"
  "buffer_store_dwordx4 v[8:11], v109, s[36:39], s56 offen offset:1024 sc1\n"
  "v_cvt_pk_bf16_f32 v24, v24, v25\n"
  "v_cvt_pk_bf16_f32 v25, v26, v27\n"
  "v_mfma_f32_16x16x32_bf16 v[36:39], v[196:199], v[56:59], v[36:39]\n"
  "ds_read_b128 v[56:59], v105 offset:256\n"
  "v_cvt_pk_bf16_f32 v26, v28, v29\n"
  "v_cvt_pk_bf16_f32 v27, v30, v31\n"
  "s_waitcnt lgkmcnt(7)\n"
  "v_mfma_f32_16x16x32_bf16 v[40:43], v[132:135], v[60:63], v[40:43]\n"
  "buffer_store_dwordx4 v[24:27], v109, s[36:39], s57 offen offset:1024 sc1\n"
  "v_mfma_f32_16x16x32_bf16 v[44:47], v[196:199], v[60:63], v[44:47]\n"
  "ds_read_b128 v[60:63], v105 offset:16640\n"
  "s_waitcnt lgkmcnt(7)\n"
  "v_mfma_f32_16x16x32_bf16 v[32:35], v[136:139], v[64:67], v[32:35]\n"
  "v_mfma_f32_16x16x32_bf16 v[36:39], v[200:203], v[64:67], v[36:39]\n"
  "ds_read_b128 v[64:67], v106 offset:256\n"
  "s_waitcnt lgkmcnt(7)\n"
  "v_mfma_f32_16x16x32_bf16 v[40:43], v[136:139], v[68:71], v[40:43]\n"
  "v_mfma_f32_16x16x32_bf16 v[44:47], v[200:203], v[68:71], v[44:47]\n"
  "ds_read_b128 v[68:71], v106 offset:16640\n"
  "s_waitcnt lgkmcnt(7)\n"
  "v_mfma_f32_16x16x32_bf16 v[32:35], v[140:143], v[72:75], v[32:35]\n"
  "v_mfma_f32_16x16x32_bf16 v[36:39], v[204:207], v[72:75], v[36:39]\n"
  "ds_read_b128 v[72:75], v107 offset:256\n"
  "s_waitcnt lgkmcnt(7)\n"
  "v_mfma_f32_16x16x32_bf16 v[40:43], v[140:143], v[76:79], v[40:43]\n"
  "v_mfma_f32_16x16x32_bf16 v[44:47], v[204:207], v[76:79], v[44:47]\n"
  "ds_read_b128 v[76:79], v107 offset:16640\n"
  "s_waitcnt lgkmcnt(7)\n"
  "v_mfma_f32_16x16x32_bf16 v[32:35], v[144:147], v[48:51], v[32:35]\n"
  "v_mfma_f32_16x16x32_bf16 v[36:39], v[208:211], v[48:51], v[36:39]\n"
  "ds_read_b128 v[48:51], v104 offset:512\n"
  "s_waitcnt lgkmcnt(7)\n"
  "v_mfma_f32_16x16x32_bf16 v[40:43], v[144:147], v[52:55], v[40:43]\n"
  "v_mfma_f32_16x16x32_bf16 v[44:47], v[208:211], v[52:55], v[44:47]\n"
  "ds_read_b128 v[52:55], v104 offset:16896\n"
  "s_waitcnt lgkmcnt(7)\n"
  "v_mfma_f32_16x16x32_bf16 v[32:35], v[148:151], v[56:59], v[32:35]\n"
  "v_mfma_f32_16x16x32_bf16 v[36:39], v[212:215], v[56:59], v[36:39]\n"
  "ds_read_b128 v[56:59], v105 offset:512\n"
  "s_waitcnt vmcnt(5)\n"
  "v_add_u32 v114, v90, v122\n"
  "v_cmp_gt_u32 vcc, s63, v114\n"
  "s_waitcnt lgkmcnt(7)\n"
  "v_mfma_f32_16x16x32_bf16 v[40:43], v[148:151], v[60:63], v[40:43]\n"
  "buffer_load_dword v115, v112, s[36:39], s60 offen sc1\n"
  "s_cbranch_vccz .Lq_w2\n"
  "v_mfma_f32_16x16x32_bf16 v[44:47], v[212:215], v[60:63], v[44:47]\n"
  "ds_read_b128 v[60:63], v105 offset:16896\n"
  "s_waitcnt lgkmcnt(7)\n"
  "v_mfma_f32_16x16x32_bf16 v[32:35], v[152:155], v[64:67], v[32:35]\n"
  "v_mfma_f32_16x16x32_bf16 v[36:39], v[216:219], v[64:67], v[36:39]\n"
  "ds_read_b128 v[64:67], v106 offset:512\n"
  "s_waitcnt lgkmcnt(7)\n"
  "v_mfma_f32_16x16x32_bf16 v[40:43], v[152:155], v[68:71], v[40:43]\n"
  "v_mfma_f32_16x16x32_bf16 v[44:47], v[216:219], v[68:71], v[44:47]\n"
  "ds_read_b128 v[68:71], v106 offset:16896\n"
  "s_waitcnt lgkmcnt(7)\n"
  "v_mfma_f32_16x16x32_bf16 v[32:35], v[156:159], v[72:75], v[32:35]\n"
  "v_mfma_f32_16x16x32_bf16 v[36:39], v[220:223], v[72:75], v[36:39]\n"
  "ds_read_b128 v[72:75], v107 offset:512\n"
  "s_waitcnt lgkmcnt(7)\n"
  "v_mfma_f32_16x16x32_bf16 v[40:43], v[156:159], v[76:79], v[40:43]\n"
  "v_mfma_f32_16x16x32_bf16 v[44:47], v[220:223], v[76:79], v[44:47]\n"
  "ds_read_b128 v[76:79], v107 offset:16896\n"
  "s_waitcnt lgkmcnt(7)\n"
  "v_mfma_f32_16x16x32_bf16 v[32:35], v[160:163], v[48:51], v[32:35]\n"
  "v_mfma_f32_16x16x32_bf16 v[36:39], v[224:227], v[48:51], v[36:39]\n"
  "ds_read_b128 v[48:51], v104 offset:768\n"
  "s_waitcnt lgkmcnt(7)\n"
  "v_mfma_f32_16x16x32_bf16 v[40:43], v[160:163], v[52:55], v[40:43]\n"
  "v_mfma_f32_16x16x32_bf16 v[44:47], v[224:227], v[52:55], v[44:47]\n"
  "ds_read_b128 v[52:55], v104 offset:17152\n"
  "s_waitcnt lgkmcnt(7)\n"
  "v_mfma_f32_16x16x32_bf16 v[32:35], v[164:167], v[56:59], v[32:35]\n"
  "v_xor_b32 v104, 0x8000, v104\n"
  "buffer_load_dword v92, v112, s[36:39], 0 offen sc1\n"
  "v_mfma_f32_16x16x32_bf16 v[36:39], v[228:231], v[56:59], v[36:39]\n"
  "ds_read_b128 v[56:59], v105 offset:768\n"
  "s_waitcnt vmcnt(6)\n"
  "v_add_u32 v114, v91, v122\n"
  "v_cmp_gt_u32 vcc, s63, v114\n"
  "s_waitcnt lgkmcnt(7)\n"
  "v_mfma_f32_16x16x32_bf16 v[40:43], v[164:167], v[60:63], v[40:43]\n"
  "s_cbranch_vccz .Lq_w3\n"
  "v_mfma_f32_16x16x32_bf16 v[44:47], v[228:231], v[60:63], v[44:47]\n"
  "ds_read_b128 v[60:63], v105 offset:17152\n"
  "s_waitcnt lgkmcnt(7)\n"
  "v_mfma_f32_16x16x32_bf16 v[32:35], v[168:171], v[64:67], v[32:35]\n"
  "v_xor_b32 v105, 0x8000, v105\n"
  "v_mfma_f32_16x16x32_bf16 v[36:39], v[232:235], v[64:67], v[36:39]\n"
  "ds_read_b128 v[64:67], v106 offset:768\n"
  "s_waitcnt lgkmcnt(7)\n"
  "v_mfma_f32_16x16x32_bf16 v[40:43], v[168:171], v[68:71], v[40:43]\n"
  "v_mfma_f32_16x16x32_bf16 v[44:47], v[232:235], v[68:71], v[44:47]\n"
  "ds_read_b128 v[68:71], v106 offset:17152\n"
  "s_add_u32 s54, s54, 1\n"
  "s_add_u32 s57, s51, 4096\n"
  "v_mov_b32 v116, s54\n"
  "s_waitcnt lgkmcnt(7)\n"
  "v_mfma_f32_16x16x32_bf16 v[32:35], v[172:175], v[72:75], v[32:35]\n"
  "v_xor_b32 v106, 0x8000, v106\n"
  "s_waitcnt vmcnt(2)\n"
  "buffer_store_dword v116, v113, s[36:39], 0 offen offset:0 sc1\n"
  "v_mfma_f32_16x16x32_bf16 v[36:39], v[236:239], v[72:75], v[36:39]\n"
  "ds_read_b128 v[72:75], v107 offset:768\n"
  "s_waitcnt lgkmcnt(7)\n"
  "v_mfma_f32_16x16x32_bf16 v[40:43], v[172:175], v[76:79], v[40:43]\n"
  "v_mfma_f32_16x16x32_bf16 v[44:47], v[236:239], v[76:79], v[44:47]\n"
  "ds_read_b128 v[76:79], v107 offset:17152\n"
  "s_waitcnt lgkmcnt(7)\n"
  "v_mfma_f32_16x16x32_bf16 v[32:35], v[176:179], v[48:51], v[32:35]\n"
  "v_xor_b32 v107, 0x8000, v107\n"
  "v_mfma_f32_16x16x32_bf16 v[36:39], v[240:243], v[48:51], v[36:39]\n"
  "s_waitcnt lgkmcnt(6)\n"
  "v_mfma_f32_16x16x32_bf16 v[40:43], v[176:179], v[52:55], v[40:43]\n"
  "v_mfma_f32_16x16x32_bf16 v[44:47], v[240:243], v[52:55], v[44:47]\n"
  "s_waitcnt lgkmcnt(5)\n"
  "v_mfma_f32_16x16x32_bf16 v[32:35], v[180:183], v[56:59], v[32:35]\n"
  "v_mfma_f32_16x16x32_bf16 v[36:39], v[244:247], v[56:59], v[36:39]\n"
  "s_waitcnt lgkmcnt(4)\n"
  "v_mfma_f32_16x16x32_bf16 v[40:43], v[180:183], v[60:63], v[40:43]\n"
  "v_mfma_f32_16x16x32_bf16 v[44:47], v[244:247], v[60:63], v[44:47]\n"
  "s_waitcnt lgkmcnt(3)\n"
  "v_mfma_f32_16x16x32_bf16 v[32:35], v[184:187], v[64:67], v[32:35]\n"
  "v_mfma_f32_16x16x32_bf16 v[36:39], v[248:251], v[64:67], v[36:39]\n"
  "s_waitcnt lgkmcnt(2)\n"
  "v_mfma_f32_16x16x32_bf16 v[40:43], v[184:187], v[68:71], v[40:43]\n"
  "v_mfma_f32_16x16x32_bf16 v[44:47], v[248:251], v[68:71], v[44:47]\n"
  "s_waitcnt lgkmcnt(1)\n"
  "v_mfma_f32_16x16x32_bf16 v[32:35], v[188:191], v[72:75], v[32:35]\n"
  "v_mfma_f32_16x16x32_bf16 v[36:39], v[252:255], v[72:75], v[36:39]\n"
  "s_waitcnt lgkmcnt(0)\n"
  "v_mfma_f32_16x16x32_bf16 v[40:43], v[188:191], v[76:79], v[40:43]\n"
  "v_mfma_f32_16x16x32_bf16 v[44:47], v[252:255], v[76:79], v[44:47]\n"
  ".Lq_e:\n"
  "s_waitcnt vmcnt(2)\n"
  "v_add_u32 v115, v115, v122\n"
  "v_max_u32 v114, v114, v115\n"
  "v_cmp_gt_u32 vcc, s63, v114\n"
  "s_cbranch_vccnz .Lq_l2\n"
  "s_mov_b32 m0, s61\n"
  "s_nop 0\n"
  "buffer_load_dwordx4 v80, s[36:39], s58 offen offset:0 sc1 lds\n"
  "buffer_load_dwordx4 v81, s[36:39], s58 offen offset:1024 sc1 lds\n"
  "buffer_load_dwordx4 v82, s[36:39], s58 offen offset:2048 sc1 lds\n"
  "buffer_load_dwordx4 v83, s[36:39], s58 offen offset:3072 sc1 lds\n"
  "s_add_u32 m0, s61, 4096\n"
  "s_nop 0\n"
  "buffer_load_dwordx4 v84, s[36:39], s59 offen offset:0 sc1 lds\n"
  "buffer_load_dwordx4 v85, s[36:39], s59 offen offset:1024 sc1 lds\n"
  "buffer_load_dwordx4 v86, s[36:39], s59 offen offset:2048 sc1 lds\n"
  "buffer_load_dwordx4 v87, s[36:39], s59 offen offset:3072 sc1 lds\n"
  "v_cvt_pk_bf16_f32 v32, v32, v33\n"
  "v_cvt_pk_bf16_f32 v33, v34, v35\n"
  "v_cvt_pk_bf16_f32 v34, v36, v37\n"
  "v_cvt_pk_bf16_f32 v35, v38, v39\n"
  "buffer_store_dwordx4 v[32:35], v110, s[36:39], s51 offen offset:0 sc1\n"
  "v_cvt_pk_bf16_f32 v40, v40, v41\n"
  "v_cvt_pk_bf16_f32 v41, v42, v43\n"
  "v_cvt_pk_bf16_f32 v42, v44, v45\n"
  "v_cvt_pk_bf16_f32 v43, v46, v47\n"
  "buffer_store_dwordx4 v[40:43], v110, s[36:39], s57 offen offset:0 sc1\n"
  "s_add_u32 s51, s51, 0x200000\n"
  "s_xor_b32 s51, s51, 8192\n"
  "s_bitset0_b32 s51, 26\n"
  "s_waitcnt vmcnt(2)\n"
  "s_branch .Lq_go\n"
  ".Lq_l2:\n"
  "buffer_load_dword v115, v112, s[36:39], 0 offen sc1\n"
  "v_cvt_pk_bf16_f32 v32, v32, v33\n"
  "v_cvt_pk_bf16_f32 v33, v34, v35\n"
  "v_cvt_pk_bf16_f32 v34, v36, v37\n"
  "v_cvt_pk_bf16_f32 v35, v38, v39\n"
  "buffer_store_dwordx4 v[32:35], v110, s[36:39], s51 offen offset:0 sc1\n"
  "v_cvt_pk_bf16_f32 v40, v40, v41\n"
  "v_cvt_pk_bf16_f32 v41, v42, v43\n"
  "v_cvt_pk_bf16_f32 v42, v44, v45\n"
  "v_cvt_pk_bf16_f32 v43, v46, v47\n"
  "buffer_store_dwordx4 v[40:43], v110, s[36:39], s57 offen offset:0 sc1\n"
  "s_add_u32 s51, s51, 0x200000\n"
  "s_xor_b32 s51, s51, 8192\n"
  "s_bitset0_b32 s51, 26\n"
  "s_waitcnt vmcnt(4)\n"
  "v_add_u32 v114, v92, v122\n"
  "v_cmp_gt_u32 vcc, s63, v114\n"
  "s_cbranch_vccz .Lq_b1b\n"
  "s_waitcnt vmcnt(2)\n"
  "v_add_u32 v114, v115, v122\n"
  "v_cmp_gt_u32 vcc, s63, v114\n"
  "s_cbranch_vccz .Lq_b1b\n"
  "s_waitcnt vmcnt(0)\n"
  "buffer_store_dword v116, v113, s[36:39], 0 offen offset:512 sc1\n"
  "s_cmp_lt_u32 s54, 64\n"
  "s_cbranch_scc1 .Lq_b1\n"
  "s_branch .Lq_end\n"
  ".Lq_w0:\n"
  "s_add_u32 s56, s66, 3\n"
  "s_cmp_gt_u32 s54, s56\n"
  "s_cbranch_scc0 .Lq_ns\n"
  "s_sleep 7\n"
  ".Lq_ns:\n"
  "v_mfma_f32_16x16x32_bf16 v[28:31], v[60:63], a[196:199], v[28:31]\n"
  "ds_read_b128 v[60:63], v105 offset:16640\n"
  "s_waitcnt lgkmcnt(7)\n"
  "v_mfma_f32_16x16x32_bf16 v[0:3], a[8:11], v[64:67], v[0:3]\n"
  "s_mov_b32 m0, s61\n"
  "s_nop 0\n"
  "buffer_load_dwordx4 v80, s[36:39], s58 offen offset:0 sc1 lds\n"
  "v_mfma_f32_16x16x32_bf16 v[16:19], v[64:67], a[136:139], v[16:19]\n"
  "v_mfma_f32_16x16x32_bf16 v[4:7], a[72:75], v[64:67], v[4:7]\n"
  "v_mfma_f32_16x16x32_bf16 v[24:27], v[64:67], a[200:203], v[24:27]\n"
  "ds_read_b128 v[64:67], v106 offset:256\n"
  "s_waitcnt lgkmcnt(7)\n"
  "v_mfma_f32_16x16x32_bf16 v[8:11], a[8:11], v[68:71], v[8:11]\n"
  "buffer_load_dwordx4 v81, s[36:39], s58 offen offset:1024 sc1 lds\n"
  "v_mfma_f32_16x16x32_bf16 v[20:23], v[68:71], a[136:139], v[20:23]\n"
  "v_mfma_f32_16x16x32_bf16 v[12:15], a[72:75], v[68:71], v[12:15]\n"
  "v_mfma_f32_16x16x32_bf16 v[28:31], v[68:71], a[200:203], v[28:31]\n"
  "ds_read_b128 v[68:71], v106 offset:16640\n"
  "s_waitcnt lgkmcnt(7)\n"
  "v_mfma_f32_16x16x32_bf16 v[0:3], a[12:15], v[72:75], v[0:3]\n"
  "buffer_load_dwordx4 v82, s[36:39], s58 offen offset:2048 sc1 lds\n"
  "v_mfma_f32_16x16x32_bf16 v[16:19], v[72:75], a[140:143], v[16:19]\n"
  "v_mfma_f32_16x16x32_bf16 v[4:7], a[76:79], v[72:75], v[4:7]\n"
  "v_mfma_f32_16x16x32_bf16 v[24:27], v[72:75], a[204:207], v[24:27]\n"
  "ds_read_b128 v[72:75], v107 offset:256\n"
  "s_waitcnt lgkmcnt(7)\n"
  "v_mfma_f32_16x16x32_bf16 v[8:11], a[12:15], v[76:79], v[8:11]\n"
  "buffer_load_dwordx4 v83, s[36:39], s58 offen offset:3072 sc1 lds\n"
  "s_add_u32 m0, s61, 4096\n"
  "s_nop 0\n"
  "v_mfma_f32_16x16x32_bf16 v[20:23], v[76:79], a[140:143], v[20:23]\n"
  "v_mfma_f32_16x16x32_bf16 v[12:15], a[76:79], v[76:79], v[12:15]\n"
  "v_mfma_f32_16x16x32_bf16 v[28:31], v[76:79], a[204:207], v[28:31]\n"
  "ds_read_b128 v[76:79], v107 offset:16640\n"
  "s_waitcnt lgkmcnt(7)\n"
  "v_mfma_f32_16x16x32_bf16 v[0:3], a[16:19], v[48:51], v[0:3]\n"
  "buffer_load_dwordx4 v84, s[36:39], s59 offen offset:0 sc1 lds\n"
  "v_mfma_f32_16x16x32_bf16 v[16:19], v[48:51], a[144:147], v[16:19]\n"
  "v_mfma_f32_16x16x32_bf16 v[4:7], a[80:83], v[48:51], v[4:7]\n"
  "v_mfma_f32_16x16x32_bf16 v[24:27], v[48:51], a[208:211], v[24:27]\n"
  "ds_read_b128 v[48:51], v104 offset:512\n"
  "s_waitcnt lgkmcnt(7)\n"
  "v_mfma_f32_16x16x32_bf16 v[8:11], a[16:19], v[52:55], v[8:11]\n"
  "buffer_load_dwordx4 v85, s[36:39], s59 offen offset:1024 sc1 lds\n"
  "v_mfma_f32_16x16x32_bf16 v[20:23], v[52:55], a[144:147], v[20:23]\n"
  "v_mfma_f32_16x16x32_bf16 v[12:15], a[80:83], v[52:55], v[12:15]\n"
  "v_mfma_f32_16x16x32_bf16 v[28:31], v[52:55], a[208:211], v[28:31]\n"
  "ds_read_b128 v[52:55], v104 offset:16896\n"
  "s_waitcnt lgkmcnt(7)\n"
  "v_mfma_f32_16x16x32_bf16 v[0:3], a[20:23], v[56:59], v[0:3]\n"
  "buffer_load_dwordx4 v86, s[36:39], s59 offen offset:2048 sc1 lds\n"
  "v_mfma_f32_16x16x32_bf16 v[16:19], v[56:59], a[148:151], v[16:19]\n"
  "v_mfma_f32_16x16x32_bf16 v[4:7], a[84:87], v[56:59], v[4:7]\n"
  "v_mfma_f32_16x16x32_bf16 v[24:27], v[56:59], a[212:215], v[24:27]\n"
  "ds_read_b128 v[56:59], v105 offset:512\n"
  "s_waitcnt lgkmcnt(7)\n"
  "v_mfma_f32_16x16x32_bf16 v[8:11], a[20:23], v[60:63], v[8:11]\n"
  "buffer_load_dwordx4 v87, s[36:39], s59 offen offset:3072 sc1 lds\n"
  "v_mfma_f32_16x16x32_bf16 v[20:23], v[60:63], a[148:151], v[20:23]\n"
  "v_mfma_f32_16x16x32_bf16 v[12:15], a[84:87], v[60:63], v[12:15]\n"
  "v_mfma_f32_16x16x32_bf16 v[28:31], v[60:63], a[212:215], v[28:31]\n"
  "ds_read_b128 v[60:63], v105 offset:16896\n"
  "s_waitcnt lgkmcnt(7)\n"
  "v_mfma_f32_16x16x32_bf16 v[0:3], a[24:27], v[64:67], v[0:3]\n"
  "v_mfma_f32_16x16x32_bf16 v[16:19], v[64:67], a[152:155], v[16:19]\n"
  "v_mfma_f32_16x16x32_bf16 v[4:7], a[88:91], v[64:67], v[4:7]\n"
  "v_mfma_f32_16x16x32_bf16 v[24:27], v[64:67], a[216:219], v[24:27]\n"
  "ds_read_b128 v[64:67], v106 offset:512\n"
  "s_waitcnt lgkmcnt(7)\n"
  "v_mfma_f32_16x16x32_bf16 v[8:11], a[24:27], v[68:71], v[8:11]\n"
  "v_mfma_f32_16x16x32_bf16 v[20:23], v[68:71], a[152:155], v[20:23]\n"
  "v_mfma_f32_16x16x32_bf16 v[12:15], a[88:91], v[68:71], v[12:15]\n"
  "v_mov_b32 v116, s54\n"
  "v_mfma_f32_16x16x32_bf16 v[28:31], v[68:71], a[216:219], v[28:31]\n"
  "ds_read_b128 v[68:71], v106 offset:16896\n"
  "s_waitcnt lgkmcnt(7)\n"
  "v_mfma_f32_16x16x32_bf16 v[0:3], a[28:31], v[72:75], v[0:3]\n"
  "s_waitcnt vmcnt(8)\n"
  "buffer_store_dword v116, v113, s[36:39], 0 offen offset:512 sc1\n"
  "v_mfma_f32_16x16x32_bf16 v[16:19], v[72:75], a[156:159], v[16:19]\n"
  "v_mfma_f32_16x16x32_bf16 v[4:7], a[92:95], v[72:75], v[4:7]\n"
  "v_mfma_f32_16x16x32_bf16 v[24:27], v[72:75], a[220:223], v[24:27]\n"
  "ds_read_b128 v[72:75], v107 offset:512\n"
  "s_waitcnt lgkmcnt(7)\n"
  "v_mfma_f32_16x16x32_bf16 v[8:11], a[28:31], v[76:79], v[8:11]\n"
  "s_add_u32 s56, s51, 16384\n"
  "s_add_u32 s57, s51, 32768\n"
  "v_mfma_f32_16x16x32_bf16 v[20:23], v[76:79], a[156:159], v[20:23]\n"
  "v_mfma_f32_16x16x32_bf16 v[12:15], a[92:95], v[76:79], v[12:15]\n"
  "v_mfma_f32_16x16x32_bf16 v[28:31], v[76:79], a[220:223], v[28:31]\n"
  "ds_read_b128 v[76:79], v107 offset:16896\n"
  "s_waitcnt lgkmcnt(7)\n"
  "v_mfma_f32_16x16x32_bf16 v[0:3], a[32:35], v[48:51], v[0:3]\n"
  "v_mfma_f32_16x16x32_bf16 v[16:19], v[48:51], a[160:163], v[16:19]\n"
  "v_mfma_f32_16x16x32_bf16 v[4:7], a[96:99], v[48:51], v[4:7]\n"
  "v_mfma_f32_16x16x32_bf16 v[24:27], v[48:51], a[224:227], v[24:27]\n"
  "ds_read_b128 v[48:51], v104 offset:768\n"
  "s_waitcnt lgkmcnt(7)\n"
  "v_mfma_f32_16x16x32_bf16 v[8:11], a[32:35], v[52:55], v[8:11]\n"
  "v_mfma_f32_16x16x32_bf16 v[20:23], v[52:55], a[160:163], v[20:23]\n"
  "v_mfma_f32_16x16x32_bf16 v[12:15], a[96:99], v[52:55], v[12:15]\n"
  "v_mfma_f32_16x16x32_bf16 v[28:31], v[52:55], a[224:227], v[28:31]\n"
  "ds_read_b128 v[52:55], v104 offset:17152\n"
  "s_waitcnt lgkmcnt(7)\n"
  "v_mfma_f32_16x16x32_bf16 v[0:3], a[36:39], v[56:59], v[0:3]\n"
  "v_mfma_f32_16x16x32_bf16 v[16:19], v[56:59], a[164:167], v[16:19]\n"
  "v_mfma_f32_16x16x32_bf16 v[4:7], a[100:103], v[56:59], v[4:7]\n"
  "v_mfma_f32_16x16x32_bf16 v[24:27], v[56:59], a[228:231], v[24:27]\n"
  "ds_read_b128 v[56:59], v105 offset:768\n"
  "s_waitcnt lgkmcnt(7)\n"
  "v_mfma_f32_16x16x32_bf16 v[8:11], a[36:39], v[60:63], v[8:11]\n"
  "v_mfma_f32_16x16x32_bf16 v[20:23], v[60:63], a[164:167], v[20:23]\n"
  "v_mfma_f32_16x16x32_bf16 v[12:15], a[100:103], v[60:63], v[12:15]\n"
  "v_mfma_f32_16x16x32_bf16 v[28:31], v[60:63], a[228:231], v[28:31]\n"
  "ds_read_b128 v[60:63], v105 offset:17152\n"
  "s_waitcnt lgkmcnt(7)\n"
  "v_mfma_f32_16x16x32_bf16 v[0:3], a[40:43], v[64:67], v[0:3]\n"
  "v_mfma_f32_16x16x32_bf16 v[16:19], v[64:67], a[168:171], v[16:19]\n"
  "v_mfma_f32_16x16x32_bf16 v[4:7], a[104:107], v[64:67], v[4:7]\n"
  "v_mfma_f32_16x16x32_bf16 v[24:27], v[64:67], a[232:235], v[24:27]\n"
  "ds_read_b128 v[64:67], v106 offset:768\n"
  "s_waitcnt lgkmcnt(7)\n"
  "v_mfma_f32_16x16x32_bf16 v[8:11], a[40:43], v[68:71], v[8:11]\n"
  "v_mfma_f32_16x16x32_bf16 v[20:23], v[68:71], a[168:171], v[20:23]\n"
  "v_mfma_f32_16x16x32_bf16 v[12:15], a[104:107], v[68:71], v[12:15]\n"
  "v_mfma_f32_16x16x32_bf16 v[28:31], v[68:71], a[232:235], v[28:31]\n"
  "ds_read_b128 v[68:71], v106 offset:17152\n"
  "s_waitcnt lgkmcnt(7)\n"
  "v_mfma_f32_16x16x32_bf16 v[0:3], a[44:47], v[72:75], v[0:3]\n"
  "v_mfma_f32_16x16x32_bf16 v[16:19], v[72:75], a[172:175], v[16:19]\n"
  "v_mfma_f32_16x16x32_bf16 v[4:7], a[108:111], v[72:75], v[4:7]\n"
  "v_mfma_f32_16x16x32_bf16 v[24:27], v[72:75], a[236:239], v[24:27]\n"
  "ds_read_b128 v[72:75], v107 offset:768\n"
  "s_waitcnt lgkmcnt(7)\n"
  "v_mfma_f32_16x16x32_bf16 v[8:11], a[44:47], v[76:79], v[8:11]\n"
  "v_mfma_f32_16x16x32_bf16 v[20:23], v[76:79], a[172:175], v[20:23]\n"
  "v_mfma_f32_16x16x32_bf16 v[12:15], a[108:111], v[76:79], v[12:15]\n"
  "v_mfma_f32_16x16x32_bf16 v[28:31], v[76:79], a[236:239], v[28:31]\n"
  "ds_read_b128 v[76:79], v107 offset:17152\n"
  "s_waitcnt lgkmcnt(7)\n"
  "v_mfma_f32_16x16x32_bf16 v[0:3], a[48:51], v[48:51], v[0:3]\n"
  "v_mfma_f32_16x16x32_bf16 v[16:19], v[48:51], a[176:179], v[16:19]\n"
  "v_mfma_f32_16x16x32_bf16 v[4:7], a[112:115], v[48:51], v[4:7]\n"
  "v_mfma_f32_16x16x32_bf16 v[24:27], v[48:51], a[240:243], v[24:27]\n"
  "ds_read_b128 v[48:51], v104 offset:0\n"
  "s_waitcnt lgkmcnt(7)\n"
  "v_mfma_f32_16x16x32_bf16 v[8:11], a[48:51], v[52:55], v[8:11]\n"
  "v_mfma_f32_16x16x32_bf16 v[20:23], v[52:55], a[176:179], v[20:23]\n"
  "v_mfma_f32_16x16x32_bf16 v[12:15], a[112:115], v[52:55], v[12:15]\n"
  "v_mfma_f32_16x16x32_bf16 v[28:31], v[52:55], a[240:243], v[28:31]\n"
  "ds_read_b128 v[52:55], v104 offset:16384\n"
  "s_waitcnt lgkmcnt(7)\n"
  "v_mfma_f32_16x16x32_bf16 v[0:3], a[52:55], v[56:59], v[0:3]\n"
  "v_mfma_f32_16x16x32_bf16 v[16:19], v[56:59], a[180:183], v[16:19]\n"
  "v_mfma_f32_16x16x32_bf16 v[4:7], a[116:119], v[56:59], v[4:7]\n"
  "v_mfma_f32_16x16x32_bf16 v[24:27], v[56:59], a[244:247], v[24:27]\n"
  "ds_read_b128 v[56:59], v105 offset:0\n"
  "s_waitcnt lgkmcnt(7)\n"
  "v_mfma_f32_16x16x32_bf16 v[8:11], a[52:55], v[60:63], v[8:11]\n"
  "v_mfma_f32_16x16x32_bf16 v[20:23], v[60:63], a[180:183], v[20:23]\n"
  "v_mfma_f32_16x16x32_bf16 v[12:15], a[116:119], v[60:63], v[12:15]\n"
  "v_mfma_f32_16x16x32_bf16 v[28:31], v[60:63], a[244:247], v[28:31]\n"
  "ds_read_b128 v[60:63], v105 offset:16384\n"
  "s_waitcnt lgkmcnt(7)\n"
  "v_mfma_f32_16x16x32_bf16 v[0:3], a[56:59], v[64:67], v[0:3]\n"
  "v_mfma_f32_16x16x32_bf16 v[16:19], v[64:67], a[184:187], v[16:19]\n"
  "v_mfma_f32_16x16x32_bf16 v[4:7], a[120:123], v[64:67], v[4:7]\n"
  "v_mfma_f32_16x16x32_bf16 v[24:27], v[64:67], a[248:251], v[24:27]\n"
  "ds_read_b128 v[64:67], v106 offset:0\n"
  "s_waitcnt lgkmcnt(7)\n"
  "v_mfma_f32_16x16x32_bf16 v[8:11], a[56:59], v[68:71], v[8:11]\n"
  "v_mfma_f32_16x16x32_bf16 v[20:23], v[68:71], a[184:187], v[20:23]\n"
  "v_mfma_f32_16x16x32_bf16 v[12:15], a[120:123], v[68:71], v[12:15]\n"
  "v_cmp_ge_u32 s[48:49], s63, v114\n"
  "v_mfma_f32_16x16x32_bf16 v[28:31], v[68:71], a[248:251], v[28:31]\n"
  "ds_read_b128 v[68:71], v106 offset:16384\n"
  "s_waitcnt lgkmcnt(7)\n"
  "v_mfma_f32_16x16x32_bf16 v[0:3], a[60:63], v[72:75], v[0:3]\n"
  "v_mfma_f32_16x16x32_bf16 v[16:19], v[72:75], a[188:191], v[16:19]\n"
  "v_mfma_f32_16x16x32_bf16 v[4:7], a[124:127], v[72:75], v[4:7]\n"
  "s_cmp_eq_u64 s[48:49], 0\n"
  "s_cselect_b32 s60, 0x80000000, 0\n"
  "v_mfma_f32_16x16x32_bf16 v[24:27], v[72:75], a[252:255], v[24:27]\n"
  "ds_read_b128 v[72:75], v107 offset:0\n"
  ".Lq_j1:\n"
  "s_waitcnt lgkmcnt(7)\n"
  "v_mfma_f32_16x16x32_bf16 v[8:11], a[60:63], v[76:79], v[8:11]\n"
  "v_mfma_f32_16x16x32_bf16 v[20:23], v[76:79], a[188:191], v[20:23]\n"
  "v_cvt_pk_bf16_f32 v0, v0, v1\n"
  "v_cvt_pk_bf16_f32 v1, v2, v3\n"
  "v_mfma_f32_16x16x32_bf16 v[12:15], a[124:127], v[76:79], v[12:15]\n"
  "v_cvt_pk_bf16_f32 v2, v4, v5\n"
  "v_cvt_pk_bf16_f32 v3, v6, v7\n"
  "s_cmp_lg_u32 s68, 0\n"
  "v_mfma_f32_16x16x32_bf16 v[28:31], v[76:79], a[252:255], v[28:31]\n"
  "ds_read_b128 v[76:79], v107 offset:16384\n"
  "buffer_store_dwordx4 v[0:3], v109, s[36:39], s56 offen offset:0 sc1\n"
  "s_cbranch_scc1 .Lq_first\n"
  "s_waitcnt lgkmcnt(7)\n"
  "v_mfma_f32_16x16x32_bf16 v[32:35], v[128:131], v[48:51], 0\n"
  "v_cvt_pk_bf16_f32 v16, v16, v17\n"
  "v_cvt_pk_bf16_f32 v17, v18, v19\n"
  "v_mfma_f32_16x16x32_bf16 v[36:39], v[192:195], v[48:51], 0\n"
  "ds_read_b128 v[48:51], v104 offset:256\n"
  "v_cvt_pk_bf16_f32 v18, v20, v21\n"
  "v_cvt_pk_bf16_f32 v19, v22, v23\n"
  "s_waitcnt lgkmcnt(7)\n"
  "v_mfma_f32_16x16x32_bf16 v[40:43], v[128:131], v[52:55], 0\n"
  "buffer_store_dwordx4 v[16:19], v109, s[36:39], s57 offen offset:0 sc1\n"
  "v_cvt_pk_bf16_f32 v8, v8, v9\n"
  "v_cvt_pk_bf16_f32 v9, v10, v11\n"
  "v_mfma_f32_16x16x32_bf16 v[44:47], v[192:195], v[52:55], 0\n"
  "ds_read_b128 v[52:55], v104 offset:16640\n"
  "v_cvt_pk_bf16_f32 v10, v12, v13\n"
  "v_cvt_pk_bf16_f32 v11, v14, v15\n"
  "s_waitcnt lgkmcnt(7)\n"
  "v_mfma_f32_16x16x32_bf16 v[32:35], v[132:135], v[56:59], v[32:35]\n"
  "buffer_store_dwordx4 v[8:11], v109, s[36:39], s56 offen offset:1024 sc1\n"
  "v_cvt_pk_bf16_f32 v24, v24, v25\n"
  "v_cvt_pk_bf16_f32 v25, v26, v27\n"
  "v_mfma_f32_16x16x32_bf16 v[36:39], v[196:199], v[56:59], v[36:39]\n"
  "ds_read_b128 v[56:59], v105 offset:256\n"
  "v_cvt_pk_bf16_f32 v26, v28, v29\n"
  "v_cvt_pk_bf16_f32 v27, v30, v31\n"
  "s_waitcnt lgkmcnt(7)\n"
  "v_mfma_f32_16x16x32_bf16 v[40:43], v[132:135], v[60:63], v[40:43]\n"
  "buffer_store_dwordx4 v[24:27], v109, s[36:39], s57 offen offset:1024 sc1\n"
  "v_mfma_f32_16x16x32_bf16 v[44:47], v[196:199], v[60:63], v[44:47]\n"
  "ds_read_b128 v[60:63], v105 offset:16640\n"
  "s_waitcnt lgkmcnt(7)\n"
  "v_mfma_f32_16x16x32_bf16 v[32:35], v[136:139], v[64:67], v[32:35]\n"
  "v_mfma_f32_16x16x32_bf16 v[36:39], v[200:203], v[64:67], v[36:39]\n"
  "ds_read_b128 v[64:67], v106 offset:256\n"
  "s_waitcnt lgkmcnt(7)\n"
  "v_mfma_f32_16x16x32_bf16 v[40:43], v[136:139], v[68:71], v[40:43]\n"
  "v_mfma_f32_16x16x32_bf16 v[44:47], v[200:203], v[68:71], v[44:47]\n"
  "ds_read_b128 v[68:71], v106 offset:16640\n"
  "s_waitcnt lgkmcnt(7)\n"
  "v_mfma_f32_16x16x32_bf16 v[32:35], v[140:143], v[72:75], v[32:35]\n"
  "v_mfma_f32_16x16x32_bf16 v[36:39], v[204:207], v[72:75], v[36:39]\n"
  "ds_read_b128 v[72:75], v107 offset:256\n"
  "s_waitcnt lgkmcnt(7)\n"
  "v_mfma_f32_16x16x32_bf16 v[40:43], v[140:143], v[76:79], v[40:43]\n"
  "v_mfma_f32_16x16x32_bf16 v[44:47], v[204:207], v[76:79], v[44:47]\n"
  "ds_read_b128 v[76:79], v107 offset:16640\n"
  "s_waitcnt lgkmcnt(7)\n"
  "v_mfma_f32_16x16x32_bf16 v[32:35], v[144:147], v[48:51], v[32:35]\n"
  "v_mfma_f32_16x16x32_bf16 v[36:39], v[208:211], v[48:51], v[36:39]\n"
  "ds_read_b128 v[48:51], v104 offset:512\n"
  "s_waitcnt lgkmcnt(7)\n"
  "v_mfma_f32_16x16x32_bf16 v[40:43], v[144:147], v[52:55], v[40:43]\n"
  "v_mfma_f32_16x16x32_bf16 v[44:47], v[208:211], v[52:55], v[44:47]\n"
  "ds_read_b128 v[52:55], v104 offset:16896\n"
  "s_waitcnt lgkmcnt(7)\n"
  "v_mfma_f32_16x16x32_bf16 v[32:35], v[148:151], v[56:59], v[32:35]\n"
  "v_mfma_f32_16x16x32_bf16 v[36:39], v[212:215], v[56:59], v[36:39]\n"
  "ds_read_b128 v[56:59], v105 offset:512\n"
  "s_waitcnt lgkmcnt(7)\n"
  "v_mfma_f32_16x16x32_bf16 v[40:43], v[148:151], v[60:63], v[40:43]\n"
  "buffer_load_dword v115, v112, s[36:39], s60 offen sc1\n"
  "v_mfma_f32_16x16x32_bf16 v[44:47], v[212:215], v[60:63], v[44:47]\n"
  "ds_read_b128 v[60:63], v105 offset:16896\n"
  "s_waitcnt lgkmcnt(7)\n"
  "v_mfma_f32_16x16x32_bf16 v[32:35], v[152:155], v[64:67], v[32:35]\n"
  "v_mfma_f32_16x16x32_bf16 v[36:39], v[216:219], v[64:67], v[36:39]\n"
  "ds_read_b128 v[64:67], v106 offset:512\n"
  "s_waitcnt lgkmcnt(7)\n"
  "v_mfma_f32_16x16x32_bf16 v[40:43], v[152:155], v[68:71], v[40:43]\n"
  "v_mfma_f32_16x16x32_bf16 v[44:47], v[216:219], v[68:71], v[44:47]\n"
  "ds_read_b128 v[68:71], v106 offset:16896\n"
  "s_waitcnt lgkmcnt(7)\n"
  "v_mfma_f32_16x16x32_bf16 v[32:35], v[156:159], v[72:75], v[32:35]\n"
  "v_mfma_f32_16x16x32_bf16 v[36:39], v[220:223], v[72:75], v[36:39]\n"
  "ds_read_b128 v[72:75], v107 offset:512\n"
  "s_waitcnt lgkmcnt(7)\n"
  "v_mfma_f32_16x16x32_bf16 v[40:43], v[156:159], v[76:79], v[40:43]\n"
  "v_mfma_f32_16x16x32_bf16 v[44:47], v[220:223], v[76:79], v[44:47]\n"
  "ds_read_b128 v[76:79], v107 offset:16896\n"
  "s_waitcnt lgkmcnt(7)\n"
  "v_mfma_f32_16x16x32_bf16 v[32:35], v[160:163], v[48:51], v[32:35]\n"
  "v_mfma_f32_16x16x32_bf16 v[36:39], v[224:227], v[48:51], v[36:39]\n"
  "ds_read_b128 v[48:51], v104 offset:768\n"
  "s_waitcnt lgkmcnt(7)\n"
  "v_mfma_f32_16x16x32_bf16 v[40:43], v[160:163], v[52:55], v[40:43]\n"
  "v_mfma_f32_16x16x32_bf16 v[44:47], v[224:227], v[52:55], v[44:47]\n"
  "ds_read_b128 v[52:55], v104 offset:17152\n"
  "s_waitcnt lgkmcnt(7)\n"
  "v_mfma_f32_16x16x32_bf16 v[32:35], v[164:167], v[56:59], v[32:35]\n"
  "v_xor_b32 v104, 0x8000, v104\n"
  "v_mfma_f32_16x16x32_bf16 v[36:39], v[228:231], v[56:59], v[36:39]\n"
  "ds_read_b128 v[56:59], v105 offset:768\n"
  "s_waitcnt lgkmcnt(7)\n"
  "v_mfma_f32_16x16x32_bf16 v[40:43], v[164:167], v[60:63], v[40:43]\n"
  "v_mfma_f32_16x16x32_bf16 v[44:47], v[228:231], v[60:63], v[44:47]\n"
  "ds_read_b128 v[60:63], v105 offset:17152\n"
  "s_waitcnt lgkmcnt(7)\n"
  "v_mfma_f32_16x16x32_bf16 v[32:35], v[168:171], v[64:67], v[32:35]\n"
  "v_xor_b32 v105, 0x8000, v105\n"
  "v_mfma_f32_16x16x32_bf16 v[36:39], v[232:235], v[64:67], v[36:39]\n"
  "ds_read_b128 v[64:67], v106 offset:768\n"
  "s_waitcnt lgkmcnt(7)\n"
  "v_mfma_f32_16x16x32_bf16 v[40:43], v[168:171], v[68:71], v[40:43]\n"
  "v_mfma_f32_16x16x32_bf16 v[44:47], v[232:235], v[68:71], v[44:47]\n"
  "ds_read_b128 v[68:71], v106 offset:17152\n"
  "s_add_u32 s54, s54, 1\n"
  "s_add_u32 s57, s51, 4096\n"
  "v_mov_b32 v116, s54\n"
  "s_waitcnt lgkmcnt(7)\n"
  "v_mfma_f32_16x16x32_bf16 v[32:35], v[172:175], v[72:75], v[32:35]\n"
  "v_xor_b32 v106, 0x8000, v106\n"
  "s_waitcnt vmcnt(1)\n"
  "buffer_store_dword v116, v113, s[36:39], 0 offen offset:0 sc1\n"
  "v_mfma_f32_16x16x32_bf16 v[36:39], v[236:239], v[72:75], v[36:39]\n"
  "ds_read_b128 v[72:75], v107 offset:768\n"
  "s_waitcnt lgkmcnt(7)\n"
  "v_mfma_f32_16x16x32_bf16 v[40:43], v[172:175], v[76:79], v[40:43]\n"
  "v_mfma_f32_16x16x32_bf16 v[44:47], v[236:239], v[76:79], v[44:47]\n"
  "ds_read_b128 v[76:79], v107 offset:17152\n"
  "s_waitcnt lgkmcnt(7)\n"
  "v_mfma_f32_16x16x32_bf16 v[32:35], v[176:179], v[48:51], v[32:35]\n"
  "v_xor_b32 v107, 0x8000, v107\n"
  "v_mfma_f32_16x16x32_bf16 v[36:39], v[240:243], v[48:51], v[36:39]\n"
  "s_waitcnt lgkmcnt(6)\n"
  "v_mfma_f32_16x16x32_bf16 v[40:43], v[176:179], v[52:55], v[40:43]\n"
  "s_mov_b32 s78, s51\n"
  "s_mov_b32 s79, s57\n"
  "v_mfma_f32_16x16x32_bf16 v[44:47], v[240:243], v[52:55], v[44:47]\n"
  "s_waitcnt lgkmcnt(5)\n"
  "v_mfma_f32_16x16x32_bf16 v[32:35], v[180:183], v[56:59], v[32:35]\n"
  "s_add_u32 s51, s51, 0x200000\n"
  "s_xor_b32 s51, s51, 8192\n"
  "s_bitset0_b32 s51, 26\n"
  "v_mfma_f32_16x16x32_bf16 v[36:39], v[244:247], v[56:59], v[36:39]\n"
  "s_waitcnt lgkmcnt(0)\n"
  "s_barrier\n"
  "ds_read_b128 v[48:51], v104 offset:0\n"
  "v_mfma_f32_16x16x32_bf16 v[40:43], v[180:183], v[60:63], v[40:43]\n"
  "ds_read_b128 v[52:55], v104 offset:16384\n"
  "v_mfma_f32_16x16x32_bf16 v[44:47], v[244:247], v[60:63], v[44:47]\n"
  "ds_read_b128 v[56:59], v105 offset:0\n"
  "v_mfma_f32_16x16x32_bf16 v[32:35], v[184:187], v[64:67], v[32:35]\n"
  "ds_read_b128 v[60:63], v105 offset:16384\n"
  "v_mfma_f32_16x16x32_bf16 v[36:39], v[248:251], v[64:67], v[36:39]\n"
  "ds_read_b128 v[64:67], v106 offset:0\n"
  "v_mfma_f32_16x16x32_bf16 v[40:43], v[184:187], v[68:71], v[40:43]\n"
  "v_mfma_f32_16x16x32_bf16 v[44:47], v[248:251], v[68:71], v[44:47]\n"
  "ds_read_b128 v[68:71], v106 offset:16384\n"
  "v_mfma_f32_16x16x32_bf16 v[32:35], v[188:191], v[72:75], v[32:35]\n"
  "v_mfma_f32_16x16x32_bf16 v[36:39], v[252:255], v[72:75], v[36:39]\n"
  "ds_read_b128 v[72:75], v107 offset:0\n"
  "v_mfma_f32_16x16x32_bf16 v[40:43], v[188:191], v[76:79], v[40:43]\n"
  "s_waitcnt vmcnt(1)\n"
  "v_add_u32 v115, v115, v122\n"
  "v_max_u32 v114, v114, v115\n"
  "v_mfma_f32_16x16x32_bf16 v[44:47], v[252:255], v[76:79], v[44:47]\n"
  "ds_read_b128 v[76:79], v107 offset:16384\n"
  "s_branch .Lq_seg\n"
  ".Lq_w1:\n"
  "v_mfma_f32_16x16x32_bf16 v[12:15], a[112:115], v[52:55], v[12:15]\n"
  "s_mov_b32 m0, s61\n"
  "s_nop 0\n"
  "buffer_load_dwordx4 v80, s[36:39], s58 offen offset:0 sc1 lds\n"
  "v_mfma_f32_16x16x32_bf16 v[28:31], v[52:55], a[240:243], v[28:31]\n"
  "ds_read_b128 v[52:55], v104 offset:16384\n"
  "s_waitcnt lgkmcnt(7)\n"
  "v_mfma_f32_16x16x32_bf16 v[0:3], a[52:55], v[56:59], v[0:3]\n"
  "buffer_load_dwordx4 v81, s[36:39], s58 offen offset:1024 sc1 lds\n"
  "v_mfma_f32_16x16x32_bf16 v[16:19], v[56:59], a[180:183], v[16:19]\n"
  "v_mfma_f32_16x16x32_bf16 v[4:7], a[116:119], v[56:59], v[4:7]\n"
  "buffer_load_dwordx4 v82, s[36:39], s58 offen offset:2048 sc1 lds\n"
  "v_mfma_f32_16x16x32_bf16 v[24:27], v[56:59], a[244:247], v[24:27]\n"
  "ds_read_b128 v[56:59], v105 offset:0\n"
  "s_waitcnt lgkmcnt(7)\n"
  "v_mfma_f32_16x16x32_bf16 v[8:11], a[52:55], v[60:63], v[8:11]\n"
  "v_mfma_f32_16x16x32_bf16 v[20:23], v[60:63], a[180:183], v[20:23]\n"
  "buffer_load_dwordx4 v83, s[36:39], s58 offen offset:3072 sc1 lds\n"
  "s_add_u32 m0, s61, 4096\n"
  "s_nop 0\n"
  "v_mfma_f32_16x16x32_bf16 v[12:15], a[116:119], v[60:63], v[12:15]\n"
  "v_mfma_f32_16x16x32_bf16 v[28:31], v[60:63], a[244:247], v[28:31]\n"
  "ds_read_b128 v[60:63], v105 offset:16384\n"
  "s_waitcnt lgkmcnt(7)\n"
  "v_mfma_f32_16x16x32_bf16 v[0:3], a[56:59], v[64:67], v[0:3]\n"
  "buffer_load_dwordx4 v84, s[36:39], s59 offen offset:0 sc1 lds\n"
  "v_mfma_f32_16x16x32_bf16 v[16:19], v[64:67], a[184:187], v[16:19]\n"
  "buffer_load_dwordx4 v85, s[36:39], s59 offen offset:1024 sc1 lds\n"
  "v_mfma_f32_16x16x32_bf16 v[4:7], a[120:123], v[64:67], v[4:7]\n"
  "v_mfma_f32_16x16x32_bf16 v[24:27], v[64:67], a[248:251], v[24:27]\n"
  "ds_read_b128 v[64:67], v106 offset:0\n"
  "s_waitcnt lgkmcnt(7)\n"
  "v_mfma_f32_16x16x32_bf16 v[8:11], a[56:59], v[68:71], v[8:11]\n"
  "buffer_load_dwordx4 v86, s[36:39], s59 offen offset:2048 sc1 lds\n"
  "v_mfma_f32_16x16x32_bf16 v[20:23], v[68:71], a[184:187], v[20:23]\n"
  "v_mfma_f32_16x16x32_bf16 v[12:15], a[120:123], v[68:71], v[12:15]\n"
  "v_cmp_ge_u32 s[48:49], s63, v114\n"
  "v_mfma_f32_16x16x32_bf16 v[28:31], v[68:71], a[248:251], v[28:31]\n"
  "ds_read_b128 v[68:71], v106 offset:16384\n"
  "s_waitcnt lgkmcnt(7)\n"
  "v_mfma_f32_16x16x32_bf16 v[0:3], a[60:63], v[72:75], v[0:3]\n"
  "buffer_load_dwordx4 v87, s[36:39], s59 offen offset:3072 sc1 lds\n"
  "v_mfma_f32_16x16x32_bf16 v[16:19], v[72:75], a[188:191], v[16:19]\n"
  "v_mfma_f32_16x16x32_bf16 v[4:7], a[124:127], v[72:75], v[4:7]\n"
  "s_cmp_eq_u64 s[48:49], 0\n"
  "s_cselect_b32 s60, 0x80000000, 0\n"
  "v_mfma_f32_16x16x32_bf16 v[24:27], v[72:75], a[252:255], v[24:27]\n"
  "ds_read_b128 v[72:75], v107 offset:0\n"
  "s_branch .Lq_j1\n"
  ".Lq_w2:\n"
  "s_mov_b32 m0, s61\n"
  "s_nop 0\n"
  "buffer_load_dwordx4 v80, s[36:39], s58 offen offset:0 sc1 lds\n"
  "v_mfma_f32_16x16x32_bf16 v[44:47], v[212:215], v[60:63], v[44:47]\n"
  "ds_read_b128 v[60:63], v105 offset:16896\n"
  "buffer_load_dwordx4 v81, s[36:39], s58 offen offset:1024 sc1 lds\n"
  "s_waitcnt lgkmcnt(7)\n"
  "v_mfma_f32_16x16x32_bf16 v[32:35], v[152:155], v[64:67], v[32:35]\n"
  "buffer_load_dwordx4 v82, s[36:39], s58 offen offset:2048 sc1 lds\n"
  "v_mfma_f32_16x16x32_bf16 v[36:39], v[216:219], v[64:67], v[36:39]\n"
  "ds_read_b128 v[64:67], v106 offset:512\n"
  "buffer_load_dwordx4 v83, s[36:39], s58 offen offset:3072 sc1 lds\n"
  "s_add_u32 m0, s61, 4096\n"
  "s_nop 0\n"
  "s_waitcnt lgkmcnt(7)\n"
  "v_mfma_f32_16x16x32_bf16 v[40:43], v[152:155], v[68:71], v[40:43]\n"
  "buffer_load_dwordx4 v84, s[36:39], s59 offen offset:0 sc1 lds\n"
  "v_mfma_f32_16x16x32_bf16 v[44:47], v[216:219], v[68:71], v[44:47]\n"
  "ds_read_b128 v[68:71], v106 offset:16896\n"
  "buffer_load_dwordx4 v85, s[36:39], s59 offen offset:1024 sc1 lds\n"
  "s_waitcnt lgkmcnt(7)\n"
  "v_mfma_f32_16x16x32_bf16 v[32:35], v[156:159], v[72:75], v[32:35]\n"
  "buffer_load_dwordx4 v86, s[36:39], s59 offen offset:2048 sc1 lds\n"
  "v_mfma_f32_16x16x32_bf16 v[36:39], v[220:223], v[72:75], v[36:39]\n"
  "ds_read_b128 v[72:75], v107 offset:512\n"
  "buffer_load_dwordx4 v87, s[36:39], s59 offen offset:3072 sc1 lds\n"
  "s_waitcnt lgkmcnt(7)\n"
  "v_mfma_f32_16x16x32_bf16 v[40:43], v[156:159], v[76:79], v[40:43]\n"
  "v_mfma_f32_16x16x32_bf16 v[44:47], v[220:223], v[76:79], v[44:47]\n"
  "ds_read_b128 v[76:79], v107 offset:16896\n"
  "s_waitcnt lgkmcnt(7)\n"
  "v_mfma_f32_16x16x32_bf16 v[32:35], v[160:163], v[48:51], v[32:35]\n"
  "v_mfma_f32_16x16x32_bf16 v[36:39], v[224:227], v[48:51], v[36:39]\n"
  "ds_read_b128 v[48:51], v104 offset:768\n"
  "s_waitcnt lgkmcnt(7)\n"
  "v_mfma_f32_16x16x32_bf16 v[40:43], v[160:163], v[52:55], v[40:43]\n"
  "v_mfma_f32_16x16x32_bf16 v[44:47], v[224:227], v[52:55], v[44:47]\n"
  "ds_read_b128 v[52:55], v104 offset:17152\n"
  "s_waitcnt lgkmcnt(7)\n"
  "v_mfma_f32_16x16x32_bf16 v[32:35], v[164:167], v[56:59], v[32:35]\n"
  "v_xor_b32 v104, 0x8000, v104\n"
  "v_mfma_f32_16x16x32_bf16 v[36:39], v[228:231], v[56:59], v[36:39]\n"
  "ds_read_b128 v[56:59], v105 offset:768\n"
  "s_waitcnt lgkmcnt(7)\n"
  "v_mfma_f32_16x16x32_bf16 v[40:43], v[164:167], v[60:63], v[40:43]\n"
  "v_mfma_f32_16x16x32_bf16 v[44:47], v[228:231], v[60:63], v[44:47]\n"
  "ds_read_b128 v[60:63], v105 offset:17152\n"
  "s_waitcnt lgkmcnt(7)\n"
  "v_mfma_f32_16x16x32_bf16 v[32:35], v[168:171], v[64:67], v[32:35]\n"
  "v_xor_b32 v105, 0x8000, v105\n"
  "v_mfma_f32_16x16x32_bf16 v[36:39], v[232:235], v[64:67], v[36:39]\n"
  "ds_read_b128 v[64:67], v106 offset:768\n"
  "s_waitcnt lgkmcnt(7)\n"
  "v_mfma_f32_16x16x32_bf16 v[40:43], v[168:171], v[68:71], v[40:43]\n"
  "v_mfma_f32_16x16x32_bf16 v[44:47], v[232:235], v[68:71], v[44:47]\n"
  "ds_read_b128 v[68:71], v106 offset:17152\n"
  "s_add_u32 s54, s54, 1\n"
  "s_add_u32 s57, s51, 4096\n"
  "v_mov_b32 v116, s54\n"
  "s_waitcnt lgkmcnt(7)\n"
  "v_mfma_f32_16x16x32_bf16 v[32:35], v[172:175], v[72:75], v[32:35]\n"
  "v_xor_b32 v106, 0x8000, v106\n"
  "s_waitcnt vmcnt(9)\n"
  "buffer_store_dword v116, v113, s[36:39], 0 offen offset:0 sc1\n"
  "v_mfma_f32_16x16x32_bf16 v[36:39], v[236:239], v[72:75], v[36:39]\n"
  "ds_read_b128 v[72:75], v107 offset:768\n"
  "s_waitcnt lgkmcnt(7)\n"
  "v_mfma_f32_16x16x32_bf16 v[40:43], v[172:175], v[76:79], v[40:43]\n"
  "v_mfma_f32_16x16x32_bf16 v[44:47], v[236:239], v[76:79], v[44:47]\n"
  "ds_read_b128 v[76:79], v107 offset:17152\n"
  "s_waitcnt lgkmcnt(7)\n"
  "v_mfma_f32_16x16x32_bf16 v[32:35], v[176:179], v[48:51], v[32:35]\n"
  "v_xor_b32 v107, 0x8000, v107\n"
  "v_mfma_f32_16x16x32_bf16 v[36:39], v[240:243], v[48:51], v[36:39]\n"
  "s_waitcnt lgkmcnt(6)\n"
  "v_mfma_f32_16x16x32_bf16 v[40:43], v[176:179], v[52:55], v[40:43]\n"
  "s_mov_b32 s78, s51\n"
  "s_mov_b32 s79, s57\n"
  "v_mfma_f32_16x16x32_bf16 v[44:47], v[240:243], v[52:55], v[44:47]\n"
  "s_waitcnt lgkmcnt(5)\n"
  "v_mfma_f32_16x16x32_bf16 v[32:35], v[180:183], v[56:59], v[32:35]\n"
  "s_add_u32 s51, s51, 0x200000\n"
  "s_xor_b32 s51, s51, 8192\n"
  "s_bitset0_b32 s51, 26\n"
  "v_mfma_f32_16x16x32_bf16 v[36:39], v[244:247], v[56:59], v[36:39]\n"
  "s_waitcnt lgkmcnt(4)\n"
  "v_mfma_f32_16x16x32_bf16 v[40:43], v[180:183], v[60:63], v[40:43]\n"
  "v_mfma_f32_16x16x32_bf16 v[44:47], v[244:247], v[60:63], v[44:47]\n"
  "s_waitcnt lgkmcnt(3)\n"
  "v_mfma_f32_16x16x32_bf16 v[32:35], v[184:187], v[64:67], v[32:35]\n"
  "v_mfma_f32_16x16x32_bf16 v[36:39], v[248:251], v[64:67], v[36:39]\n"
  "s_waitcnt lgkmcnt(2)\n"
  "v_mfma_f32_16x16x32_bf16 v[40:43], v[184:187], v[68:71], v[40:43]\n"
  "s_waitcnt vmcnt(1)\n"
  "s_waitcnt lgkmcnt(0)\n"
  "s_barrier\n"
  "ds_read_b128 v[48:51], v104 offset:0\n"
  "v_mfma_f32_16x16x32_bf16 v[44:47], v[248:251], v[68:71], v[44:47]\n"
  "ds_read_b128 v[52:55], v104 offset:16384\n"
  "ds_read_b128 v[56:59], v105 offset:0\n"
  "v_mfma_f32_16x16x32_bf16 v[32:35], v[188:191], v[72:75], v[32:35]\n"
  "ds_read_b128 v[60:63], v105 offset:16384\n"
  "ds_read_b128 v[64:67], v106 offset:0\n"
  "v_mfma_f32_16x16x32_bf16 v[36:39], v[252:255], v[72:75], v[36:39]\n"
  "ds_read_b128 v[68:71], v106 offset:16384\n"
  "ds_read_b128 v[72:75], v107 offset:0\n"
  "v_mfma_f32_16x16x32_bf16 v[40:43], v[188:191], v[76:79], v[40:43]\n"
  "s_waitcnt vmcnt(9)\n"
  "v_add_u32 v115, v115, v122\n"
  "v_max_u32 v114, v114, v115\n"
  "v_mfma_f32_16x16x32_bf16 v[44:47], v[252:255], v[76:79], v[44:47]\n"
  "ds_read_b128 v[76:79], v107 offset:16384\n"
  "s_branch .Lq_seg\n"
  ".Lq_w3:\n"
  "s_mov_b32 m0, s61\n"
  "s_nop 0\n"
  "buffer_load_dwordx4 v80, s[36:39], s58 offen offset:0 sc1 lds\n"
  "v_mfma_f32_16x16x32_bf16 v[44:47], v[228:231], v[60:63], v[44:47]\n"
  "ds_read_b128 v[60:63], v105 offset:17152\n"
  "buffer_load_dwordx4 v81, s[36:39], s58 offen offset:1024 sc1 lds\n"
  "s_waitcnt lgkmcnt(7)\n"
  "v_mfma_f32_16x16x32_bf16 v[32:35], v[168:171], v[64:67], v[32:35]\n"
  "v_xor_b32 v105, 0x8000, v105\n"
  "buffer_load_dwordx4 v82, s[36:39], s58 offen offset:2048 sc1 lds\n"
  "v_mfma_f32_16x16x32_bf16 v[36:39], v[232:235], v[64:67], v[36:39]\n"
  "ds_read_b128 v[64:67], v106 offset:768\n"
  "buffer_load_dwordx4 v83, s[36:39], s58 offen offset:3072 sc1 lds\n"
  "s_add_u32 m0, s61, 4096\n"
  "s_nop 0\n"
  "s_waitcnt lgkmcnt(7)\n"
  "v_mfma_f32_16x16x32_bf16 v[40:43], v[168:171], v[68:71], v[40:43]\n"
  "buffer_load_dwordx4 v84, s[36:39], s59 offen offset:0 sc1 lds\n"
  "v_mfma_f32_16x16x32_bf16 v[44:47], v[232:235], v[68:71], v[44:47]\n"
  "ds_read_b128 v[68:71], v106 offset:17152\n"
  "s_add_u32 s54, s54, 1\n"
  "s_add_u32 s57, s51, 4096\n"
  "v_mov_b32 v116, s54\n"
  "buffer_load_dwordx4 v85, s[36:39], s59 offen offset:1024 sc1 lds\n"
  "s_waitcnt lgkmcnt(7)\n"
  "v_mfma_f32_16x16x32_bf16 v[32:35], v[172:175], v[72:75], v[32:35]\n"
  "v_xor_b32 v106, 0x8000, v106\n"
  "s_waitcnt vmcnt(8)\n"
  "buffer_store_dword v116, v113, s[36:39], 0 offen offset:0 sc1\n"
  "buffer_load_dwordx4 v86, s[36:39], s59 offen offset:2048 sc1 lds\n"
  "v_mfma_f32_16x16x32_bf16 v[36:39], v[236:239], v[72:75], v[36:39]\n"
  "ds_read_b128 v[72:75], v107 offset:768\n"
  "buffer_load_dwordx4 v87, s[36:39], s59 offen offset:3072 sc1 lds\n"
  "s_waitcnt lgkmcnt(7)\n"
  "v_mfma_f32_16x16x32_bf16 v[40:43], v[172:175], v[76:79], v[40:43]\n"
  "v_mfma_f32_16x16x32_bf16 v[44:47], v[236:239], v[76:79], v[44:47]\n"
  "ds_read_b128 v[76:79], v107 offset:17152\n"
  "s_waitcnt lgkmcnt(7)\n"
  "v_mfma_f32_16x16x32_bf16 v[32:35], v[176:179], v[48:51], v[32:35]\n"
  "v_xor_b32 v107, 0x8000, v107\n"
  "v_mfma_f32_16x16x32_bf16 v[36:39], v[240:243], v[48:51], v[36:39]\n"
  "s_waitcnt lgkmcnt(6)\n"
  "v_mfma_f32_16x16x32_bf16 v[40:43], v[176:179], v[52:55], v[40:43]\n"
  "s_mov_b32 s78, s51\n"
  "s_mov_b32 s79, s57\n"
  "v_mfma_f32_16x16x32_bf16 v[44:47], v[240:243], v[52:55], v[44:47]\n"
  "s_waitcnt lgkmcnt(5)\n"
  "v_mfma_f32_16x16x32_bf16 v[32:35], v[180:183], v[56:59], v[32:35]\n"
  "s_add_u32 s51, s51, 0x200000\n"
  "s_xor_b32 s51, s51, 8192\n"
  "s_bitset0_b32 s51, 26\n"
  "v_mfma_f32_16x16x32_bf16 v[36:39], v[244:247], v[56:59], v[36:39]\n"
  "s_waitcnt lgkmcnt(4)\n"
  "v_mfma_f32_16x16x32_bf16 v[40:43], v[180:183], v[60:63], v[40:43]\n"
  "v_mfma_f32_16x16x32_bf16 v[44:47], v[244:247], v[60:63], v[44:47]\n"
  "s_waitcnt lgkmcnt(3)\n"
  "v_mfma_f32_16x16x32_bf16 v[32:35], v[184:187], v[64:67], v[32:35]\n"
  "v_mfma_f32_16x16x32_bf16 v[36:39], v[248:251], v[64:67], v[36:39]\n"
  "s_waitcnt lgkmcnt(2)\n"
  "v_mfma_f32_16x16x32_bf16 v[40:43], v[184:187], v[68:71], v[40:43]\n"
  "s_waitcnt vmcnt(0)\n"
  "s_waitcnt lgkmcnt(0)\n"
  "s_barrier\n"
  "ds_read_b128 v[48:51], v104 offset:0\n"
  "v_mfma_f32_16x16x32_bf16 v[44:47], v[248:251], v[68:71], v[44:47]\n"
  "ds_read_b128 v[52:55], v104 offset:16384\n"
  "ds_read_b128 v[56:59], v105 offset:0\n"
  "v_mfma_f32_16x16x32_bf16 v[32:35], v[188:191], v[72:75], v[32:35]\n"
  "ds_read_b128 v[60:63], v105 offset:16384\n"
  "ds_read_b128 v[64:67], v106 offset:0\n"
  "v_mfma_f32_16x16x32_bf16 v[36:39], v[252:255], v[72:75], v[36:39]\n"
  "ds_read_b128 v[68:71], v106 offset:16384\n"
  "ds_read_b128 v[72:75], v107 offset:0\n"
  "v_mfma_f32_16x16x32_bf16 v[40:43], v[188:191], v[76:79], v[40:43]\n"
  "s_waitcnt vmcnt(10)\n"
  "v_add_u32 v115, v115, v122\n"
  "v_max_u32 v114, v114, v115\n"
  "v_mfma_f32_16x16x32_bf16 v[44:47], v[252:255], v[76:79], v[44:47]\n"
  "ds_read_b128 v[76:79], v107 offset:16384\n"
  "s_branch .Lq_seg\n"
  ".Lq_e2:\n"
  "s_nop 7\n"
  "v_cvt_pk_bf16_f32 v32, v32, v33\n"
  "v_cvt_pk_bf16_f32 v33, v34, v35\n"
  "v_cvt_pk_bf16_f32 v34, v36, v37\n"
  "v_cvt_pk_bf16_f32 v35, v38, v39\n"
  "buffer_store_dwordx4 v[32:35], v110, s[36:39], s51 offen offset:0 sc1\n"
  "v_cvt_pk_bf16_f32 v40, v40, v41\n"
  "v_cvt_pk_bf16_f32 v41, v42, v43\n"
  "v_cvt_pk_bf16_f32 v42, v44, v45\n"
  "v_cvt_pk_bf16_f32 v43, v46, v47\n"
  "buffer_store_dwordx4 v[40:43], v110, s[36:39], s57 offen offset:0 sc1\n"
  "s_add_u32 s51, s51, 0x200000\n"
  "s_xor_b32 s51, s51, 8192\n"
  "s_bitset0_b32 s51, 26\n"
  "s_waitcnt vmcnt(2)\n"
  "v_add_u32 v115, v115, v122\n"
  "v_max_u32 v114, v114, v115\n"
  "s_branch .Lq_go\n"
  ".Lq_first:\n"
  "s_mov_b32 s68, 0\n"
  "v_cvt_pk_bf16_f32 v16, v16, v17\n"
  "v_cvt_pk_bf16_f32 v17, v18, v19\n"
  "v_cvt_pk_bf16_f32 v18, v20, v21\n"
  "v_cvt_pk_bf16_f32 v19, v22, v23\n"
  "buffer_store_dwordx4 v[16:19], v109, s[36:39], s57 offen offset:0 sc1\n"
  "v_cvt_pk_bf16_f32 v8, v8, v9\n"
  "v_cvt_pk_bf16_f32 v9, v10, v11\n"
  "v_cvt_pk_bf16_f32 v10, v12, v13\n"
  "v_cvt_pk_bf16_f32 v11, v14, v15\n"
  "buffer_store_dwordx4 v[8:11], v109, s[36:39], s56 offen offset:1024 sc1\n"
  "v_cvt_pk_bf16_f32 v24, v24, v25\n"
  "v_cvt_pk_bf16_f32 v25, v26, v27\n"
  "v_cvt_pk_bf16_f32 v26, v28, v29\n"
  "v_cvt_pk_bf16_f32 v27, v30, v31\n"
  "buffer_store_dwordx4 v[24:27], v109, s[36:39], s57 offen offset:1024 sc1\n"
  "ds_read_b64_tr_b16 v[128:129], v119 offset:16384\n"
  "ds_read_b64_tr_b16 v[192:193], v119 offset:16392\n"
  "ds_read_b64_tr_b16 v[132:133], v119 offset:17408\n"
  "ds_read_b64_tr_b16 v[196:197], v119 offset:17416\n"
  "ds_read_b64_tr_b16 v[144:145], v119 offset:18432\n"
  "ds_read_b64_tr_b16 v[208:209], v119 offset:18440\n"
  "ds_read_b64_tr_b16 v[148:149], v119 offset:19456\n"
  "ds_read_b64_tr_b16 v[212:213], v119 offset:19464\n"
  "ds_read_b64_tr_b16 v[160:161], v119 offset:20480\n"
  "ds_read_b64_tr_b16 v[224:225], v119 offset:20488\n"
  "ds_read_b64_tr_b16 v[164:165], v119 offset:21504\n"
  "ds_read_b64_tr_b16 v[228:229], v119 offset:21512\n"
  "ds_read_b64_tr_b16 v[176:177], v119 offset:22528\n"
  "ds_read_b64_tr_b16 v[240:241], v119 offset:22536\n"
  "ds_read_b64_tr_b16 v[180:181], v119 offset:23552\n"
  "ds_read_b64_tr_b16 v[244:245], v119 offset:23560\n"
  "s_waitcnt lgkmcnt(0)\n"
  "s_add_u32 m0, s50, 16384\n"
  "s_nop 0\n"
  "global_load_lds_dwordx4 v118, s[44:45]\n"
  "s_add_u32 s44, s44, 0x20000\n"
  "s_addc_u32 s45, s45, 0\n"
  "s_add_u32 m0, m0, 1024\n"
  "s_nop 0\n"
  "global_load_lds_dwordx4 v118, s[44:45]\n"
  "s_add_u32 s44, s44, 0x20000\n"
  "s_addc_u32 s45, s45, 0\n"
  "s_add_u32 m0, m0, 1024\n"
  "s_nop 0\n"
  "global_load_lds_dwordx4 v118, s[44:45]\n"
  "s_add_u32 s44, s44, 0x20000\n"
  "s_addc_u32 s45, s45, 0\n"
  "s_add_u32 m0, m0, 1024\n"
  "s_nop 0\n"
  "global_load_lds_dwordx4 v118, s[44:45]\n"
  "s_add_u32 s44, s44, 0x20000\n"
  "s_addc_u32 s45, s45, 0\n"
  "s_add_u32 m0, m0, 1024\n"
  "s_nop 0\n"
  "global_load_lds_dwordx4 v118, s[44:45]\n"
  "s_add_u32 s44, s44, 0x20000\n"
  "s_addc_u32 s45, s45, 0\n"
  "s_add_u32 m0, m0, 1024\n"
  "s_nop 0\n"
  "global_load_lds_dwordx4 v118, s[44:45]\n"
  "s_add_u32 s44, s44, 0x20000\n"
  "s_addc_u32 s45, s45, 0\n"
  "s_add_u32 m0, m0, 1024\n"
  "s_nop 0\n"
  "global_load_lds_dwordx4 v118, s[44:45]\n"
  "s_add_u32 s44, s44, 0x20000\n"
  "s_addc_u32 s45, s45, 0\n"
  "s_add_u32 m0, m0, 1024\n"
  "s_nop 0\n"
  "global_load_lds_dwordx4 v118, s[44:45]\n"
  "s_add_u32 s44, s44, 0x20000\n"
  "s_addc_u32 s45, s45, 0\n"
  "ds_read_b64_tr_b16 v[136:137], v119 offset:0\n"
  "ds_read_b64_tr_b16 v[200:201], v119 offset:8\n"
  "ds_read_b64_tr_b16 v[140:141], v119 offset:1024\n"
  "ds_read_b64_tr_b16 v[204:205], v119 offset:1032\n"
  "ds_read_b64_tr_b16 v[152:153], v119 offset:2048\n"
  "ds_read_b64_tr_b16 v[216:217], v119 offset:2056\n"
  "ds_read_b64_tr_b16 v[156:157], v119 offset:3072\n"
  "ds_read_b64_tr_b16 v[220:221], v119 offset:3080\n"
  "ds_read_b64_tr_b16 v[168:169], v119 offset:4096\n"
  "ds_read_b64_tr_b16 v[232:233], v119 offset:4104\n"
  "ds_read_b64_tr_b16 v[172:173], v119 offset:5120\n"
  "ds_read_b64_tr_b16 v[236:237], v119 offset:5128\n"
  "ds_read_b64_tr_b16 v[184:185], v119 offset:6144\n"
  "ds_read_b64_tr_b16 v[248:249], v119 offset:6152\n"
  "ds_read_b64_tr_b16 v[188:189], v119 offset:7168\n"
  "ds_read_b64_tr_b16 v[252:253], v119 offset:7176\n"
  "s_waitcnt lgkmcnt(0)\n"
  "ds_read_b64_tr_b16 v[130:131], v119 offset:8192\n"
  "ds_read_b64_tr_b16 v[194:195], v119 offset:8200\n"
  "ds_read_b64_tr_b16 v[134:135], v119 offset:9216\n"
  "ds_read_b64_tr_b16 v[198:199], v119 offset:9224\n"
  "ds_read_b64_tr_b16 v[146:147], v119 offset:10240\n"
  "ds_read_b64_tr_b16 v[210:211], v119 offset:10248\n"
  "ds_read_b64_tr_b16 v[150:151], v119 offset:11264\n"
  "ds_read_b64_tr_b16 v[214:215], v119 offset:11272\n"
  "ds_read_b64_tr_b16 v[162:163], v119 offset:12288\n"
  "ds_read_b64_tr_b16 v[226:227], v119 offset:12296\n"
  "ds_read_b64_tr_b16 v[166:167], v119 offset:13312\n"
  "ds_read_b64_tr_b16 v[230:231], v119 offset:13320\n"
  "ds_read_b64_tr_b16 v[178:179], v119 offset:14336\n"
  "ds_read_b64_tr_b16 v[242:243], v119 offset:14344\n"
  "ds_read_b64_tr_b16 v[182:183], v119 offset:15360\n"
  "ds_read_b64_tr_b16 v[246:247], v119 offset:15368\n"
  "s_waitcnt lgkmcnt(0)\n"
  "s_add_u32 s54, s54, 1\n"
  "s_add_u32 s57, s51, 4096\n"
  "v_mov_b32 v116, s54\n"
  "s_waitcnt vmcnt(8)\n"
  "buffer_store_dword v116, v113, s[36:39], 0 offen offset:0 sc1\n"
  "s_waitcnt vmcnt(1)\n"
  "ds_read_b64_tr_b16 v[138:139], v119 offset:16384\n"
  "ds_read_b64_tr_b16 v[202:203], v119 offset:16392\n"
  "ds_read_b64_tr_b16 v[142:143], v119 offset:17408\n"
  "ds_read_b64_tr_b16 v[206:207], v119 offset:17416\n"
  "ds_read_b64_tr_b16 v[154:155], v119 offset:18432\n"
  "ds_read_b64_tr_b16 v[218:219], v119 offset:18440\n"
  "ds_read_b64_tr_b16 v[158:159], v119 offset:19456\n"
  "ds_read_b64_tr_b16 v[222:223], v119 offset:19464\n"
  "ds_read_b64_tr_b16 v[170:171], v119 offset:20480\n"
  "ds_read_b64_tr_b16 v[234:235], v119 offset:20488\n"
  "ds_read_b64_tr_b16 v[174:175], v119 offset:21504\n"
  "ds_read_b64_tr_b16 v[238:239], v119 offset:21512\n"
  "ds_read_b64_tr_b16 v[186:187], v119 offset:22528\n"
  "ds_read_b64_tr_b16 v[250:251], v119 offset:22536\n"
  "ds_read_b64_tr_b16 v[190:191], v119 offset:23552\n"
  "ds_read_b64_tr_b16 v[254:255], v119 offset:23560\n"
  "s_waitcnt lgkmcnt(0)\n"
  "v_mfma_f32_16x16x32_bf16 v[32:35], v[128:131], v[48:51], 0\n"
  "v_mfma_f32_16x16x32_bf16 v[36:39], v[192:195], v[48:51], 0\n"
  "ds_read_b128 v[48:51], v104 offset:256\n"
  "v_mfma_f32_16x16x32_bf16 v[40:43], v[128:131], v[52:55], 0\n"
  "v_mfma_f32_16x16x32_bf16 v[44:47], v[192:195], v[52:55], 0\n"
  "ds_read_b128 v[52:55], v104 offset:16640\n"
  "v_mfma_f32_16x16x32_bf16 v[32:35], v[132:135], v[56:59], v[32:35]\n"
  "v_mfma_f32_16x16x32_bf16 v[36:39], v[196:199], v[56:59], v[36:39]\n"
  "ds_read_b128 v[56:59], v105 offset:256\n"
  "v_mfma_f32_16x16x32_bf16 v[40:43], v[132:135], v[60:63], v[40:43]\n"
  "v_mfma_f32_16x16x32_bf16 v[44:47], v[196:199], v[60:63], v[44:47]\n"
  "ds_read_b128 v[60:63], v105 offset:16640\n"
  "v_mfma_f32_16x16x32_bf16 v[32:35], v[136:139], v[64:67], v[32:35]\n"
  "v_mfma_f32_16x16x32_bf16 v[36:39], v[200:203], v[64:67], v[36:39]\n"
  "ds_read_b128 v[64:67], v106 offset:256\n"
  "v_mfma_f32_16x16x32_bf16 v[40:43], v[136:139], v[68:71], v[40:43]\n"
  "v_mfma_f32_16x16x32_bf16 v[44:47], v[200:203], v[68:71], v[44:47]\n"
  "ds_read_b128 v[68:71], v106 offset:16640\n"
  "v_mfma_f32_16x16x32_bf16 v[32:35], v[140:143], v[72:75], v[32:35]\n"
  "v_mfma_f32_16x16x32_bf16 v[36:39], v[204:207], v[72:75], v[36:39]\n"
  "ds_read_b128 v[72:75], v107 offset:256\n"
  "v_mfma_f32_16x16x32_bf16 v[40:43], v[140:143], v[76:79], v[40:43]\n"
  "v_mfma_f32_16x16x32_bf16 v[44:47], v[204:207], v[76:79], v[44:47]\n"
  "ds_read_b128 v[76:79], v107 offset:16640\n"
  "s_waitcnt lgkmcnt(7)\n"
  "v_mfma_f32_16x16x32_bf16 v[32:35], v[144:147], v[48:51], v[32:35]\n"
  "v_mfma_f32_16x16x32_bf16 v[36:39], v[208:211], v[48:51], v[36:39]\n"
  "ds_read_b128 v[48:51], v104 offset:512\n"
  "s_waitcnt lgkmcnt(7)\n"
  "v_mfma_f32_16x16x32_bf16 v[40:43], v[144:147], v[52:55], v[40:43]\n"
  "v_mfma_f32_16x16x32_bf16 v[44:47], v[208:211], v[52:55], v[44:47]\n"
  "ds_read_b128 v[52:55], v104 offset:16896\n"
  "s_waitcnt lgkmcnt(7)\n"
  "v_mfma_f32_16x16x32_bf16 v[32:35], v[148:151], v[56:59], v[32:35]\n"
  "v_mfma_f32_16x16x32_bf16 v[36:39], v[212:215], v[56:59], v[36:39]\n"
  "ds_read_b128 v[56:59], v105 offset:512\n"
  "s_waitcnt lgkmcnt(7)\n"
  "v_mfma_f32_16x16x32_bf16 v[40:43], v[148:151], v[60:63], v[40:43]\n"
  "buffer_load_dword v115, v112, s[36:39], s60 offen sc1\n"
  "v_mfma_f32_16x16x32_bf16 v[44:47], v[212:215], v[60:63], v[44:47]\n"
  "ds_read_b128 v[60:63], v105 offset:16896\n"
  "s_waitcnt lgkmcnt(7)\n"
  "v_mfma_f32_16x16x32_bf16 v[32:35], v[152:155], v[64:67], v[32:35]\n"
  "v_mfma_f32_16x16x32_bf16 v[36:39], v[216:219], v[64:67], v[36:39]\n"
  "ds_read_b128 v[64:67], v106 offset:512\n"
  "s_waitcnt lgkmcnt(7)\n"
  "v_mfma_f32_16x16x32_bf16 v[40:43], v[152:155], v[68:71], v[40:43]\n"
  "v_mfma_f32_16x16x32_bf16 v[44:47], v[216:219], v[68:71], v[44:47]\n"
  "ds_read_b128 v[68:71], v106 offset:16896\n"
  "s_waitcnt lgkmcnt(7)\n"
  "v_mfma_f32_16x16x32_bf16 v[32:35], v[156:159], v[72:75], v[32:35]\n"
  "v_mfma_f32_16x16x32_bf16 v[36:39], v[220:223], v[72:75], v[36:39]\n"
  "ds_read_b128 v[72:75], v107 offset:512\n"
  "s_waitcnt lgkmcnt(7)\n"
  "v_mfma_f32_16x16x32_bf16 v[40:43], v[156:159], v[76:79], v[40:43]\n"
  "v_mfma_f32_16x16x32_bf16 v[44:47], v[220:223], v[76:79], v[44:47]\n"
  "ds_read_b128 v[76:79], v107 offset:16896\n"
  "s_waitcnt lgkmcnt(7)\n"
  "v_mfma_f32_16x16x32_bf16 v[32:35], v[160:163], v[48:51], v[32:35]\n"
  "v_mfma_f32_16x16x32_bf16 v[36:39], v[224:227], v[48:51], v[36:39]\n"
  "ds_read_b128 v[48:51], v104 offset:768\n"
  "s_waitcnt lgkmcnt(7)\n"
  "v_mfma_f32_16x16x32_bf16 v[40:43], v[160:163], v[52:55], v[40:43]\n"
  "v_mfma_f32_16x16x32_bf16 v[44:47], v[224:227], v[52:55], v[44:47]\n"
  "ds_read_b128 v[52:55], v104 offset:17152\n"
  "s_waitcnt lgkmcnt(7)\n"
  "v_mfma_f32_16x16x32_bf16 v[32:35], v[164:167], v[56:59], v[32:35]\n"
  "v_xor_b32 v104, 0x8000, v104\n"
  "buffer_load_dword v92, v112, s[36:39], 0 offen sc1\n"
  "v_mfma_f32_16x16x32_bf16 v[36:39], v[228:231], v[56:59], v[36:39]\n"
  "ds_read_b128 v[56:59], v105 offset:768\n"
  "s_waitcnt lgkmcnt(7)\n"
  "v_mfma_f32_16x16x32_bf16 v[40:43], v[164:167], v[60:63], v[40:43]\n"
  "v_mfma_f32_16x16x32_bf16 v[44:47], v[228:231], v[60:63], v[44:47]\n"
  "ds_read_b128 v[60:63], v105 offset:17152\n"
  "s_waitcnt lgkmcnt(7)\n"
  "v_mfma_f32_16x16x32_bf16 v[32:35], v[168:171], v[64:67], v[32:35]\n"
  "v_xor_b32 v105, 0x8000, v105\n"
  "v_mfma_f32_16x16x32_bf16 v[36:39], v[232:235], v[64:67], v[36:39]\n"
  "ds_read_b128 v[64:67], v106 offset:768\n"
  "s_waitcnt lgkmcnt(7)\n"
  "v_mfma_f32_16x16x32_bf16 v[40:43], v[168:171], v[68:71], v[40:43]\n"
  "v_mfma_f32_16x16x32_bf16 v[44:47], v[232:235], v[68:71], v[44:47]\n"
  "ds_read_b128 v[68:71], v106 offset:17152\n"
  "s_waitcnt lgkmcnt(7)\n"
  "v_mfma_f32_16x16x32_bf16 v[32:35], v[172:175], v[72:75], v[32:35]\n"
  "v_xor_b32 v106, 0x8000, v106\n"
  "v_mfma_f32_16x16x32_bf16 v[36:39], v[236:239], v[72:75], v[36:39]\n"
  "ds_read_b128 v[72:75], v107 offset:768\n"
  "s_waitcnt lgkmcnt(7)\n"
  "v_mfma_f32_16x16x32_bf16 v[40:43], v[172:175], v[76:79], v[40:43]\n"
  "v_mfma_f32_16x16x32_bf16 v[44:47], v[236:239], v[76:79], v[44:47]\n"
  "ds_read_b128 v[76:79], v107 offset:17152\n"
  "s_waitcnt lgkmcnt(7)\n"
  "v_mfma_f32_16x16x32_bf16 v[32:35], v[176:179], v[48:51], v[32:35]\n"
  "v_xor_b32 v107, 0x8000, v107\n"
  "v_mfma_f32_16x16x32_bf16 v[36:39], v[240:243], v[48:51], v[36:39]\n"
  "s_waitcnt lgkmcnt(6)\n"
  "v_mfma_f32_16x16x32_bf16 v[40:43], v[176:179], v[52:55], v[40:43]\n"
  "v_mfma_f32_16x16x32_bf16 v[44:47], v[240:243], v[52:55], v[44:47]\n"
  "s_waitcnt lgkmcnt(5)\n"
  "v_mfma_f32_16x16x32_bf16 v[32:35], v[180:183], v[56:59], v[32:35]\n"
  "v_mfma_f32_16x16x32_bf16 v[36:39], v[244:247], v[56:59], v[36:39]\n"
  "s_waitcnt lgkmcnt(4)\n"
  "v_mfma_f32_16x16x32_bf16 v[40:43], v[180:183], v[60:63], v[40:43]\n"
  "v_mfma_f32_16x16x32_bf16 v[44:47], v[244:247], v[60:63], v[44:47]\n"
  "s_waitcnt lgkmcnt(3)\n"
  "v_mfma_f32_16x16x32_bf16 v[32:35], v[184:187], v[64:67], v[32:35]\n"
  "v_mfma_f32_16x16x32_bf16 v[36:39], v[248:251], v[64:67], v[36:39]\n"
  "s_waitcnt lgkmcnt(2)\n"
  "v_mfma_f32_16x16x32_bf16 v[40:43], v[184:187], v[68:71], v[40:43]\n"
  "v_mfma_f32_16x16x32_bf16 v[44:47], v[248:251], v[68:71], v[44:47]\n"
  "s_waitcnt lgkmcnt(1)\n"
  "v_mfma_f32_16x16x32_bf16 v[32:35], v[188:191], v[72:75], v[32:35]\n"
  "v_mfma_f32_16x16x32_bf16 v[36:39], v[252:255], v[72:75], v[36:39]\n"
  "s_waitcnt lgkmcnt(0)\n"
  "v_mfma_f32_16x16x32_bf16 v[40:43], v[188:191], v[76:79], v[40:43]\n"
  "v_mfma_f32_16x16x32_bf16 v[44:47], v[252:255], v[76:79], v[44:47]\n"
  "s_waitcnt vmcnt(0)\n"
  "s_cbranch_vccz .Lq_e2\n"
  "s_branch .Lq_e\n"
  ".Lq_end:\n"
  "s_endpgm\n"
      : "+{v[104:107]}"(xa), "+{s[40:41]}"(pk_), "+{s[42:43]}"(pv_), "+{s[44:45]}"(pq_), "+{s51}"(sq), "+{s54}"(st), "+{s55}"(bud), "+{s62}"(dbg)
      : "{v109}"(lane * 16 + w * 2048), "{v110}"(lane * 16 + w * 1024), "{v111}"(((lane ^ (8 * (w & 1))) << 4) + w * 16384), "{v112}"(sink ? FL_X0 + (lane & 31) * 128 + 4 : l ? fl(0, F2_XN + h % RN * 32 + 8 * w + (lane & 7)) : FL_X0 + (8 * w + (lane & 7)) * 128), "{v122}"(sink ? RING + !HSM : 0),
        "{v113}"(lane == 0 ? fl(l, FL_KV + h * 8 + w) : OOR), "{v118}"((lane >> 2) * 8192 + (lane & 3) * 16), "{v119}"(reg + (4 * g + (li >> 2)) * 64 + (li & 3) * 16),
        "{s[36:39]}"(rs), "{s50}"(reg), "{s52}"(RFL(l ? (128u << 20) + 983040u : OFF_X0)), "{s53}"(0), "{s61}"(w * 8192), "{s64}"(HS), "{s65}"(RFL(l * DL1 + DQK)), "{s66}"(RFL(l << 13)), "{s69}"(RFL(OFF_DBG + (6u << 20) + (BID * 4 + w) * 32 + 16))
      : "memory", "vcc", "scc",
        "s101", "v255", "a255"
  );
  __builtin_unreachable();
}

#define RSRC(ws) i4v{(int)(u64)(ws), (int)((u64)(ws) >> 32), (int)WS_USED, 0x00020000}
#define COMMON_IN "{s[36:39]}"(RSRC(ws)), "{s50}"(l), "{s51}"(0), "{s52}"(RFL(C.budget)), "{s57}"(RFL(OFF_DBG + (BID * 4 + w) * 64 * 64)), \
                  "{s69}"(RFL(OFF_DBG + (6u << 20) + (BID * 4 + w) * 32 + 16))
#define NORET : "memory", "vcc", "scc", "s101", "v255", "a255"); __builtin_unreachable()
DEV void role_op(Ctx& C, char* ws, u32 l, u32 v0, int kq, int n4, int th, int w, int lane, const u16* Wo) {
  const int g = lane >> 4, li = lane & 15, q = lane & 3;
  asm volatile(
  "s_nop 4\n"
  "s_add_u32 m0, s58, 0\n"
  "s_nop 0\n"
  "global_load_lds_dwordx4 v118, s[40:41]\n"
  "s_add_u32 s40, s40, 4096\n"
  "s_addc_u32 s41, s41, 0\n"
  "s_add_u32 m0, m0, 1024\n"
  "s_nop 0\n"
  "global_load_lds_dwordx4 v118, s[40:41]\n"
  "s_add_u32 s40, s40, 4096\n"
  "s_addc_u32 s41, s41, 0\n"
  "s_add_u32 m0, m0, 1024\n"
  "s_nop 0\n"
  "global_load_lds_dwordx4 v118, s[40:41]\n"
  "s_add_u32 s40, s40, 4096\n"
  "s_addc_u32 s41, s41, 0\n"
  "s_add_u32 m0, m0, 1024\n"
  "s_nop 0\n"
  "global_load_lds_dwordx4 v118, s[40:41]\n"
  "s_add_u32 s40, s40, 4096\n"
  "s_addc_u32 s41, s41, 0\n"
  "s_add_u32 m0, m0, 1024\n"
  "s_nop 0\n"
  "global_load_lds_dwordx4 v118, s[40:41]\n"
  "s_add_u32 s40, s40, 4096\n"
  "s_addc_u32 s41, s41, 0\n"
  "s_add_u32 m0, m0, 1024\n"
  "s_nop 0\n"
  "global_load_lds_dwordx4 v118, s[40:41]\n"
  "s_add_u32 s40, s40, 4096\n"
  "s_addc_u32 s41, s41, 0\n"
  "s_add_u32 m0, m0, 1024\n"
  "s_nop 0\n"
  "global_load_lds_dwordx4 v118, s[40:41]\n"
  "s_add_u32 s40, s40, 4096\n"
  "s_addc_u32 s41, s41, 0\n"
  "s_add_u32 m0, m0, 1024\n"
  "s_nop 0\n"
  "global_load_lds_dwordx4 v118, s[40:41]\n"
  "s_add_u32 s40, s40, 4096\n"
  "s_addc_u32 s41, s41, 0\n"
  "s_add_u32 m0, s58, 8192\n"
  "s_nop 0\n"
  "global_load_lds_dwordx4 v118, s[40:41]\n"
  "s_add_u32 s40, s40, 4096\n"
  "s_addc_u32 s41, s41, 0\n"
  "s_add_u32 m0, m0, 1024\n"
  "s_nop 0\n"
  "global_load_lds_dwordx4 v118, s[40:41]\n"
  "s_add_u32 s40, s40, 4096\n"
  "s_addc_u32 s41, s41, 0\n"
  "s_add_u32 m0, m0, 1024\n"
  "s_nop 0\n"
  "global_load_lds_dwordx4 v118, s[40:41]\n"
  "s_add_u32 s40, s40, 4096\n"
  "s_addc_u32 s41, s41, 0\n"
  "s_add_u32 m0, m0, 1024\n"
  "s_nop 0\n"
  "global_load_lds_dwordx4 v118, s[40:41]\n"
  "s_add_u32 s40, s40, 4096\n"
  "s_addc_u32 s41, s41, 0\n"
  "s_add_u32 m0, m0, 1024\n"
  "s_nop 0\n"
  "global_load_lds_dwordx4 v118, s[40:41]\n"
  "s_add_u32 s40, s40, 4096\n"
  "s_addc_u32 s41, s41, 0\n"
  "s_add_u32 m0, m0, 1024\n"
  "s_nop 0\n"
  "global_load_lds_dwordx4 v118, s[40:41]\n"
  "s_add_u32 s40, s40, 4096\n"
  "s_addc_u32 s41, s41, 0\n"
  "s_add_u32 m0, m0, 1024\n"
  "s_nop 0\n"
  "global_load_lds_dwordx4 v118, s[40:41]\n"
  "s_add_u32 s40, s40, 4096\n"
  "s_addc_u32 s41, s41, 0\n"
  "s_add_u32 m0, m0, 1024\n"
  "s_nop 0\n"
  "global_load_lds_dwordx4 v118, s[40:41]\n"
  "s_add_u32 s40, s40, 4096\n"
  "s_addc_u32 s41, s41, 0\n"
  "s_add_u32 m0, s58, 16384\n"
  "s_nop 0\n"
  "global_load_lds_dwordx4 v118, s[40:41]\n"
  "s_add_u32 s40, s40, 4096\n"
  "s_addc_u32 s41, s41, 0\n"
  "s_add_u32 m0, m0, 1024\n"
  "s_nop 0\n"
  "global_load_lds_dwordx4 v118, s[40:41]\n"
  "s_add_u32 s40, s40, 4096\n"
  "s_addc_u32 s41, s41, 0\n"
  "s_add_u32 m0, m0, 1024\n"
  "s_nop 0\n"
  "global_load_lds_dwordx4 v118, s[40:41]\n"
  "s_add_u32 s40, s40, 4096\n"
  "s_addc_u32 s41, s41, 0\n"
  "s_add_u32 m0, m0, 1024\n"
  "s_nop 0\n"
  "global_load_lds_dwordx4 v118, s[40:41]\n"
  "s_add_u32 s40, s40, 4096\n"
  "s_addc_u32 s41, s41, 0\n"
  "s_add_u32 m0, m0, 1024\n"
  "s_nop 0\n"
  "global_load_lds_dwordx4 v118, s[40:41]\n"
  "s_add_u32 s40, s40, 4096\n"
  "s_addc_u32 s41, s41, 0\n"
  "s_add_u32 m0, m0, 1024\n"
  "s_nop 0\n"
  "global_load_lds_dwordx4 v118, s[40:41]\n"
  "s_add_u32 s40, s40, 4096\n"
  "s_addc_u32 s41, s41, 0\n"
  "s_add_u32 m0, m0, 1024\n"
  "s_nop 0\n"
  "global_load_lds_dwordx4 v118, s[40:41]\n"
  "s_add_u32 s40, s40, 4096\n"
  "s_addc_u32 s41, s41, 0\n"
  "s_add_u32 m0, m0, 1024\n"
  "s_nop 0\n"
  "global_load_lds_dwordx4 v118, s[40:41]\n"
  "s_add_u32 s40, s40, 4096\n"
  "s_addc_u32 s41, s41, 0\n"
  "s_add_u32 m0, s58, 24576\n"
  "s_nop 0\n"
  "global_load_lds_dwordx4 v118, s[40:41]\n"
  "s_add_u32 s40, s40, 4096\n"
  "s_addc_u32 s41, s41, 0\n"
  "s_add_u32 m0, m0, 1024\n"
  "s_nop 0\n"
  "global_load_lds_dwordx4 v118, s[40:41]\n"
  "s_add_u32 s40, s40, 4096\n"
  "s_addc_u32 s41, s41, 0\n"
  "s_add_u32 m0, m0, 1024\n"
  "s_nop 0\n"
  "global_load_lds_dwordx4 v118, s[40:41]\n"
  "s_add_u32 s40, s40, 4096\n"
  "s_addc_u32 s41, s41, 0\n"
  "s_add_u32 m0, m0, 1024\n"
  "s_nop 0\n"
  "global_load_lds_dwordx4 v118, s[40:41]\n"
  "s_add_u32 s40, s40, 4096\n"
  "s_addc_u32 s41, s41, 0\n"
  "s_add_u32 m0, m0, 1024\n"
  "s_nop 0\n"
  "global_load_lds_dwordx4 v118, s[40:41]\n"
  "s_add_u32 s40, s40, 4096\n"
  "s_addc_u32 s41, s41, 0\n"
  "s_add_u32 m0, m0, 1024\n"
  "s_nop 0\n"
  "global_load_lds_dwordx4 v118, s[40:41]\n"
  "s_add_u32 s40, s40, 4096\n"
  "s_addc_u32 s41, s41, 0\n"
  "s_add_u32 m0, m0, 1024\n"
  "s_nop 0\n"
  "global_load_lds_dwordx4 v118, s[40:41]\n"
  "s_add_u32 s40, s40, 4096\n"
  "s_addc_u32 s41, s41, 0\n"
  "s_add_u32 m0, m0, 1024\n"
  "s_nop 0\n"
  "global_load_lds_dwordx4 v118, s[40:41]\n"
  "s_add_u32 s40, s40, 4096\n"
  "s_addc_u32 s41, s41, 0\n"
  "s_waitcnt vmcnt(24)\n"
  "ds_read_b64_tr_b16 a[0:1], v119 offset:0\n"
  "ds_read_b64_tr_b16 a[2:3], v119 offset:1024\n"
  "ds_read_b64_tr_b16 a[4:5], v119 offset:32\n"
  "ds_read_b64_tr_b16 a[6:7], v119 offset:1056\n"
  "ds_read_b64_tr_b16 a[8:9], v119 offset:64\n"
  "ds_read_b64_tr_b16 a[10:11], v119 offset:1088\n"
  "ds_read_b64_tr_b16 a[12:13], v119 offset:96\n"
  "ds_read_b64_tr_b16 a[14:15], v119 offset:1120\n"
  "ds_read_b64_tr_b16 a[16:17], v119 offset:128\n"
  "ds_read_b64_tr_b16 a[18:19], v119 offset:1152\n"
  "ds_read_b64_tr_b16 a[20:21], v119 offset:160\n"
  "ds_read_b64_tr_b16 a[22:23], v119 offset:1184\n"
  "ds_read_b64_tr_b16 a[24:25], v119 offset:192\n"
  "ds_read_b64_tr_b16 a[26:27], v119 offset:1216\n"
  "ds_read_b64_tr_b16 a[28:29], v119 offset:224\n"
  "ds_read_b64_tr_b16 a[30:31], v119 offset:1248\n"
  "s_waitcnt lgkmcnt(0)\n"
  "s_add_u32 m0, s58, 0\n"
  "s_nop 0\n"
  "global_load_lds_dwordx4 v118, s[40:41]\n"
  "s_add_u32 s40, s40, 4096\n"
  "s_addc_u32 s41, s41, 0\n"
  "s_add_u32 m0, m0, 1024\n"
  "s_nop 0\n"
  "global_load_lds_dwordx4 v118, s[40:41]\n"
  "s_add_u32 s40, s40, 4096\n"
  "s_addc_u32 s41, s41, 0\n"
  "s_add_u32 m0, m0, 1024\n"
  "s_nop 0\n"
  "global_load_lds_dwordx4 v118, s[40:41]\n"
  "s_add_u32 s40, s40, 4096\n"
  "s_addc_u32 s41, s41, 0\n"
  "s_add_u32 m0, m0, 1024\n"
  "s_nop 0\n"
  "global_load_lds_dwordx4 v118, s[40:41]\n"
  "s_add_u32 s40, s40, 4096\n"
  "s_addc_u32 s41, s41, 0\n"
  "s_add_u32 m0, m0, 1024\n"
  "s_nop 0\n"
  "global_load_lds_dwordx4 v118, s[40:41]\n"
  "s_add_u32 s40, s40, 4096\n"
  "s_addc_u32 s41, s41, 0\n"
  "s_add_u32 m0, m0, 1024\n"
  "s_nop 0\n"
  "global_load_lds_dwordx4 v118, s[40:41]\n"
  "s_add_u32 s40, s40, 4096\n"
  "s_addc_u32 s41, s41, 0\n"
  "s_add_u32 m0, m0, 1024\n"
  "s_nop 0\n"
  "global_load_lds_dwordx4 v118, s[40:41]\n"
  "s_add_u32 s40, s40, 4096\n"
  "s_addc_u32 s41, s41, 0\n"
  "s_add_u32 m0, m0, 1024\n"
  "s_nop 0\n"
  "global_load_lds_dwordx4 v118, s[40:41]\n"
  "s_add_u32 s40, s40, 4096\n"
  "s_addc_u32 s41, s41, 0\n"
  "s_waitcnt vmcnt(24)\n"
  "ds_read_b64_tr_b16 a[32:33], v119 offset:8192\n"
  "ds_read_b64_tr_b16 a[34:35], v119 offset:9216\n"
  "ds_read_b64_tr_b16 a[36:37], v119 offset:8224\n"
  "ds_read_b64_tr_b16 a[38:39], v119 offset:9248\n"
  "ds_read_b64_tr_b16 a[40:41], v119 offset:8256\n"
  "ds_read_b64_tr_b16 a[42:43], v119 offset:9280\n"
  "ds_read_b64_tr_b16 a[44:45], v119 offset:8288\n"
  "ds_read_b64_tr_b16 a[46:47], v119 offset:9312\n"
  "ds_read_b64_tr_b16 a[48:49], v119 offset:8320\n"
  "ds_read_b64_tr_b16 a[50:51], v119 offset:9344\n"
  "ds_read_b64_tr_b16 a[52:53], v119 offset:8352\n"
  "ds_read_b64_tr_b16 a[54:55], v119 offset:9376\n"
  "ds_read_b64_tr_b16 a[56:57], v119 offset:8384\n"
  "ds_read_b64_tr_b16 a[58:59], v119 offset:9408\n"
  "ds_read_b64_tr_b16 a[60:61], v119 offset:8416\n"
  "ds_read_b64_tr_b16 a[62:63], v119 offset:9440\n"
  "s_waitcnt lgkmcnt(0)\n"
  "s_add_u32 m0, s58, 8192\n"
  "s_nop 0\n"
  "global_load_lds_dwordx4 v118, s[40:41]\n"
  "s_add_u32 s40, s40, 4096\n"
  "s_addc_u32 s41, s41, 0\n"
  "s_add_u32 m0, m0, 1024\n"
  "s_nop 0\n"
  "global_load_lds_dwordx4 v118, s[40:41]\n"
  "s_add_u32 s40, s40, 4096\n"
  "s_addc_u32 s41, s41, 0\n"
  "s_add_u32 m0, m0, 1024\n"
  "s_nop 0\n"
  "global_load_lds_dwordx4 v118, s[40:41]\n"
  "s_add_u32 s40, s40, 4096\n"
  "s_addc_u32 s41, s41, 0\n"
  "s_add_u32 m0, m0, 1024\n"
  "s_nop 0\n"
  "global_load_lds_dwordx4 v118, s[40:41]\n"
  "s_add_u32 s40, s40, 4096\n"
  "s_addc_u32 s41, s41, 0\n"
  "s_add_u32 m0, m0, 1024\n"
  "s_nop 0\n"
  "global_load_lds_dwordx4 v118, s[40:41]\n"
  "s_add_u32 s40, s40, 4096\n"
  "s_addc_u32 s41, s41, 0\n"
  "s_add_u32 m0, m0, 1024\n"
  "s_nop 0\n"
  "global_load_lds_dwordx4 v118, s[40:41]\n"
  "s_add_u32 s40, s40, 4096\n"
  "s_addc_u32 s41, s41, 0\n"
  "s_add_u32 m0, m0, 1024\n"
  "s_nop 0\n"
  "global_load_lds_dwordx4 v118, s[40:41]\n"
  "s_add_u32 s40, s40, 4096\n"
  "s_addc_u32 s41, s41, 0\n"
  "s_add_u32 m0, m0, 1024\n"
  "s_nop 0\n"
  "global_load_lds_dwordx4 v118, s[40:41]\n"
  "s_add_u32 s40, s40, 4096\n"
  "s_addc_u32 s41, s41, 0\n"
  "s_waitcnt vmcnt(24)\n"
  "ds_read_b64_tr_b16 a[64:65], v119 offset:16384\n"
  "ds_read_b64_tr_b16 a[66:67], v119 offset:17408\n"
  "ds_read_b64_tr_b16 a[68:69], v119 offset:16416\n"
  "ds_read_b64_tr_b16 a[70:71], v119 offset:17440\n"
  "ds_read_b64_tr_b16 a[72:73], v119 offset:16448\n"
  "ds_read_b64_tr_b16 a[74:75], v119 offset:17472\n"
  "ds_read_b64_tr_b16 a[76:77], v119 offset:16480\n"
  "ds_read_b64_tr_b16 a[78:79], v119 offset:17504\n"
  "ds_read_b64_tr_b16 a[80:81], v119 offset:16512\n"
  "ds_read_b64_tr_b16 a[82:83], v119 offset:17536\n"
  "ds_read_b64_tr_b16 a[84:85], v119 offset:16544\n"
  "ds_read_b64_tr_b16 a[86:87], v119 offset:17568\n"
  "ds_read_b64_tr_b16 a[88:89], v119 offset:16576\n"
  "ds_read_b64_tr_b16 a[90:91], v119 offset:17600\n"
  "ds_read_b64_tr_b16 a[92:93], v119 offset:16608\n"
  "ds_read_b64_tr_b16 a[94:95], v119 offset:17632\n"
  "s_waitcnt lgkmcnt(0)\n"
  "s_add_u32 m0, s58, 16384\n"
  "s_nop 0\n"
  "global_load_lds_dwordx4 v118, s[40:41]\n"
  "s_add_u32 s40, s40, 4096\n"
  "s_addc_u32 s41, s41, 0\n"
  "s_add_u32 m0, m0, 1024\n"
  "s_nop 0\n"
  "global_load_lds_dwordx4 v118, s[40:41]\n"
  "s_add_u32 s40, s40, 4096\n"
  "s_addc_u32 s41, s41, 0\n"
  "s_add_u32 m0, m0, 1024\n"
  "s_nop 0\n"
  "global_load_lds_dwordx4 v118, s[40:41]\n"
  "s_add_u32 s40, s40, 4096\n"
  "s_addc_u32 s41, s41, 0\n"
  "s_add_u32 m0, m0, 1024\n"
  "s_nop 0\n"
  "global_load_lds_dwordx4 v118, s[40:41]\n"
  "s_add_u32 s40, s40, 4096\n"
  "s_addc_u32 s41, s41, 0\n"
  "s_add_u32 m0, m0, 1024\n"
  "s_nop 0\n"
  "global_load_lds_dwordx4 v118, s[40:41]\n"
  "s_add_u32 s40, s40, 4096\n"
  "s_addc_u32 s41, s41, 0\n"
  "s_add_u32 m0, m0, 1024\n"
  "s_nop 0\n"
  "global_load_lds_dwordx4 v118, s[40:41]\n"
  "s_add_u32 s40, s40, 4096\n"
  "s_addc_u32 s41, s41, 0\n"
  "s_add_u32 m0, m0, 1024\n"
  "s_nop 0\n"
  "global_load_lds_dwordx4 v118, s[40:41]\n"
  "s_add_u32 s40, s40, 4096\n"
  "s_addc_u32 s41, s41, 0\n"
  "s_add_u32 m0, m0, 1024\n"
  "s_nop 0\n"
  "global_load_lds_dwordx4 v118, s[40:41]\n"
  "s_add_u32 s40, s40, 4096\n"
  "s_addc_u32 s41, s41, 0\n"
  "s_waitcnt vmcnt(24)\n"
  "ds_read_b64_tr_b16 a[96:97], v119 offset:24576\n"
  "ds_read_b64_tr_b16 a[98:99], v119 offset:25600\n"
  "ds_read_b64_tr_b16 a[100:101], v119 offset:24608\n"
  "ds_read_b64_tr_b16 a[102:103], v119 offset:25632\n"
  "ds_read_b64_tr_b16 a[104:105], v119 offset:24640\n"
  "ds_read_b64_tr_b16 a[106:107], v119 offset:25664\n"
  "ds_read_b64_tr_b16 a[108:109], v119 offset:24672\n"
  "ds_read_b64_tr_b16 a[110:111], v119 offset:25696\n"
  "ds_read_b64_tr_b16 a[112:113], v119 offset:24704\n"
  "ds_read_b64_tr_b16 a[114:115], v119 offset:25728\n"
  "ds_read_b64_tr_b16 a[116:117], v119 offset:24736\n"
  "ds_read_b64_tr_b16 a[118:119], v119 offset:25760\n"
  "ds_read_b64_tr_b16 a[120:121], v119 offset:24768\n"
  "ds_read_b64_tr_b16 a[122:123], v119 offset:25792\n"
  "ds_read_b64_tr_b16 a[124:125], v119 offset:24800\n"
  "ds_read_b64_tr_b16 a[126:127], v119 offset:25824\n"
  "s_waitcnt lgkmcnt(0)\n"
  "s_add_u32 m0, s58, 24576\n"
  "s_nop 0\n"
  "global_load_lds_dwordx4 v118, s[40:41]\n"
  "s_add_u32 s40, s40, 4096\n"
  "s_addc_u32 s41, s41, 0\n"
  "s_add_u32 m0, m0, 1024\n"
  "s_nop 0\n"
  "global_load_lds_dwordx4 v118, s[40:41]\n"
  "s_add_u32 s40, s40, 4096\n"
  "s_addc_u32 s41, s41, 0\n"
  "s_add_u32 m0, m0, 1024\n"
  "s_nop 0\n"
  "global_load_lds_dwordx4 v118, s[40:41]\n"
  "s_add_u32 s40, s40, 4096\n"
  "s_addc_u32 s41, s41, 0\n"
  "s_add_u32 m0, m0, 1024\n"
  "s_nop 0\n"
  "global_load_lds_dwordx4 v118, s[40:41]\n"
  "s_add_u32 s40, s40, 4096\n"
  "s_addc_u32 s41, s41, 0\n"
  "s_add_u32 m0, m0, 1024\n"
  "s_nop 0\n"
  "global_load_lds_dwordx4 v118, s[40:41]\n"
  "s_add_u32 s40, s40, 4096\n"
  "s_addc_u32 s41, s41, 0\n"
  "s_add_u32 m0, m0, 1024\n"
  "s_nop 0\n"
  "global_load_lds_dwordx4 v118, s[40:41]\n"
  "s_add_u32 s40, s40, 4096\n"
  "s_addc_u32 s41, s41, 0\n"
  "s_add_u32 m0, m0, 1024\n"
  "s_nop 0\n"
  "global_load_lds_dwordx4 v118, s[40:41]\n"
  "s_add_u32 s40, s40, 4096\n"
  "s_addc_u32 s41, s41, 0\n"
  "s_add_u32 m0, m0, 1024\n"
  "s_nop 0\n"
  "global_load_lds_dwordx4 v118, s[40:41]\n"
  "s_add_u32 s40, s40, 4096\n"
  "s_addc_u32 s41, s41, 0\n"
  "s_waitcnt vmcnt(24)\n"
  "ds_read_b64_tr_b16 a[128:129], v119 offset:0\n"
  "ds_read_b64_tr_b16 a[130:131], v119 offset:1024\n"
  "ds_read_b64_tr_b16 a[132:133], v119 offset:32\n"
  "ds_read_b64_tr_b16 a[134:135], v119 offset:1056\n"
  "ds_read_b64_tr_b16 a[136:137], v119 offset:64\n"
  "ds_read_b64_tr_b16 a[138:139], v119 offset:1088\n"
  "ds_read_b64_tr_b16 a[140:141], v119 offset:96\n"
  "ds_read_b64_tr_b16 a[142:143], v119 offset:1120\n"
  "ds_read_b64_tr_b16 a[144:145], v119 offset:128\n"
  "ds_read_b64_tr_b16 a[146:147], v119 offset:1152\n"
  "ds_read_b64_tr_b16 a[148:149], v119 offset:160\n"
  "ds_read_b64_tr_b16 a[150:151], v119 offset:1184\n"
  "ds_read_b64_tr_b16 a[152:153], v119 offset:192\n"
  "ds_read_b64_tr_b16 a[154:155], v119 offset:1216\n"
  "ds_read_b64_tr_b16 a[156:157], v119 offset:224\n"
  "ds_read_b64_tr_b16 a[158:159], v119 offset:1248\n"
  "s_waitcnt lgkmcnt(0)\n"
  "s_waitcnt vmcnt(16)\n"
  "ds_read_b64_tr_b16 a[160:161], v119 offset:8192\n"
  "ds_read_b64_tr_b16 a[162:163], v119 offset:9216\n"
  "ds_read_b64_tr_b16 a[164:165], v119 offset:8224\n"
  "ds_read_b64_tr_b16 a[166:167], v119 offset:9248\n"
  "ds_read_b64_tr_b16 a[168:169], v119 offset:8256\n"
  "ds_read_b64_tr_b16 a[170:171], v119 offset:9280\n"
  "ds_read_b64_tr_b16 a[172:173], v119 offset:8288\n"
  "ds_read_b64_tr_b16 a[174:175], v119 offset:9312\n"
  "ds_read_b64_tr_b16 a[176:177], v119 offset:8320\n"
  "ds_read_b64_tr_b16 a[178:179], v119 offset:9344\n"
  "ds_read_b64_tr_b16 a[180:181], v119 offset:8352\n"
  "ds_read_b64_tr_b16 a[182:183], v119 offset:9376\n"
  "ds_read_b64_tr_b16 a[184:185], v119 offset:8384\n"
  "ds_read_b64_tr_b16 a[186:187], v119 offset:9408\n"
  "ds_read_b64_tr_b16 a[188:189], v119 offset:8416\n"
  "ds_read_b64_tr_b16 a[190:191], v119 offset:9440\n"
  "s_waitcnt lgkmcnt(0)\n"
  "s_waitcnt vmcnt(8)\n"
  "ds_read_b64_tr_b16 a[192:193], v119 offset:16384\n"
  "ds_read_b64_tr_b16 a[194:195], v119 offset:17408\n"
  "ds_read_b64_tr_b16 a[196:197], v119 offset:16416\n"
  "ds_read_b64_tr_b16 a[198:199], v119 offset:17440\n"
  "ds_read_b64_tr_b16 a[200:201], v119 offset:16448\n"
  "ds_read_b64_tr_b16 a[202:203], v119 offset:17472\n"
  "ds_read_b64_tr_b16 a[204:205], v119 offset:16480\n"
  "ds_read_b64_tr_b16 a[206:207], v119 offset:17504\n"
  "ds_read_b64_tr_b16 a[208:209], v119 offset:16512\n"
  "ds_read_b64_tr_b16 a[210:211], v119 offset:17536\n"
  "ds_read_b64_tr_b16 a[212:213], v119 offset:16544\n"
  "ds_read_b64_tr_b16 a[214:215], v119 offset:17568\n"
  "ds_read_b64_tr_b16 a[216:217], v119 offset:16576\n"
  "ds_read_b64_tr_b16 a[218:219], v119 offset:17600\n"
  "ds_read_b64_tr_b16 a[220:221], v119 offset:16608\n"
  "ds_read_b64_tr_b16 a[222:223], v119 offset:17632\n"
  "s_waitcnt lgkmcnt(0)\n"
  "s_waitcnt vmcnt(0)\n"
  "ds_read_b64_tr_b16 a[224:225], v119 offset:24576\n"
  "ds_read_b64_tr_b16 a[226:227], v119 offset:25600\n"
  "ds_read_b64_tr_b16 a[228:229], v119 offset:24608\n"
  "ds_read_b64_tr_b16 a[230:231], v119 offset:25632\n"
  "ds_read_b64_tr_b16 a[232:233], v119 offset:24640\n"
  "ds_read_b64_tr_b16 a[234:235], v119 offset:25664\n"
  "ds_read_b64_tr_b16 a[236:237], v119 offset:24672\n"
  "ds_read_b64_tr_b16 a[238:239], v119 offset:25696\n"
  "ds_read_b64_tr_b16 a[240:241], v119 offset:24704\n"
  "ds_read_b64_tr_b16 a[242:243], v119 offset:25728\n"
  "ds_read_b64_tr_b16 a[244:245], v119 offset:24736\n"
  "ds_read_b64_tr_b16 a[246:247], v119 offset:25760\n"
  "ds_read_b64_tr_b16 a[248:249], v119 offset:24768\n"
  "ds_read_b64_tr_b16 a[250:251], v119 offset:25792\n"
  "ds_read_b64_tr_b16 a[252:253], v119 offset:24800\n"
  "ds_read_b64_tr_b16 a[254:255], v119 offset:25824\n"
  "s_waitcnt lgkmcnt(0)\n"
  "s_waitcnt lgkmcnt(0)\n"
  "s_barrier\n"
  "v_mov_b32 v117, 0\n"
  "s_memtime s[64:65]\n"
  "s_waitcnt lgkmcnt(0)\n"
  "s_mov_b32 s56, s64\n"
  "v_mov_b32 v128, -1\n"
  "v_mov_b32 v129, -1\n"
  "v_mov_b32 v130, -1\n"
  "v_mov_b32 v131, -1\n"
  "s_lshr_b32 s53, s51, 1\n"
  "s_xor_b32 s53, s53, s50\n"
  "s_and_b32 s53, s53, 1\n"
  "s_lshl_b32 s53, s53, 13\n"
  "s_and_b32 s61, s51, 31\n"
  "s_lshl_b32 s61, s61, 20\n"
  "s_add_u32 s61, s61, s62\n"
  "s_or_b32 s61, s61, s53\n"
  "buffer_store_dwordx4 v[128:131], v112, s[36:39], s61 offen offset:0 sc1\n"
  ".Lop_loop:\n"
  "s_add_u32 s55, s51, 1\n"
  "v_cmp_gt_u32 vcc, s55, v117\n"
  "s_lshr_b32 s53, s51, 1\n"
  "s_xor_b32 s53, s53, s50\n"
  "s_and_b32 s53, s53, 1\n"
  "s_lshl_b32 s53, s53, 13\n"
  "s_and_b32 s61, s51, 31\n"
  "s_lshl_b32 s61, s61, 20\n"
  "s_add_u32 s61, s61, s62\n"
  "s_or_b32 s61, s61, s53\n"
  "s_add_u32 s60, s59, 16384\n"
  "s_cbranch_vccz .Lop_go\n"
  ".Lop_p0:\n"
  "s_memtime s[64:65]\n"
  "s_waitcnt lgkmcnt(0)\n"
  "s_sub_u32 s53, s64, s56\n"
  "s_cmp_lt_i32 s53, 0\n"
  "s_cbranch_scc0 .Lop_p1\n"
  "s_sleep 1\n"
  "s_branch .Lop_p0\n"
  ".Lop_p1:\n"
  "buffer_load_dword v117, v115, s[36:39], 0 offen sc1\n"
  "s_memtime s[64:65]\n"
  "s_sub_u32 s52, s52, 1\n"
  "s_cbranch_scc1 .Lop_end\n"
  "s_waitcnt lgkmcnt(0)\n"
  "s_add_u32 s56, s64, 3400\n"
  "s_waitcnt vmcnt(0)\n"
  "v_cmp_gt_u32 vcc, s55, v117\n"
  "s_cbranch_vccz .Lop_go\n"
  "s_sleep 1\n"
  "s_cmp_eq_u32 s51, 0\n"
  "s_cselect_b32 s53, 0, s52\n"
  "s_and_b32 s53, s53, 15\n"
  "s_cbranch_scc1 .Lop_p1\n"
  "s_sub_u32 s53, s66, s52\n"
  "v_mov_b32 v120, s53\n"
  "s_mov_b64 exec, 1\n"
  "buffer_store_dword v120, off, s[36:39], s63 sc1\n"
  "s_mov_b64 exec, -1\n"
  "s_branch .Lop_p1\n"
  ".Lop_go:\n"
  "buffer_load_dwordx4 v[0:3], v112, s[36:39], s59 offen offset:0 sc1\n"
  "buffer_load_dwordx4 v[4:7], v112, s[36:39], s59 offen offset:1024 sc1\n"
  "buffer_load_dwordx4 v[8:11], v112, s[36:39], s59 offen offset:2048 sc1\n"
  "buffer_load_dwordx4 v[12:15], v112, s[36:39], s59 offen offset:3072 sc1\n"
  "buffer_load_dwordx4 v[16:19], v112, s[36:39], s60 offen offset:0 sc1\n"
  "buffer_load_dwordx4 v[20:23], v112, s[36:39], s60 offen offset:1024 sc1\n"
  "buffer_load_dwordx4 v[24:27], v112, s[36:39], s60 offen offset:2048 sc1\n"
  "buffer_load_dwordx4 v[28:31], v112, s[36:39], s60 offen offset:3072 sc1\n"
  "s_add_u32 s59, s59, 0x200000\n"
  "s_xor_b32 s59, s59, 8192\n"
  "s_bitset0_b32 s59, 26\n"
  "s_waitcnt vmcnt(7)\n"
  "v_mfma_f32_16x16x32_bf16 v[32:35], a[0:3], v[0:3], 0\n"
  "v_mfma_f32_16x16x32_bf16 v[36:39], a[4:7], v[0:3], 0\n"
  "v_mfma_f32_16x16x32_bf16 v[40:43], a[8:11], v[0:3], 0\n"
  "v_mfma_f32_16x16x32_bf16 v[44:47], a[12:15], v[0:3], 0\n"
  "v_mfma_f32_16x16x32_bf16 v[48:51], a[16:19], v[0:3], 0\n"
  "v_mfma_f32_16x16x32_bf16 v[52:55], a[20:23], v[0:3], 0\n"
  "v_mfma_f32_16x16x32_bf16 v[56:59], a[24:27], v[0:3], 0\n"
  "v_mfma_f32_16x16x32_bf16 v[60:63], a[28:31], v[0:3], 0\n"
  "s_waitcnt vmcnt(6)\n"
  "v_mfma_f32_16x16x32_bf16 v[32:35], a[32:35], v[4:7], v[32:35]\n"
  "v_mfma_f32_16x16x32_bf16 v[36:39], a[36:39], v[4:7], v[36:39]\n"
  "v_mfma_f32_16x16x32_bf16 v[40:43], a[40:43], v[4:7], v[40:43]\n"
  "v_mfma_f32_16x16x32_bf16 v[44:47], a[44:47], v[4:7], v[44:47]\n"
  "v_mfma_f32_16x16x32_bf16 v[48:51], a[48:51], v[4:7], v[48:51]\n"
  "v_mfma_f32_16x16x32_bf16 v[52:55], a[52:55], v[4:7], v[52:55]\n"
  "v_mfma_f32_16x16x32_bf16 v[56:59], a[56:59], v[4:7], v[56:59]\n"
  "v_mfma_f32_16x16x32_bf16 v[60:63], a[60:63], v[4:7], v[60:63]\n"
  "s_waitcnt vmcnt(5)\n"
  "v_mfma_f32_16x16x32_bf16 v[32:35], a[64:67], v[8:11], v[32:35]\n"
  "v_mfma_f32_16x16x32_bf16 v[36:39], a[68:71], v[8:11], v[36:39]\n"
  "v_mfma_f32_16x16x32_bf16 v[40:43], a[72:75], v[8:11], v[40:43]\n"
  "v_mfma_f32_16x16x32_bf16 v[44:47], a[76:79], v[8:11], v[44:47]\n"
  "v_mfma_f32_16x16x32_bf16 v[48:51], a[80:83], v[8:11], v[48:51]\n"
  "v_mfma_f32_16x16x32_bf16 v[52:55], a[84:87], v[8:11], v[52:55]\n"
  "v_mfma_f32_16x16x32_bf16 v[56:59], a[88:91], v[8:11], v[56:59]\n"
  "v_mfma_f32_16x16x32_bf16 v[60:63], a[92:95], v[8:11], v[60:63]\n"
  "s_waitcnt vmcnt(4)\n"
  "v_mfma_f32_16x16x32_bf16 v[32:35], a[96:99], v[12:15], v[32:35]\n"
  "v_mfma_f32_16x16x32_bf16 v[36:39], a[100:103], v[12:15], v[36:39]\n"
  "v_mfma_f32_16x16x32_bf16 v[40:43], a[104:107], v[12:15], v[40:43]\n"
  "v_mfma_f32_16x16x32_bf16 v[44:47], a[108:111], v[12:15], v[44:47]\n"
  "v_mfma_f32_16x16x32_bf16 v[48:51], a[112:115], v[12:15], v[48:51]\n"
  "v_mfma_f32_16x16x32_bf16 v[52:55], a[116:119], v[12:15], v[52:55]\n"
  "v_mfma_f32_16x16x32_bf16 v[56:59], a[120:123], v[12:15], v[56:59]\n"
  "v_mfma_f32_16x16x32_bf16 v[60:63], a[124:127], v[12:15], v[60:63]\n"
  "s_waitcnt vmcnt(3)\n"
  "v_mfma_f32_16x16x32_bf16 v[32:35], a[128:131], v[16:19], v[32:35]\n"
  "s_waitcnt vmcnt(2)\n"
  "v_mfma_f32_16x16x32_bf16 v[32:35], a[160:163], v[20:23], v[32:35]\n"
  "s_waitcnt vmcnt(1)\n"
  "v_mfma_f32_16x16x32_bf16 v[32:35], a[192:195], v[24:27], v[32:35]\n"
  "s_waitcnt vmcnt(0)\n"
  "v_mfma_f32_16x16x32_bf16 v[32:35], a[224:227], v[28:31], v[32:35]\n"
  "s_nop 2\n"
  "v_mfma_f32_16x16x32_bf16 v[36:39], a[132:135], v[16:19], v[36:39]\n"
  "s_nop 2\n"
  "v_mfma_f32_16x16x32_bf16 v[36:39], a[164:167], v[20:23], v[36:39]\n"
  "ds_write_b128 v113, v[32:35] offset:0\n"
  "v_mfma_f32_16x16x32_bf16 v[36:39], a[196:199], v[24:27], v[36:39]\n"
  "v_mfma_f32_16x16x32_bf16 v[36:39], a[228:231], v[28:31], v[36:39]\n"
  "s_nop 2\n"
  "v_mfma_f32_16x16x32_bf16 v[40:43], a[136:139], v[16:19], v[40:43]\n"
  "s_nop 2\n"
  "v_mfma_f32_16x16x32_bf16 v[40:43], a[168:171], v[20:23], v[40:43]\n"
  "ds_write_b128 v113, v[36:39] offset:1024\n"
  "v_mfma_f32_16x16x32_bf16 v[40:43], a[200:203], v[24:27], v[40:43]\n"
  "v_mfma_f32_16x16x32_bf16 v[40:43], a[232:235], v[28:31], v[40:43]\n"
  "s_nop 2\n"
  "v_mfma_f32_16x16x32_bf16 v[44:47], a[140:143], v[16:19], v[44:47]\n"
  "s_nop 2\n"
  "v_mfma_f32_16x16x32_bf16 v[44:47], a[172:175], v[20:23], v[44:47]\n"
  "ds_write_b128 v113, v[40:43] offset:2048\n"
  "v_mfma_f32_16x16x32_bf16 v[44:47], a[204:207], v[24:27], v[44:47]\n"
  "v_mfma_f32_16x16x32_bf16 v[44:47], a[236:239], v[28:31], v[44:47]\n"
  "s_nop 2\n"
  "v_mfma_f32_16x16x32_bf16 v[48:51], a[144:147], v[16:19], v[48:51]\n"
  "s_nop 2\n"
  "v_mfma_f32_16x16x32_bf16 v[48:51], a[176:179], v[20:23], v[48:51]\n"
  "ds_write_b128 v113, v[44:47] offset:3072\n"
  "v_mfma_f32_16x16x32_bf16 v[48:51], a[208:211], v[24:27], v[48:51]\n"
  "v_mfma_f32_16x16x32_bf16 v[48:51], a[240:243], v[28:31], v[48:51]\n"
  "s_nop 0\n"
  "v_mov_b32 v123, s55\n"
  "buffer_store_dword v123, v116, s[36:39], 0 offen sc1\n"
  "v_mfma_f32_16x16x32_bf16 v[52:55], a[148:151], v[16:19], v[52:55]\n"
  "s_nop 2\n"
  "v_mfma_f32_16x16x32_bf16 v[52:55], a[180:183], v[20:23], v[52:55]\n"
  "ds_write_b128 v113, v[48:51] offset:4096\n"
  "v_mfma_f32_16x16x32_bf16 v[52:55], a[212:215], v[24:27], v[52:55]\n"
  "v_mfma_f32_16x16x32_bf16 v[52:55], a[244:247], v[28:31], v[52:55]\n"
  "s_nop 2\n"
  "v_mfma_f32_16x16x32_bf16 v[56:59], a[152:155], v[16:19], v[56:59]\n"
  "s_nop 2\n"
  "v_mfma_f32_16x16x32_bf16 v[56:59], a[184:187], v[20:23], v[56:59]\n"
  "ds_write_b128 v113, v[52:55] offset:5120\n"
  "v_mfma_f32_16x16x32_bf16 v[56:59], a[216:219], v[24:27], v[56:59]\n"
  "v_mfma_f32_16x16x32_bf16 v[56:59], a[248:251], v[28:31], v[56:59]\n"
  "s_nop 2\n"
  "v_mfma_f32_16x16x32_bf16 v[60:63], a[156:159], v[16:19], v[60:63]\n"
  "s_nop 2\n"
  "v_mfma_f32_16x16x32_bf16 v[60:63], a[188:191], v[20:23], v[60:63]\n"
  "ds_write_b128 v113, v[56:59] offset:6144\n"
  "s_nop 0\n"
  "v_mfma_f32_16x16x32_bf16 v[60:63], a[220:223], v[24:27], v[60:63]\n"
  "s_nop 2\n"
  "v_mfma_f32_16x16x32_bf16 v[60:63], a[252:255], v[28:31], v[60:63]\n"
  "s_nop 7\n"
  "ds_write_b128 v113, v[60:63] offset:7168\n"
  "s_waitcnt lgkmcnt(0)\n"
  "s_barrier\n"
  "ds_read_b128 v[64:67], v114 offset:0\n"
  "ds_read_b128 v[68:71], v114 offset:1024\n"
  "ds_read_b128 v[72:75], v114 offset:8192\n"
  "ds_read_b128 v[76:79], v114 offset:9216\n"
  "ds_read_b128 v[80:83], v114 offset:16384\n"
  "ds_read_b128 v[84:87], v114 offset:17408\n"
  "ds_read_b128 v[88:91], v114 offset:24576\n"
  "ds_read_b128 v[92:95], v114 offset:25600\n"
  "v_xor_b32 v113, 0x8000, v113\n"
  "v_xor_b32 v114, 0x8000, v114\n"
  "s_waitcnt lgkmcnt(5)\n"
  "v_pk_add_f32 v[64:65], v[64:65], v[72:73]\n"
  "v_pk_add_f32 v[66:67], v[66:67], v[74:75]\n"
  "s_waitcnt lgkmcnt(4)\n"
  "v_pk_add_f32 v[68:69], v[68:69], v[76:77]\n"
  "v_pk_add_f32 v[70:71], v[70:71], v[78:79]\n"
  "s_waitcnt lgkmcnt(3)\n"
  "v_pk_add_f32 v[64:65], v[64:65], v[80:81]\n"
  "v_pk_add_f32 v[66:67], v[66:67], v[82:83]\n"
  "s_waitcnt lgkmcnt(2)\n"
  "v_pk_add_f32 v[68:69], v[68:69], v[84:85]\n"
  "v_pk_add_f32 v[70:71], v[70:71], v[86:87]\n"
  "s_waitcnt lgkmcnt(1)\n"
  "v_pk_add_f32 v[64:65], v[64:65], v[88:89]\n"
  "v_pk_add_f32 v[66:67], v[66:67], v[90:91]\n"
  "s_waitcnt lgkmcnt(0)\n"
  "v_pk_add_f32 v[68:69], v[68:69], v[92:93]\n"
  "v_pk_add_f32 v[70:71], v[70:71], v[94:95]\n"
  "v_cvt_pk_bf16_f32 v64, v64, v65\n"
  "v_cvt_pk_bf16_f32 v65, v66, v67\n"
  "v_cvt_pk_bf16_f32 v66, v68, v69\n"
  "v_cvt_pk_bf16_f32 v67, v70, v71\n"
  "buffer_store_dwordx4 v[64:67], v112, s[36:39], s61 offen offset:0 sc1\n"
  "s_add_u32 s51, s51, 1\n"
  "s_sleep 12\n"
  "s_lshr_b32 s53, s51, 1\n"
  "s_xor_b32 s53, s53, s50\n"
  "s_and_b32 s53, s53, 1\n"
  "s_lshl_b32 s53, s53, 13\n"
  "s_and_b32 s61, s51, 31\n"
  "s_lshl_b32 s61, s61, 20\n"
  "s_add_u32 s61, s61, s62\n"
  "s_or_b32 s61, s61, s53\n"
  "buffer_store_dwordx4 v[128:131], v112, s[36:39], s61 offen offset:0 sc1\n"
  "s_cmp_lt_u32 s51, 64\n"
  "s_cbranch_scc1 .Lop_loop\n"
  ".Lop_end:\n"
  "s_endpgm\n"
  ".Lop_lim:\n"
      :: "{v112}"(lane * 16), "{v113}"(w * 8192 + g * 256 + ((li ^ (4 * g)) << 4)), "{v114}"(g * 2048 + q * 256 + (((4 * w + (li >> 2)) ^ (4 * q)) << 4)),
        "{v115}"(fl(l, FL_O + (8 * kq + 2 * w + (lane & 1)) * 2 + th)), "{v116}"(lane == 0 ? fl(l, FL_OP + (th * 4 + w) * 16 + kq * 4 + n4) : OOR),
        "{v118}"(g * 1024 + li * 16), "{v119}"(w * 32768 + (8 * g + (li >> 2)) * 256 + q * 8),
        "{s[40:41]}"(PTR(Wo, RFL((8 * kq + 2 * w) * 131072 + 256 * n4))), "{s58}"(RFL(w * 32768)), "{s59}"(RFL(aO(0, l) + (8 * kq + 2 * w) * 16384 + th * 4096)),
        "{s62}"(RFL((128u << 20) + 720896 + (kq * 4 + n4) * 16384 + (th * 4 + w) * 1024)), "{s63}"(RFL(BID == MB ? HS : OOR)), "{s66}"(RFL(v0 + 1 + BUDGET)), COMMON_IN NORET;
}

DEV u4v lnorm(u4v x) {
  float v[8] = {blo(x.x), bhi(x.x), blo(x.y), bhi(x.y), blo(x.z), bhi(x.z), blo(x.w), bhi(x.w)};
  float s = ((v[0] + v[1]) + (v[2] + v[3])) + ((v[4] + v[5]) + (v[6] + v[7]));
  const float m = sum_wave(s) * (1.f / 512);
  float q = 0;
#pragma unroll
  for (int j = 0; j < 8; ++j) { v[j] -= m; q = fmaf(v[j], v[j], q); }
  const float r = __builtin_amdgcn_rsqf(sum_wave(q) * (1.f / 512) + 1e-5f);
  return u4v{pk(v[0] * r, v[1] * r), pk(v[2] * r, v[3] * r), pk(v[4] * r, v[5] * r), pk(v[6] * r, v[7] * r)};
}
DEV bool reduce_row(Ctx& C, u32 ra, const u32* o, u4v sel, u4v& y) {
  const u4v res = ld128(C.ws, ra);
  u4v d[6][2];
#pragma unroll
  for (int q = 0; q < 6; ++q) { MEMB(); d[q][0] = ld128(C.ws, o[q]); MEMB(); d[q][1] = ld128(C.ws, o[q] + 512); }
  MEMB();
  f4 a0 = {blo(res.x), bhi(res.x), blo(res.y), bhi(res.y)}, a1 = {blo(res.z), bhi(res.z), blo(res.w), bhi(res.w)};
  u32 m = 0;
#pragma unroll
  for (int q = 0; q < 6; ++q) { a0 = mf(d[q][0], sel, a0); a1 = mf(d[q][1], sel, a1); m = max3u(max3u(m, d[q][0].x, d[q][0].z), d[q][1].x, d[q][1].z); SB(); }
  y = pk8(a0, a1);
  return !(HSM & 4) || __builtin_amdgcn_ballot_w64(m == ~0u) == 0;
}
DEV u4v mksel(int lane) {
  const int j = (lane & 15) - 8 * (lane >> 5);
  u4v sel;
#pragma unroll
  for (int d = 0; d < 4; ++d) sel[d] = (j == 2 * d) ? 0x3f80u : (j == 2 * d + 1) ? 0x3f800000u : 0u;
  return sel;
}
DEV u4v ldnat(const char* p) { u2v a = *(const u2v*)p, b = *(const u2v*)(p + 512); return u4v{a.x, a.y, b.x, b.y}; }

DEV void role_ffn(Ctx& C, char* ws, u32 l, int f, int th, int w, int lane, const u16* Wg, const u16* Wu, const u16* Wd) {
  const int g = lane >> 4, li = lane & 15;
  const bool valid = (128 * f + 32 * w) < 1376;
  const u32 c0 = RFL(valid ? 256 * f + 64 * w : 0);
  u4v xa; i4v ro;
#pragma unroll
  for (int k = 0; k < 4; ++k) { xa[k] = li * 1024 + (((4 * k + g) ^ li) << 4); const int r = 128 * f + 32 * (w ^ k); ro[k] = RFL(r < 1376 ? r * 1024 : 0); }
  asm volatile(
  "s_nop 4\n"
  "s_add_u32 m0, s58, 0\n"
  "s_nop 0\n"
  "global_load_lds_dwordx4 v107, s[40:41]\n"
  "s_add_u32 s40, s40, 44032\n"
  "s_addc_u32 s41, s41, 0\n"
  "s_add_u32 m0, m0, 1024\n"
  "s_nop 0\n"
  "global_load_lds_dwordx4 v107, s[40:41]\n"
  "s_add_u32 s40, s40, 44032\n"
  "s_addc_u32 s41, s41, 0\n"
  "s_add_u32 m0, m0, 1024\n"
  "s_nop 0\n"
  "global_load_lds_dwordx4 v107, s[40:41]\n"
  "s_add_u32 s40, s40, 44032\n"
  "s_addc_u32 s41, s41, 0\n"
  "s_add_u32 m0, m0, 1024\n"
  "s_nop 0\n"
  "global_load_lds_dwordx4 v107, s[40:41]\n"
  "s_add_u32 s40, s40, 44032\n"
  "s_addc_u32 s41, s41, 0\n"
  "s_add_u32 m0, m0, 1024\n"
  "s_nop 0\n"
  "global_load_lds_dwordx4 v107, s[40:41]\n"
  "s_add_u32 s40, s40, 44032\n"
  "s_addc_u32 s41, s41, 0\n"
  "s_add_u32 m0, m0, 1024\n"
  "s_nop 0\n"
  "global_load_lds_dwordx4 v107, s[40:41]\n"
  "s_add_u32 s40, s40, 44032\n"
  "s_addc_u32 s41, s41, 0\n"
  "s_add_u32 m0, m0, 1024\n"
  "s_nop 0\n"
  "global_load_lds_dwordx4 v107, s[40:41]\n"
  "s_add_u32 s40, s40, 44032\n"
  "s_addc_u32 s41, s41, 0\n"
  "s_add_u32 m0, m0, 1024\n"
  "s_nop 0\n"
  "global_load_lds_dwordx4 v107, s[40:41]\n"
  "s_add_u32 s40, s40, 44032\n"
  "s_addc_u32 s41, s41, 0\n"
  "s_add_u32 m0, s58, 8192\n"
  "s_nop 0\n"
  "global_load_lds_dwordx4 v107, s[40:41]\n"
  "s_add_u32 s40, s40, 44032\n"
  "s_addc_u32 s41, s41, 0\n"
  "s_add_u32 m0, m0, 1024\n"
  "s_nop 0\n"
  "global_load_lds_dwordx4 v107, s[40:41]\n"
  "s_add_u32 s40, s40, 44032\n"
  "s_addc_u32 s41, s41, 0\n"
  "s_add_u32 m0, m0, 1024\n"
  "s_nop 0\n"
  "global_load_lds_dwordx4 v107, s[40:41]\n"
  "s_add_u32 s40, s40, 44032\n"
  "s_addc_u32 s41, s41, 0\n"
  "s_add_u32 m0, m0, 1024\n"
  "s_nop 0\n"
  "global_load_lds_dwordx4 v107, s[40:41]\n"
  "s_add_u32 s40, s40, 44032\n"
  "s_addc_u32 s41, s41, 0\n"
  "s_add_u32 m0, m0, 1024\n"
  "s_nop 0\n"
  "global_load_lds_dwordx4 v107, s[40:41]\n"
  "s_add_u32 s40, s40, 44032\n"
  "s_addc_u32 s41, s41, 0\n"
  "s_add_u32 m0, m0, 1024\n"
  "s_nop 0\n"
  "global_load_lds_dwordx4 v107, s[40:41]\n"
  "s_add_u32 s40, s40, 44032\n"
  "s_addc_u32 s41, s41, 0\n"
  "s_add_u32 m0, m0, 1024\n"
  "s_nop 0\n"
  "global_load_lds_dwordx4 v107, s[40:41]\n"
  "s_add_u32 s40, s40, 44032\n"
  "s_addc_u32 s41, s41, 0\n"
  "s_add_u32 m0, m0, 1024\n"
  "s_nop 0\n"
  "global_load_lds_dwordx4 v107, s[40:41]\n"
  "s_add_u32 s40, s40, 44032\n"
  "s_addc_u32 s41, s41, 0\n"
  "s_add_u32 m0, s58, 16384\n"
  "s_nop 0\n"
  "global_load_lds_dwordx4 v107, s[40:41]\n"
  "s_add_u32 s40, s40, 44032\n"
  "s_addc_u32 s41, s41, 0\n"
  "s_add_u32 m0, m0, 1024\n"
  "s_nop 0\n"
  "global_load_lds_dwordx4 v107, s[40:41]\n"
  "s_add_u32 s40, s40, 44032\n"
  "s_addc_u32 s41, s41, 0\n"
  "s_add_u32 m0, m0, 1024\n"
  "s_nop 0\n"
  "global_load_lds_dwordx4 v107, s[40:41]\n"
  "s_add_u32 s40, s40, 44032\n"
  "s_addc_u32 s41, s41, 0\n"
  "s_add_u32 m0, m0, 1024\n"
  "s_nop 0\n"
  "global_load_lds_dwordx4 v107, s[40:41]\n"
  "s_add_u32 s40, s40, 44032\n"
  "s_addc_u32 s41, s41, 0\n"
  "s_add_u32 m0, m0, 1024\n"
  "s_nop 0\n"
  "global_load_lds_dwordx4 v107, s[40:41]\n"
  "s_add_u32 s40, s40, 44032\n"
  "s_addc_u32 s41, s41, 0\n"
  "s_add_u32 m0, m0, 1024\n"
  "s_nop 0\n"
  "global_load_lds_dwordx4 v107, s[40:41]\n"
  "s_add_u32 s40, s40, 44032\n"
  "s_addc_u32 s41, s41, 0\n"
  "s_add_u32 m0, m0, 1024\n"
  "s_nop 0\n"
  "global_load_lds_dwordx4 v107, s[40:41]\n"
  "s_add_u32 s40, s40, 44032\n"
  "s_addc_u32 s41, s41, 0\n"
  "s_add_u32 m0, m0, 1024\n"
  "s_nop 0\n"
  "global_load_lds_dwordx4 v107, s[40:41]\n"
  "s_add_u32 s40, s40, 44032\n"
  "s_addc_u32 s41, s41, 0\n"
  "s_add_u32 m0, s58, 24576\n"
  "s_nop 0\n"
  "global_load_lds_dwordx4 v107, s[40:41]\n"
  "s_add_u32 s40, s40, 44032\n"
  "s_addc_u32 s41, s41, 0\n"
  "s_add_u32 m0, m0, 1024\n"
  "s_nop 0\n"
  "global_load_lds_dwordx4 v107, s[40:41]\n"
  "s_add_u32 s40, s40, 44032\n"
  "s_addc_u32 s41, s41, 0\n"
  "s_add_u32 m0, m0, 1024\n"
  "s_nop 0\n"
  "global_load_lds_dwordx4 v107, s[40:41]\n"
  "s_add_u32 s40, s40, 44032\n"
  "s_addc_u32 s41, s41, 0\n"
  "s_add_u32 m0, m0, 1024\n"
  "s_nop 0\n"
  "global_load_lds_dwordx4 v107, s[40:41]\n"
  "s_add_u32 s40, s40, 44032\n"
  "s_addc_u32 s41, s41, 0\n"
  "s_add_u32 m0, m0, 1024\n"
  "s_nop 0\n"
  "global_load_lds_dwordx4 v107, s[40:41]\n"
  "s_add_u32 s40, s40, 44032\n"
  "s_addc_u32 s41, s41, 0\n"
  "s_add_u32 m0, m0, 1024\n"
  "s_nop 0\n"
  "global_load_lds_dwordx4 v107, s[40:41]\n"
  "s_add_u32 s40, s40, 44032\n"
  "s_addc_u32 s41, s41, 0\n"
  "s_add_u32 m0, m0, 1024\n"
  "s_nop 0\n"
  "global_load_lds_dwordx4 v107, s[40:41]\n"
  "s_add_u32 s40, s40, 44032\n"
  "s_addc_u32 s41, s41, 0\n"
  "s_add_u32 m0, m0, 1024\n"
  "s_nop 0\n"
  "global_load_lds_dwordx4 v107, s[40:41]\n"
  "s_add_u32 s40, s40, 44032\n"
  "s_addc_u32 s41, s41, 0\n"
  "s_waitcnt vmcnt(24)\n"
  "ds_read_b64_tr_b16 a[0:1], v109 offset:0\n"
  "ds_read_b64_tr_b16 a[64:65], v109 offset:32\n"
  "ds_read_b64_tr_b16 a[4:5], v109 offset:1024\n"
  "ds_read_b64_tr_b16 a[68:69], v109 offset:1056\n"
  "ds_read_b64_tr_b16 a[16:17], v109 offset:2048\n"
  "ds_read_b64_tr_b16 a[80:81], v109 offset:2080\n"
  "ds_read_b64_tr_b16 a[20:21], v109 offset:3072\n"
  "ds_read_b64_tr_b16 a[84:85], v109 offset:3104\n"
  "ds_read_b64_tr_b16 a[32:33], v109 offset:4096\n"
  "ds_read_b64_tr_b16 a[96:97], v109 offset:4128\n"
  "ds_read_b64_tr_b16 a[36:37], v109 offset:5120\n"
  "ds_read_b64_tr_b16 a[100:101], v109 offset:5152\n"
  "ds_read_b64_tr_b16 a[48:49], v109 offset:6144\n"
  "ds_read_b64_tr_b16 a[112:113], v109 offset:6176\n"
  "ds_read_b64_tr_b16 a[52:53], v109 offset:7168\n"
  "ds_read_b64_tr_b16 a[116:117], v109 offset:7200\n"
  "s_waitcnt lgkmcnt(0)\n"
  "s_add_u32 m0, s58, 0\n"
  "s_nop 0\n"
  "global_load_lds_dwordx4 v107, s[42:43]\n"
  "s_add_u32 s42, s42, 44032\n"
  "s_addc_u32 s43, s43, 0\n"
  "s_add_u32 m0, m0, 1024\n"
  "s_nop 0\n"
  "global_load_lds_dwordx4 v107, s[42:43]\n"
  "s_add_u32 s42, s42, 44032\n"
  "s_addc_u32 s43, s43, 0\n"
  "s_add_u32 m0, m0, 1024\n"
  "s_nop 0\n"
  "global_load_lds_dwordx4 v107, s[42:43]\n"
  "s_add_u32 s42, s42, 44032\n"
  "s_addc_u32 s43, s43, 0\n"
  "s_add_u32 m0, m0, 1024\n"
  "s_nop 0\n"
  "global_load_lds_dwordx4 v107, s[42:43]\n"
  "s_add_u32 s42, s42, 44032\n"
  "s_addc_u32 s43, s43, 0\n"
  "s_add_u32 m0, m0, 1024\n"
  "s_nop 0\n"
  "global_load_lds_dwordx4 v107, s[42:43]\n"
  "s_add_u32 s42, s42, 44032\n"
  "s_addc_u32 s43, s43, 0\n"
  "s_add_u32 m0, m0, 1024\n"
  "s_nop 0\n"
  "global_load_lds_dwordx4 v107, s[42:43]\n"
  "s_add_u32 s42, s42, 44032\n"
  "s_addc_u32 s43, s43, 0\n"
  "s_add_u32 m0, m0, 1024\n"
  "s_nop 0\n"
  "global_load_lds_dwordx4 v107, s[42:43]\n"
  "s_add_u32 s42, s42, 44032\n"
  "s_addc_u32 s43, s43, 0\n"
  "s_add_u32 m0, m0, 1024\n"
  "s_nop 0\n"
  "global_load_lds_dwordx4 v107, s[42:43]\n"
  "s_add_u32 s42, s42, 44032\n"
  "s_addc_u32 s43, s43, 0\n"
  "s_waitcnt vmcnt(24)\n"
  "ds_read_b64_tr_b16 a[8:9], v109 offset:8192\n"
  "ds_read_b64_tr_b16 a[72:73], v109 offset:8224\n"
  "ds_read_b64_tr_b16 a[12:13], v109 offset:9216\n"
  "ds_read_b64_tr_b16 a[76:77], v109 offset:9248\n"
  "ds_read_b64_tr_b16 a[24:25], v109 offset:10240\n"
  "ds_read_b64_tr_b16 a[88:89], v109 offset:10272\n"
  "ds_read_b64_tr_b16 a[28:29], v109 offset:11264\n"
  "ds_read_b64_tr_b16 a[92:93], v109 offset:11296\n"
  "ds_read_b64_tr_b16 a[40:41], v109 offset:12288\n"
  "ds_read_b64_tr_b16 a[104:105], v109 offset:12320\n"
  "ds_read_b64_tr_b16 a[44:45], v109 offset:13312\n"
  "ds_read_b64_tr_b16 a[108:109], v109 offset:13344\n"
  "ds_read_b64_tr_b16 a[56:57], v109 offset:14336\n"
  "ds_read_b64_tr_b16 a[120:121], v109 offset:14368\n"
  "ds_read_b64_tr_b16 a[60:61], v109 offset:15360\n"
  "ds_read_b64_tr_b16 a[124:125], v109 offset:15392\n"
  "s_waitcnt lgkmcnt(0)\n"
  "s_add_u32 m0, s58, 8192\n"
  "s_nop 0\n"
  "global_load_lds_dwordx4 v107, s[42:43]\n"
  "s_add_u32 s42, s42, 44032\n"
  "s_addc_u32 s43, s43, 0\n"
  "s_add_u32 m0, m0, 1024\n"
  "s_nop 0\n"
  "global_load_lds_dwordx4 v107, s[42:43]\n"
  "s_add_u32 s42, s42, 44032\n"
  "s_addc_u32 s43, s43, 0\n"
  "s_add_u32 m0, m0, 1024\n"
  "s_nop 0\n"
  "global_load_lds_dwordx4 v107, s[42:43]\n"
  "s_add_u32 s42, s42, 44032\n"
  "s_addc_u32 s43, s43, 0\n"
  "s_add_u32 m0, m0, 1024\n"
  "s_nop 0\n"
  "global_load_lds_dwordx4 v107, s[42:43]\n"
  "s_add_u32 s42, s42, 44032\n"
  "s_addc_u32 s43, s43, 0\n"
  "s_add_u32 m0, m0, 1024\n"
  "s_nop 0\n"
  "global_load_lds_dwordx4 v107, s[42:43]\n"
  "s_add_u32 s42, s42, 44032\n"
  "s_addc_u32 s43, s43, 0\n"
  "s_add_u32 m0, m0, 1024\n"
  "s_nop 0\n"
  "global_load_lds_dwordx4 v107, s[42:43]\n"
  "s_add_u32 s42, s42, 44032\n"
  "s_addc_u32 s43, s43, 0\n"
  "s_add_u32 m0, m0, 1024\n"
  "s_nop 0\n"
  "global_load_lds_dwordx4 v107, s[42:43]\n"
  "s_add_u32 s42, s42, 44032\n"
  "s_addc_u32 s43, s43, 0\n"
  "s_add_u32 m0, m0, 1024\n"
  "s_nop 0\n"
  "global_load_lds_dwordx4 v107, s[42:43]\n"
  "s_add_u32 s42, s42, 44032\n"
  "s_addc_u32 s43, s43, 0\n"
  "s_waitcnt vmcnt(24)\n"
  "ds_read_b64_tr_b16 a[2:3], v109 offset:16384\n"
  "ds_read_b64_tr_b16 a[66:67], v109 offset:16416\n"
  "ds_read_b64_tr_b16 a[6:7], v109 offset:17408\n"
  "ds_read_b64_tr_b16 a[70:71], v109 offset:17440\n"
  "ds_read_b64_tr_b16 a[18:19], v109 offset:18432\n"
  "ds_read_b64_tr_b16 a[82:83], v109 offset:18464\n"
  "ds_read_b64_tr_b16 a[22:23], v109 offset:19456\n"
  "ds_read_b64_tr_b16 a[86:87], v109 offset:19488\n"
  "ds_read_b64_tr_b16 a[34:35], v109 offset:20480\n"
  "ds_read_b64_tr_b16 a[98:99], v109 offset:20512\n"
  "ds_read_b64_tr_b16 a[38:39], v109 offset:21504\n"
  "ds_read_b64_tr_b16 a[102:103], v109 offset:21536\n"
  "ds_read_b64_tr_b16 a[50:51], v109 offset:22528\n"
  "ds_read_b64_tr_b16 a[114:115], v109 offset:22560\n"
  "ds_read_b64_tr_b16 a[54:55], v109 offset:23552\n"
  "ds_read_b64_tr_b16 a[118:119], v109 offset:23584\n"
  "s_waitcnt lgkmcnt(0)\n"
  "s_add_u32 m0, s58, 16384\n"
  "s_nop 0\n"
  "global_load_lds_dwordx4 v107, s[42:43]\n"
  "s_add_u32 s42, s42, 44032\n"
  "s_addc_u32 s43, s43, 0\n"
  "s_add_u32 m0, m0, 1024\n"
  "s_nop 0\n"
  "global_load_lds_dwordx4 v107, s[42:43]\n"
  "s_add_u32 s42, s42, 44032\n"
  "s_addc_u32 s43, s43, 0\n"
  "s_add_u32 m0, m0, 1024\n"
  "s_nop 0\n"
  "global_load_lds_dwordx4 v107, s[42:43]\n"
  "s_add_u32 s42, s42, 44032\n"
  "s_addc_u32 s43, s43, 0\n"
  "s_add_u32 m0, m0, 1024\n"
  "s_nop 0\n"
  "global_load_lds_dwordx4 v107, s[42:43]\n"
  "s_add_u32 s42, s42, 44032\n"
  "s_addc_u32 s43, s43, 0\n"
  "s_add_u32 m0, m0, 1024\n"
  "s_nop 0\n"
  "global_load_lds_dwordx4 v107, s[42:43]\n"
  "s_add_u32 s42, s42, 44032\n"
  "s_addc_u32 s43, s43, 0\n"
  "s_add_u32 m0, m0, 1024\n"
  "s_nop 0\n"
  "global_load_lds_dwordx4 v107, s[42:43]\n"
  "s_add_u32 s42, s42, 44032\n"
  "s_addc_u32 s43, s43, 0\n"
  "s_add_u32 m0, m0, 1024\n"
  "s_nop 0\n"
  "global_load_lds_dwordx4 v107, s[42:43]\n"
  "s_add_u32 s42, s42, 44032\n"
  "s_addc_u32 s43, s43, 0\n"
  "s_add_u32 m0, m0, 1024\n"
  "s_nop 0\n"
  "global_load_lds_dwordx4 v107, s[42:43]\n"
  "s_add_u32 s42, s42, 44032\n"
  "s_addc_u32 s43, s43, 0\n"
  "s_waitcnt vmcnt(24)\n"
  "ds_read_b64_tr_b16 a[10:11], v109 offset:24576\n"
  "ds_read_b64_tr_b16 a[74:75], v109 offset:24608\n"
  "ds_read_b64_tr_b16 a[14:15], v109 offset:25600\n"
  "ds_read_b64_tr_b16 a[78:79], v109 offset:25632\n"
  "ds_read_b64_tr_b16 a[26:27], v109 offset:26624\n"
  "ds_read_b64_tr_b16 a[90:91], v109 offset:26656\n"
  "ds_read_b64_tr_b16 a[30:31], v109 offset:27648\n"
  "ds_read_b64_tr_b16 a[94:95], v109 offset:27680\n"
  "ds_read_b64_tr_b16 a[42:43], v109 offset:28672\n"
  "ds_read_b64_tr_b16 a[106:107], v109 offset:28704\n"
  "ds_read_b64_tr_b16 a[46:47], v109 offset:29696\n"
  "ds_read_b64_tr_b16 a[110:111], v109 offset:29728\n"
  "ds_read_b64_tr_b16 a[58:59], v109 offset:30720\n"
  "ds_read_b64_tr_b16 a[122:123], v109 offset:30752\n"
  "ds_read_b64_tr_b16 a[62:63], v109 offset:31744\n"
  "ds_read_b64_tr_b16 a[126:127], v109 offset:31776\n"
  "s_waitcnt lgkmcnt(0)\n"
  "s_add_u32 m0, s58, 24576\n"
  "s_nop 0\n"
  "global_load_lds_dwordx4 v107, s[42:43]\n"
  "s_add_u32 s42, s42, 44032\n"
  "s_addc_u32 s43, s43, 0\n"
  "s_add_u32 m0, m0, 1024\n"
  "s_nop 0\n"
  "global_load_lds_dwordx4 v107, s[42:43]\n"
  "s_add_u32 s42, s42, 44032\n"
  "s_addc_u32 s43, s43, 0\n"
  "s_add_u32 m0, m0, 1024\n"
  "s_nop 0\n"
  "global_load_lds_dwordx4 v107, s[42:43]\n"
  "s_add_u32 s42, s42, 44032\n"
  "s_addc_u32 s43, s43, 0\n"
  "s_add_u32 m0, m0, 1024\n"
  "s_nop 0\n"
  "global_load_lds_dwordx4 v107, s[42:43]\n"
  "s_add_u32 s42, s42, 44032\n"
  "s_addc_u32 s43, s43, 0\n"
  "s_add_u32 m0, m0, 1024\n"
  "s_nop 0\n"
  "global_load_lds_dwordx4 v107, s[42:43]\n"
  "s_add_u32 s42, s42, 44032\n"
  "s_addc_u32 s43, s43, 0\n"
  "s_add_u32 m0, m0, 1024\n"
  "s_nop 0\n"
  "global_load_lds_dwordx4 v107, s[42:43]\n"
  "s_add_u32 s42, s42, 44032\n"
  "s_addc_u32 s43, s43, 0\n"
  "s_add_u32 m0, m0, 1024\n"
  "s_nop 0\n"
  "global_load_lds_dwordx4 v107, s[42:43]\n"
  "s_add_u32 s42, s42, 44032\n"
  "s_addc_u32 s43, s43, 0\n"
  "s_add_u32 m0, m0, 1024\n"
  "s_nop 0\n"
  "global_load_lds_dwordx4 v107, s[42:43]\n"
  "s_add_u32 s42, s42, 44032\n"
  "s_addc_u32 s43, s43, 0\n"
  "s_waitcnt vmcnt(24)\n"
  "ds_read_b64_tr_b16 a[128:129], v109 offset:0\n"
  "ds_read_b64_tr_b16 a[192:193], v109 offset:32\n"
  "ds_read_b64_tr_b16 a[132:133], v109 offset:1024\n"
  "ds_read_b64_tr_b16 a[196:197], v109 offset:1056\n"
  "ds_read_b64_tr_b16 a[144:145], v109 offset:2048\n"
  "ds_read_b64_tr_b16 a[208:209], v109 offset:2080\n"
  "ds_read_b64_tr_b16 a[148:149], v109 offset:3072\n"
  "ds_read_b64_tr_b16 a[212:213], v109 offset:3104\n"
  "ds_read_b64_tr_b16 a[160:161], v109 offset:4096\n"
  "ds_read_b64_tr_b16 a[224:225], v109 offset:4128\n"
  "ds_read_b64_tr_b16 a[164:165], v109 offset:5120\n"
  "ds_read_b64_tr_b16 a[228:229], v109 offset:5152\n"
  "ds_read_b64_tr_b16 a[176:177], v109 offset:6144\n"
  "ds_read_b64_tr_b16 a[240:241], v109 offset:6176\n"
  "ds_read_b64_tr_b16 a[180:181], v109 offset:7168\n"
  "ds_read_b64_tr_b16 a[244:245], v109 offset:7200\n"
  "s_waitcnt lgkmcnt(0)\n"
  "s_add_u32 m0, s58, 0\n"
  "s_add_u32 s66, s44, s46\n"
  "s_addc_u32 s67, s45, 0\n"
  "global_load_lds_dwordx4 v108, s[66:67]\n"
  "s_add_u32 s66, s66, 4096\n"
  "s_addc_u32 s67, s67, 0\n"
  "s_add_u32 m0, m0, 1024\n"
  "s_nop 0\n"
  "global_load_lds_dwordx4 v108, s[66:67]\n"
  "s_add_u32 s66, s66, 4096\n"
  "s_addc_u32 s67, s67, 0\n"
  "s_add_u32 m0, m0, 1024\n"
  "s_nop 0\n"
  "global_load_lds_dwordx4 v108, s[66:67]\n"
  "s_add_u32 s66, s66, 4096\n"
  "s_addc_u32 s67, s67, 0\n"
  "s_add_u32 m0, m0, 1024\n"
  "s_nop 0\n"
  "global_load_lds_dwordx4 v108, s[66:67]\n"
  "s_add_u32 s66, s66, 4096\n"
  "s_addc_u32 s67, s67, 0\n"
  "s_add_u32 m0, m0, 1024\n"
  "s_nop 0\n"
  "global_load_lds_dwordx4 v108, s[66:67]\n"
  "s_add_u32 s66, s66, 4096\n"
  "s_addc_u32 s67, s67, 0\n"
  "s_add_u32 m0, m0, 1024\n"
  "s_nop 0\n"
  "global_load_lds_dwordx4 v108, s[66:67]\n"
  "s_add_u32 s66, s66, 4096\n"
  "s_addc_u32 s67, s67, 0\n"
  "s_add_u32 m0, m0, 1024\n"
  "s_nop 0\n"
  "global_load_lds_dwordx4 v108, s[66:67]\n"
  "s_add_u32 s66, s66, 4096\n"
  "s_addc_u32 s67, s67, 0\n"
  "s_add_u32 m0, m0, 1024\n"
  "s_nop 0\n"
  "global_load_lds_dwordx4 v108, s[66:67]\n"
  "s_add_u32 s66, s66, 4096\n"
  "s_addc_u32 s67, s67, 0\n"
  "s_waitcnt vmcnt(24)\n"
  "ds_read_b64_tr_b16 a[136:137], v109 offset:8192\n"
  "ds_read_b64_tr_b16 a[200:201], v109 offset:8224\n"
  "ds_read_b64_tr_b16 a[140:141], v109 offset:9216\n"
  "ds_read_b64_tr_b16 a[204:205], v109 offset:9248\n"
  "ds_read_b64_tr_b16 a[152:153], v109 offset:10240\n"
  "ds_read_b64_tr_b16 a[216:217], v109 offset:10272\n"
  "ds_read_b64_tr_b16 a[156:157], v109 offset:11264\n"
  "ds_read_b64_tr_b16 a[220:221], v109 offset:11296\n"
  "ds_read_b64_tr_b16 a[168:169], v109 offset:12288\n"
  "ds_read_b64_tr_b16 a[232:233], v109 offset:12320\n"
  "ds_read_b64_tr_b16 a[172:173], v109 offset:13312\n"
  "ds_read_b64_tr_b16 a[236:237], v109 offset:13344\n"
  "ds_read_b64_tr_b16 a[184:185], v109 offset:14336\n"
  "ds_read_b64_tr_b16 a[248:249], v109 offset:14368\n"
  "ds_read_b64_tr_b16 a[188:189], v109 offset:15360\n"
  "ds_read_b64_tr_b16 a[252:253], v109 offset:15392\n"
  "s_waitcnt lgkmcnt(0)\n"
  "s_add_u32 m0, s58, 8192\n"
  "s_add_u32 s66, s44, s47\n"
  "s_addc_u32 s67, s45, 0\n"
  "global_load_lds_dwordx4 v108, s[66:67]\n"
  "s_add_u32 s66, s66, 4096\n"
  "s_addc_u32 s67, s67, 0\n"
  "s_add_u32 m0, m0, 1024\n"
  "s_nop 0\n"
  "global_load_lds_dwordx4 v108, s[66:67]\n"
  "s_add_u32 s66, s66, 4096\n"
  "s_addc_u32 s67, s67, 0\n"
  "s_add_u32 m0, m0, 1024\n"
  "s_nop 0\n"
  "global_load_lds_dwordx4 v108, s[66:67]\n"
  "s_add_u32 s66, s66, 4096\n"
  "s_addc_u32 s67, s67, 0\n"
  "s_add_u32 m0, m0, 1024\n"
  "s_nop 0\n"
  "global_load_lds_dwordx4 v108, s[66:67]\n"
  "s_add_u32 s66, s66, 4096\n"
  "s_addc_u32 s67, s67, 0\n"
  "s_add_u32 m0, m0, 1024\n"
  "s_nop 0\n"
  "global_load_lds_dwordx4 v108, s[66:67]\n"
  "s_add_u32 s66, s66, 4096\n"
  "s_addc_u32 s67, s67, 0\n"
  "s_add_u32 m0, m0, 1024\n"
  "s_nop 0\n"
  "global_load_lds_dwordx4 v108, s[66:67]\n"
  "s_add_u32 s66, s66, 4096\n"
  "s_addc_u32 s67, s67, 0\n"
  "s_add_u32 m0, m0, 1024\n"
  "s_nop 0\n"
  "global_load_lds_dwordx4 v108, s[66:67]\n"
  "s_add_u32 s66, s66, 4096\n"
  "s_addc_u32 s67, s67, 0\n"
  "s_add_u32 m0, m0, 1024\n"
  "s_nop 0\n"
  "global_load_lds_dwordx4 v108, s[66:67]\n"
  "s_add_u32 s66, s66, 4096\n"
  "s_addc_u32 s67, s67, 0\n"
  "s_waitcnt vmcnt(24)\n"
  "ds_read_b64_tr_b16 a[130:131], v109 offset:16384\n"
  "ds_read_b64_tr_b16 a[194:195], v109 offset:16416\n"
  "ds_read_b64_tr_b16 a[134:135], v109 offset:17408\n"
  "ds_read_b64_tr_b16 a[198:199], v109 offset:17440\n"
  "ds_read_b64_tr_b16 a[146:147], v109 offset:18432\n"
  "ds_read_b64_tr_b16 a[210:211], v109 offset:18464\n"
  "ds_read_b64_tr_b16 a[150:151], v109 offset:19456\n"
  "ds_read_b64_tr_b16 a[214:215], v109 offset:19488\n"
  "ds_read_b64_tr_b16 a[162:163], v109 offset:20480\n"
  "ds_read_b64_tr_b16 a[226:227], v109 offset:20512\n"
  "ds_read_b64_tr_b16 a[166:167], v109 offset:21504\n"
  "ds_read_b64_tr_b16 a[230:231], v109 offset:21536\n"
  "ds_read_b64_tr_b16 a[178:179], v109 offset:22528\n"
  "ds_read_b64_tr_b16 a[242:243], v109 offset:22560\n"
  "ds_read_b64_tr_b16 a[182:183], v109 offset:23552\n"
  "ds_read_b64_tr_b16 a[246:247], v109 offset:23584\n"
  "s_waitcnt lgkmcnt(0)\n"
  "s_add_u32 m0, s58, 16384\n"
  "s_add_u32 s66, s44, s48\n"
  "s_addc_u32 s67, s45, 0\n"
  "global_load_lds_dwordx4 v108, s[66:67]\n"
  "s_add_u32 s66, s66, 4096\n"
  "s_addc_u32 s67, s67, 0\n"
  "s_add_u32 m0, m0, 1024\n"
  "s_nop 0\n"
  "global_load_lds_dwordx4 v108, s[66:67]\n"
  "s_add_u32 s66, s66, 4096\n"
  "s_addc_u32 s67, s67, 0\n"
  "s_add_u32 m0, m0, 1024\n"
  "s_nop 0\n"
  "global_load_lds_dwordx4 v108, s[66:67]\n"
  "s_add_u32 s66, s66, 4096\n"
  "s_addc_u32 s67, s67, 0\n"
  "s_add_u32 m0, m0, 1024\n"
  "s_nop 0\n"
  "global_load_lds_dwordx4 v108, s[66:67]\n"
  "s_add_u32 s66, s66, 4096\n"
  "s_addc_u32 s67, s67, 0\n"
  "s_add_u32 m0, m0, 1024\n"
  "s_nop 0\n"
  "global_load_lds_dwordx4 v108, s[66:67]\n"
  "s_add_u32 s66, s66, 4096\n"
  "s_addc_u32 s67, s67, 0\n"
  "s_add_u32 m0, m0, 1024\n"
  "s_nop 0\n"
  "global_load_lds_dwordx4 v108, s[66:67]\n"
  "s_add_u32 s66, s66, 4096\n"
  "s_addc_u32 s67, s67, 0\n"
  "s_add_u32 m0, m0, 1024\n"
  "s_nop 0\n"
  "global_load_lds_dwordx4 v108, s[66:67]\n"
  "s_add_u32 s66, s66, 4096\n"
  "s_addc_u32 s67, s67, 0\n"
  "s_add_u32 m0, m0, 1024\n"
  "s_nop 0\n"
  "global_load_lds_dwordx4 v108, s[66:67]\n"
  "s_add_u32 s66, s66, 4096\n"
  "s_addc_u32 s67, s67, 0\n"
  "s_waitcnt vmcnt(24)\n"
  "ds_read_b64_tr_b16 a[138:139], v109 offset:24576\n"
  "ds_read_b64_tr_b16 a[202:203], v109 offset:24608\n"
  "ds_read_b64_tr_b16 a[142:143], v109 offset:25600\n"
  "ds_read_b64_tr_b16 a[206:207], v109 offset:25632\n"
  "ds_read_b64_tr_b16 a[154:155], v109 offset:26624\n"
  "ds_read_b64_tr_b16 a[218:219], v109 offset:26656\n"
  "ds_read_b64_tr_b16 a[158:159], v109 offset:27648\n"
  "ds_read_b64_tr_b16 a[222:223], v109 offset:27680\n"
  "ds_read_b64_tr_b16 a[170:171], v109 offset:28672\n"
  "ds_read_b64_tr_b16 a[234:235], v109 offset:28704\n"
  "ds_read_b64_tr_b16 a[174:175], v109 offset:29696\n"
  "ds_read_b64_tr_b16 a[238:239], v109 offset:29728\n"
  "ds_read_b64_tr_b16 a[186:187], v109 offset:30720\n"
  "ds_read_b64_tr_b16 a[250:251], v109 offset:30752\n"
  "ds_read_b64_tr_b16 a[190:191], v109 offset:31744\n"
  "ds_read_b64_tr_b16 a[254:255], v109 offset:31776\n"
  "s_waitcnt lgkmcnt(0)\n"
  "s_add_u32 m0, s58, 24576\n"
  "s_add_u32 s66, s44, s49\n"
  "s_addc_u32 s67, s45, 0\n"
  "global_load_lds_dwordx4 v108, s[66:67]\n"
  "s_add_u32 s66, s66, 4096\n"
  "s_addc_u32 s67, s67, 0\n"
  "s_add_u32 m0, m0, 1024\n"
  "s_nop 0\n"
  "global_load_lds_dwordx4 v108, s[66:67]\n"
  "s_add_u32 s66, s66, 4096\n"
  "s_addc_u32 s67, s67, 0\n"
  "s_add_u32 m0, m0, 1024\n"
  "s_nop 0\n"
  "global_load_lds_dwordx4 v108, s[66:67]\n"
  "s_add_u32 s66, s66, 4096\n"
  "s_addc_u32 s67, s67, 0\n"
  "s_add_u32 m0, m0, 1024\n"
  "s_nop 0\n"
  "global_load_lds_dwordx4 v108, s[66:67]\n"
  "s_add_u32 s66, s66, 4096\n"
  "s_addc_u32 s67, s67, 0\n"
  "s_add_u32 m0, m0, 1024\n"
  "s_nop 0\n"
  "global_load_lds_dwordx4 v108, s[66:67]\n"
  "s_add_u32 s66, s66, 4096\n"
  "s_addc_u32 s67, s67, 0\n"
  "s_add_u32 m0, m0, 1024\n"
  "s_nop 0\n"
  "global_load_lds_dwordx4 v108, s[66:67]\n"
  "s_add_u32 s66, s66, 4096\n"
  "s_addc_u32 s67, s67, 0\n"
  "s_add_u32 m0, m0, 1024\n"
  "s_nop 0\n"
  "global_load_lds_dwordx4 v108, s[66:67]\n"
  "s_add_u32 s66, s66, 4096\n"
  "s_addc_u32 s67, s67, 0\n"
  "s_add_u32 m0, m0, 1024\n"
  "s_nop 0\n"
  "global_load_lds_dwordx4 v108, s[66:67]\n"
  "s_add_u32 s66, s66, 4096\n"
  "s_addc_u32 s67, s67, 0\n"
  "s_waitcnt vmcnt(24)\n"
  "ds_read_b64_tr_b16 v[128:129], v110 offset:0\n"
  "ds_read_b64_tr_b16 v[130:131], v110 offset:4096\n"
  "ds_read_b64_tr_b16 v[132:133], v110 offset:8\n"
  "ds_read_b64_tr_b16 v[134:135], v110 offset:4104\n"
  "ds_read_b64_tr_b16 v[136:137], v110 offset:16\n"
  "ds_read_b64_tr_b16 v[138:139], v110 offset:4112\n"
  "ds_read_b64_tr_b16 v[140:141], v110 offset:24\n"
  "ds_read_b64_tr_b16 v[142:143], v110 offset:4120\n"
  "ds_read_b64_tr_b16 v[144:145], v110 offset:32\n"
  "ds_read_b64_tr_b16 v[146:147], v110 offset:4128\n"
  "ds_read_b64_tr_b16 v[148:149], v110 offset:40\n"
  "ds_read_b64_tr_b16 v[150:151], v110 offset:4136\n"
  "ds_read_b64_tr_b16 v[152:153], v110 offset:48\n"
  "ds_read_b64_tr_b16 v[154:155], v110 offset:4144\n"
  "ds_read_b64_tr_b16 v[156:157], v110 offset:56\n"
  "ds_read_b64_tr_b16 v[158:159], v110 offset:4152\n"
  "s_waitcnt lgkmcnt(0)\n"
  "s_waitcnt vmcnt(16)\n"
  "ds_read_b64_tr_b16 v[160:161], v110 offset:8192\n"
  "ds_read_b64_tr_b16 v[162:163], v110 offset:12288\n"
  "ds_read_b64_tr_b16 v[164:165], v110 offset:8200\n"
  "ds_read_b64_tr_b16 v[166:167], v110 offset:12296\n"
  "ds_read_b64_tr_b16 v[168:169], v110 offset:8208\n"
  "ds_read_b64_tr_b16 v[170:171], v110 offset:12304\n"
  "ds_read_b64_tr_b16 v[172:173], v110 offset:8216\n"
  "ds_read_b64_tr_b16 v[174:175], v110 offset:12312\n"
  "ds_read_b64_tr_b16 v[176:177], v110 offset:8224\n"
  "ds_read_b64_tr_b16 v[178:179], v110 offset:12320\n"
  "ds_read_b64_tr_b16 v[180:181], v110 offset:8232\n"
  "ds_read_b64_tr_b16 v[182:183], v110 offset:12328\n"
  "ds_read_b64_tr_b16 v[184:185], v110 offset:8240\n"
  "ds_read_b64_tr_b16 v[186:187], v110 offset:12336\n"
  "ds_read_b64_tr_b16 v[188:189], v110 offset:8248\n"
  "ds_read_b64_tr_b16 v[190:191], v110 offset:12344\n"
  "s_waitcnt lgkmcnt(0)\n"
  "s_waitcnt vmcnt(8)\n"
  "ds_read_b64_tr_b16 v[192:193], v110 offset:16384\n"
  "ds_read_b64_tr_b16 v[194:195], v110 offset:20480\n"
  "ds_read_b64_tr_b16 v[196:197], v110 offset:16392\n"
  "ds_read_b64_tr_b16 v[198:199], v110 offset:20488\n"
  "ds_read_b64_tr_b16 v[200:201], v110 offset:16400\n"
  "ds_read_b64_tr_b16 v[202:203], v110 offset:20496\n"
  "ds_read_b64_tr_b16 v[204:205], v110 offset:16408\n"
  "ds_read_b64_tr_b16 v[206:207], v110 offset:20504\n"
  "ds_read_b64_tr_b16 v[208:209], v110 offset:16416\n"
  "ds_read_b64_tr_b16 v[210:211], v110 offset:20512\n"
  "ds_read_b64_tr_b16 v[212:213], v110 offset:16424\n"
  "ds_read_b64_tr_b16 v[214:215], v110 offset:20520\n"
  "ds_read_b64_tr_b16 v[216:217], v110 offset:16432\n"
  "ds_read_b64_tr_b16 v[218:219], v110 offset:20528\n"
  "ds_read_b64_tr_b16 v[220:221], v110 offset:16440\n"
  "ds_read_b64_tr_b16 v[222:223], v110 offset:20536\n"
  "s_waitcnt lgkmcnt(0)\n"
  "s_waitcnt vmcnt(0)\n"
  "ds_read_b64_tr_b16 v[224:225], v110 offset:24576\n"
  "ds_read_b64_tr_b16 v[226:227], v110 offset:28672\n"
  "ds_read_b64_tr_b16 v[228:229], v110 offset:24584\n"
  "ds_read_b64_tr_b16 v[230:231], v110 offset:28680\n"
  "ds_read_b64_tr_b16 v[232:233], v110 offset:24592\n"
  "ds_read_b64_tr_b16 v[234:235], v110 offset:28688\n"
  "ds_read_b64_tr_b16 v[236:237], v110 offset:24600\n"
  "ds_read_b64_tr_b16 v[238:239], v110 offset:28696\n"
  "ds_read_b64_tr_b16 v[240:241], v110 offset:24608\n"
  "ds_read_b64_tr_b16 v[242:243], v110 offset:28704\n"
  "ds_read_b64_tr_b16 v[244:245], v110 offset:24616\n"
  "ds_read_b64_tr_b16 v[246:247], v110 offset:28712\n"
  "ds_read_b64_tr_b16 v[248:249], v110 offset:24624\n"
  "ds_read_b64_tr_b16 v[250:251], v110 offset:28720\n"
  "ds_read_b64_tr_b16 v[252:253], v110 offset:24632\n"
  "ds_read_b64_tr_b16 v[254:255], v110 offset:28728\n"
  "s_waitcnt lgkmcnt(0)\n"
  "s_cmp_eq_u32 s63, 0\n"
  "s_cbranch_scc0 .Lff_v\n"
  "v_accvgpr_write_b32 a128, 0\n"
  "v_accvgpr_write_b32 a129, 0\n"
  "v_accvgpr_write_b32 a130, 0\n"
  "v_accvgpr_write_b32 a131, 0\n"
  "v_accvgpr_write_b32 a132, 0\n"
  "v_accvgpr_write_b32 a133, 0\n"
  "v_accvgpr_write_b32 a134, 0\n"
  "v_accvgpr_write_b32 a135, 0\n"
  "v_accvgpr_write_b32 a136, 0\n"
  "v_accvgpr_write_b32 a137, 0\n"
  "v_accvgpr_write_b32 a138, 0\n"
  "v_accvgpr_write_b32 a139, 0\n"
  "v_accvgpr_write_b32 a140, 0\n"
  "v_accvgpr_write_b32 a141, 0\n"
  "v_accvgpr_write_b32 a142, 0\n"
  "v_accvgpr_write_b32 a143, 0\n"
  "v_accvgpr_write_b32 a144, 0\n"
  "v_accvgpr_write_b32 a145, 0\n"
  "v_accvgpr_write_b32 a146, 0\n"
  "v_accvgpr_write_b32 a147, 0\n"
  "v_accvgpr_write_b32 a148, 0\n"
  "v_accvgpr_write_b32 a149, 0\n"
  "v_accvgpr_write_b32 a150, 0\n"
  "v_accvgpr_write_b32 a151, 0\n"
  "v_accvgpr_write_b32 a152, 0\n"
  "v_accvgpr_write_b32 a153, 0\n"
  "v_accvgpr_write_b32 a154, 0\n"
  "v_accvgpr_write_b32 a155, 0\n"
  "v_accvgpr_write_b32 a156, 0\n"
  "v_accvgpr_write_b32 a157, 0\n"
  "v_accvgpr_write_b32 a158, 0\n"
  "v_accvgpr_write_b32 a159, 0\n"
  "v_accvgpr_write_b32 a160, 0\n"
  "v_accvgpr_write_b32 a161, 0\n"
  "v_accvgpr_write_b32 a162, 0\n"
  "v_accvgpr_write_b32 a163, 0\n"
  "v_accvgpr_write_b32 a164, 0\n"
  "v_accvgpr_write_b32 a165, 0\n"
  "v_accvgpr_write_b32 a166, 0\n"
  "v_accvgpr_write_b32 a167, 0\n"
  "v_accvgpr_write_b32 a168, 0\n"
  "v_accvgpr_write_b32 a169, 0\n"
  "v_accvgpr_write_b32 a170, 0\n"
  "v_accvgpr_write_b32 a171, 0\n"
  "v_accvgpr_write_b32 a172, 0\n"
  "v_accvgpr_write_b32 a173, 0\n"
  "v_accvgpr_write_b32 a174, 0\n"
  "v_accvgpr_write_b32 a175, 0\n"
  "v_accvgpr_write_b32 a176, 0\n"
  "v_accvgpr_write_b32 a177, 0\n"
  "v_accvgpr_write_b32 a178, 0\n"
  "v_accvgpr_write_b32 a179, 0\n"
  "v_accvgpr_write_b32 a180, 0\n"
  "v_accvgpr_write_b32 a181, 0\n"
  "v_accvgpr_write_b32 a182, 0\n"
  "v_accvgpr_write_b32 a183, 0\n"
  "v_accvgpr_write_b32 a184, 0\n"
  "v_accvgpr_write_b32 a185, 0\n"
  "v_accvgpr_write_b32 a186, 0\n"
  "v_accvgpr_write_b32 a187, 0\n"
  "v_accvgpr_write_b32 a188, 0\n"
  "v_accvgpr_write_b32 a189, 0\n"
  "v_accvgpr_write_b32 a190, 0\n"
  "v_accvgpr_write_b32 a191, 0\n"
  "v_accvgpr_write_b32 a192, 0\n"
  "v_accvgpr_write_b32 a193, 0\n"
  "v_accvgpr_write_b32 a194, 0\n"
  "v_accvgpr_write_b32 a195, 0\n"
  "v_accvgpr_write_b32 a196, 0\n"
  "v_accvgpr_write_b32 a197, 0\n"
  "v_accvgpr_write_b32 a198, 0\n"
  "v_accvgpr_write_b32 a199, 0\n"
  "v_accvgpr_write_b32 a200, 0\n"
  "v_accvgpr_write_b32 a201, 0\n"
  "v_accvgpr_write_b32 a202, 0\n"
  "v_accvgpr_write_b32 a203, 0\n"
  "v_accvgpr_write_b32 a204, 0\n"
  "v_accvgpr_write_b32 a205, 0\n"
  "v_accvgpr_write_b32 a206, 0\n"
  "v_accvgpr_write_b32 a207, 0\n"
  "v_accvgpr_write_b32 a208, 0\n"
  "v_accvgpr_write_b32 a209, 0\n"
  "v_accvgpr_write_b32 a210, 0\n"
  "v_accvgpr_write_b32 a211, 0\n"
  "v_accvgpr_write_b32 a212, 0\n"
  "v_accvgpr_write_b32 a213, 0\n"
  "v_accvgpr_write_b32 a214, 0\n"
  "v_accvgpr_write_b32 a215, 0\n"
  "v_accvgpr_write_b32 a216, 0\n"
  "v_accvgpr_write_b32 a217, 0\n"
  "v_accvgpr_write_b32 a218, 0\n"
  "v_accvgpr_write_b32 a219, 0\n"
  "v_accvgpr_write_b32 a220, 0\n"
  "v_accvgpr_write_b32 a221, 0\n"
  "v_accvgpr_write_b32 a222, 0\n"
  "v_accvgpr_write_b32 a223, 0\n"
  "v_accvgpr_write_b32 a224, 0\n"
  "v_accvgpr_write_b32 a225, 0\n"
  "v_accvgpr_write_b32 a226, 0\n"
  "v_accvgpr_write_b32 a227, 0\n"
  "v_accvgpr_write_b32 a228, 0\n"
  "v_accvgpr_write_b32 a229, 0\n"
  "v_accvgpr_write_b32 a230, 0\n"
  "v_accvgpr_write_b32 a231, 0\n"
  "v_accvgpr_write_b32 a232, 0\n"
  "v_accvgpr_write_b32 a233, 0\n"
  "v_accvgpr_write_b32 a234, 0\n"
  "v_accvgpr_write_b32 a235, 0\n"
  "v_accvgpr_write_b32 a236, 0\n"
  "v_accvgpr_write_b32 a237, 0\n"
  "v_accvgpr_write_b32 a238, 0\n"
  "v_accvgpr_write_b32 a239, 0\n"
  "v_accvgpr_write_b32 a240, 0\n"
  "v_accvgpr_write_b32 a241, 0\n"
  "v_accvgpr_write_b32 a242, 0\n"
  "v_accvgpr_write_b32 a243, 0\n"
  "v_accvgpr_write_b32 a244, 0\n"
  "v_accvgpr_write_b32 a245, 0\n"
  "v_accvgpr_write_b32 a246, 0\n"
  "v_accvgpr_write_b32 a247, 0\n"
  "v_accvgpr_write_b32 a248, 0\n"
  "v_accvgpr_write_b32 a249, 0\n"
  "v_accvgpr_write_b32 a250, 0\n"
  "v_accvgpr_write_b32 a251, 0\n"
  "v_accvgpr_write_b32 a252, 0\n"
  "v_accvgpr_write_b32 a253, 0\n"
  "v_accvgpr_write_b32 a254, 0\n"
  "v_accvgpr_write_b32 a255, 0\n"
  ".Lff_v:\n"
  "s_waitcnt lgkmcnt(0)\n"
  "s_barrier\n"
  "v_mov_b32 v106, 0\n"
  "s_memtime s[64:65]\n"
  "s_waitcnt lgkmcnt(0)\n"
  "s_mov_b32 s56, s64\n"
  "v_xor_b32 v117, 16, v100\n"
  "v_xor_b32 v121, 1024, v102\n"
  "v_xor_b32 v118, 32, v100\n"
  "v_xor_b32 v122, 2048, v102\n"
  "v_xor_b32 v119, 48, v100\n"
  "v_xor_b32 v123, 3072, v102\n"
  "v_mov_b32 v124, -1\n"
  "v_mov_b32 v125, -1\n"
  "v_mov_b32 v126, -1\n"
  "v_mov_b32 v127, -1\n"
  "s_lshr_b32 s53, s51, 1\n"
  "s_xor_b32 s53, s53, s50\n"
  "s_and_b32 s53, s53, 1\n"
  "s_lshl_b32 s53, s53, 13\n"
  "s_and_b32 s60, s51, 31\n"
  "s_lshl_b32 s60, s60, 20\n"
  "s_add_u32 s60, s60, s61\n"
  "s_or_b32 s60, s60, s53\n"
  "buffer_store_dwordx4 v[124:127], v103, s[36:39], s60 offen offset:0 sc1\n"
  "buffer_store_dwordx4 v[124:127], v103, s[36:39], s60 offen offset:1024 sc1\n"
  "buffer_store_dwordx4 v[124:127], v103, s[36:39], s60 offen offset:2048 sc1\n"
  "buffer_store_dwordx4 v[124:127], v103, s[36:39], s60 offen offset:3072 sc1\n"
  ".Lff_loop:\n"
  "s_add_u32 s55, s51, 1\n"
  "v_cmp_gt_u32 vcc, s55, v106\n"
  "s_lshr_b32 s53, s51, 3\n"
  "s_xor_b32 s53, s53, s50\n"
  "s_and_b32 s53, s53, 1\n"
  "s_lshl_b32 s53, s53, 13\n"
  "s_and_b32 s59, s51, 31\n"
  "s_lshl_b32 s59, s59, 18\n"
  "s_add_u32 s59, s59, 201326592\n"
  "s_or_b32 s59, s59, s53\n"
  "s_lshr_b32 s53, s51, 1\n"
  "s_xor_b32 s53, s53, s50\n"
  "s_and_b32 s53, s53, 1\n"
  "s_lshl_b32 s53, s53, 13\n"
  "s_and_b32 s60, s51, 31\n"
  "s_lshl_b32 s60, s60, 20\n"
  "s_add_u32 s60, s60, s61\n"
  "s_or_b32 s60, s60, s53\n"
  "s_lshr_b32 s53, s55, 1\n"
  "s_xor_b32 s53, s53, s50\n"
  "s_and_b32 s53, s53, 1\n"
  "s_lshl_b32 s53, s53, 13\n"
  "s_and_b32 s62, s55, 31\n"
  "s_lshl_b32 s62, s62, 20\n"
  "s_add_u32 s62, s62, s61\n"
  "s_or_b32 s62, s62, s53\n"
  "s_cbranch_vccz .Lff_go\n"
  ".Lff_p0:\n"
  "s_memtime s[64:65]\n"
  "s_waitcnt lgkmcnt(0)\n"
  "s_sub_u32 s53, s64, s56\n"
  "s_cmp_lt_i32 s53, 0\n"
  "s_cbranch_scc0 .Lff_p1\n"
  "s_sleep 1\n"
  "s_branch .Lff_p0\n"
  ".Lff_p1:\n"
  "buffer_load_dword v106, v104, s[36:39], 0 offen sc1\n"
  "s_memtime s[64:65]\n"
  "s_sub_u32 s52, s52, 1\n"
  "s_cbranch_scc1 .Lff_end\n"
  "s_waitcnt lgkmcnt(0)\n"
  "s_add_u32 s56, s64, 3400\n"
  "s_waitcnt vmcnt(0)\n"
  "v_cmp_gt_u32 vcc, s55, v106\n"
  "s_cbranch_vccz .Lff_go\n"
  "s_sleep 1\n"
  "s_branch .Lff_p1\n"
  ".Lff_go:\n"
  "buffer_load_dwordx4 v[80:83], v101, s[36:39], s59 offen offset:0 sc1\n"
  "buffer_load_dwordx4 v[84:87], v101, s[36:39], s59 offen offset:1024 sc1\n"
  "buffer_load_dwordx4 v[88:91], v101, s[36:39], s59 offen offset:2048 sc1\n"
  "buffer_load_dwordx4 v[92:95], v101, s[36:39], s59 offen offset:3072 sc1\n"
  ".Lff_c:\n"
  "s_waitcnt vmcnt(3)\n"
  "ds_write_b128 v100, v[80:83] offset:0\n"
  "s_waitcnt vmcnt(2)\n"
  "ds_write_b128 v117, v[84:87] offset:1024\n"
  "s_waitcnt vmcnt(1)\n"
  "ds_write_b128 v118, v[88:91] offset:2048\n"
  "s_waitcnt vmcnt(0)\n"
  "ds_write_b128 v119, v[92:95] offset:3072\n"
  "v_max3_u32 v112, v80, v82, v84\n"
  "v_max3_u32 v112, v112, v86, v88\n"
  "v_max3_u32 v112, v112, v90, v92\n"
  "v_max_u32 v112, v112, v94\n"
  "v_cmp_eq_u32 vcc, -1, v112\n"
  "s_cbranch_vccnz .Lff_m\n"
  "s_waitcnt lgkmcnt(0)\n"
  "s_barrier\n"
  "ds_read_b128 v[0:3], v96 offset:0\n"
  "ds_read_b128 v[4:7], v97 offset:0\n"
  "ds_read_b128 v[8:11], v98 offset:0\n"
  "ds_read_b128 v[12:15], v99 offset:0\n"
  "ds_read_b128 v[16:19], v96 offset:256\n"
  "ds_read_b128 v[20:23], v97 offset:256\n"
  "s_waitcnt lgkmcnt(5)\n"
  "v_mfma_f32_16x16x32_bf16 v[64:67], a[0:3], v[0:3], 0\n"
  "ds_read_b128 v[24:27], v98 offset:256\n"
  "v_mfma_f32_16x16x32_bf16 v[68:71], a[64:67], v[0:3], 0\n"
  "buffer_store_dwordx4 v[124:127], v103, s[36:39], s62 offen offset:0 sc1\n"
  "s_waitcnt lgkmcnt(5)\n"
  "v_mfma_f32_16x16x32_bf16 v[64:67], a[4:7], v[4:7], v[64:67]\n"
  "ds_read_b128 v[28:31], v99 offset:256\n"
  "v_mfma_f32_16x16x32_bf16 v[68:71], a[68:71], v[4:7], v[68:71]\n"
  "s_waitcnt lgkmcnt(5)\n"
  "v_mfma_f32_16x16x32_bf16 v[64:67], a[8:11], v[8:11], v[64:67]\n"
  "ds_read_b128 v[32:35], v96 offset:512\n"
  "v_mfma_f32_16x16x32_bf16 v[68:71], a[72:75], v[8:11], v[68:71]\n"
  "buffer_store_dwordx4 v[124:127], v103, s[36:39], s62 offen offset:1024 sc1\n"
  "s_waitcnt lgkmcnt(5)\n"
  "v_mfma_f32_16x16x32_bf16 v[64:67], a[12:15], v[12:15], v[64:67]\n"
  "ds_read_b128 v[36:39], v97 offset:512\n"
  "v_mfma_f32_16x16x32_bf16 v[68:71], a[76:79], v[12:15], v[68:71]\n"
  "s_waitcnt lgkmcnt(5)\n"
  "v_mfma_f32_16x16x32_bf16 v[64:67], a[16:19], v[16:19], v[64:67]\n"
  "ds_read_b128 v[40:43], v98 offset:512\n"
  "v_mfma_f32_16x16x32_bf16 v[68:71], a[80:83], v[16:19], v[68:71]\n"
  "buffer_store_dwordx4 v[124:127], v103, s[36:39], s62 offen offset:2048 sc1\n"
  "s_waitcnt lgkmcnt(5)\n"
  "v_mfma_f32_16x16x32_bf16 v[64:67], a[20:23], v[20:23], v[64:67]\n"
  "ds_read_b128 v[44:47], v99 offset:512\n"
  "v_mfma_f32_16x16x32_bf16 v[68:71], a[84:87], v[20:23], v[68:71]\n"
  "s_waitcnt lgkmcnt(5)\n"
  "v_mfma_f32_16x16x32_bf16 v[64:67], a[24:27], v[24:27], v[64:67]\n"
  "ds_read_b128 v[48:51], v96 offset:768\n"
  "v_mfma_f32_16x16x32_bf16 v[68:71], a[88:91], v[24:27], v[68:71]\n"
  "buffer_store_dwordx4 v[124:127], v103, s[36:39], s62 offen offset:3072 sc1\n"
  "s_waitcnt lgkmcnt(5)\n"
  "v_mfma_f32_16x16x32_bf16 v[64:67], a[28:31], v[28:31], v[64:67]\n"
  "ds_read_b128 v[52:55], v97 offset:768\n"
  "v_mfma_f32_16x16x32_bf16 v[68:71], a[92:95], v[28:31], v[68:71]\n"
  "s_waitcnt lgkmcnt(5)\n"
  "v_mfma_f32_16x16x32_bf16 v[64:67], a[32:35], v[32:35], v[64:67]\n"
  "ds_read_b128 v[56:59], v98 offset:768\n"
  "v_mfma_f32_16x16x32_bf16 v[68:71], a[96:99], v[32:35], v[68:71]\n"
  "s_waitcnt lgkmcnt(5)\n"
  "v_mfma_f32_16x16x32_bf16 v[64:67], a[36:39], v[36:39], v[64:67]\n"
  "ds_read_b128 v[60:63], v99 offset:768\n"
  "v_mfma_f32_16x16x32_bf16 v[68:71], a[100:103], v[36:39], v[68:71]\n"
  "s_waitcnt lgkmcnt(5)\n"
  "v_mfma_f32_16x16x32_bf16 v[64:67], a[40:43], v[40:43], v[64:67]\n"
  "v_mfma_f32_16x16x32_bf16 v[68:71], a[104:107], v[40:43], v[68:71]\n"
  "s_waitcnt lgkmcnt(4)\n"
  "v_mfma_f32_16x16x32_bf16 v[64:67], a[44:47], v[44:47], v[64:67]\n"
  "v_mfma_f32_16x16x32_bf16 v[68:71], a[108:111], v[44:47], v[68:71]\n"
  "s_waitcnt lgkmcnt(3)\n"
  "v_mfma_f32_16x16x32_bf16 v[64:67], a[48:51], v[48:51], v[64:67]\n"
  "v_mfma_f32_16x16x32_bf16 v[68:71], a[112:115], v[48:51], v[68:71]\n"
  "s_waitcnt lgkmcnt(2)\n"
  "v_mfma_f32_16x16x32_bf16 v[64:67], a[52:55], v[52:55], v[64:67]\n"
  "v_mfma_f32_16x16x32_bf16 v[68:71], a[116:119], v[52:55], v[68:71]\n"
  "s_waitcnt lgkmcnt(1)\n"
  "v_mfma_f32_16x16x32_bf16 v[64:67], a[56:59], v[56:59], v[64:67]\n"
  "v_mfma_f32_16x16x32_bf16 v[68:71], a[120:123], v[56:59], v[68:71]\n"
  "s_waitcnt lgkmcnt(0)\n"
  "v_mfma_f32_16x16x32_bf16 v[64:67], a[60:63], v[60:63], v[64:67]\n"
  "v_mfma_f32_16x16x32_bf16 v[68:71], a[124:127], v[60:63], v[68:71]\n"
  "s_nop 0\n"
  "v_mfma_f32_16x16x32_bf16 v[72:75], a[128:131], v[0:3], 0\n"
  "s_nop 2\n"
  "v_mfma_f32_16x16x32_bf16 v[72:75], a[132:135], v[4:7], v[72:75]\n"
  "s_nop 0\n"
  "v_mul_f32 v84, 0xbfb8aa3b, v64\n"
  "v_mul_f32 v85, 0xbfb8aa3b, v65\n"
  "v_mfma_f32_16x16x32_bf16 v[72:75], a[136:139], v[8:11], v[72:75]\n"
  "v_mul_f32 v86, 0xbfb8aa3b, v66\n"
  "v_mul_f32 v87, 0xbfb8aa3b, v67\n"
  "v_mfma_f32_16x16x32_bf16 v[72:75], a[140:143], v[12:15], v[72:75]\n"
  "v_exp_f32 v84, v84\n"
  "v_mfma_f32_16x16x32_bf16 v[72:75], a[144:147], v[16:19], v[72:75]\n"
  "v_exp_f32 v85, v85\n"
  "v_mfma_f32_16x16x32_bf16 v[72:75], a[148:151], v[20:23], v[72:75]\n"
  "v_exp_f32 v86, v86\n"
  "v_mfma_f32_16x16x32_bf16 v[72:75], a[152:155], v[24:27], v[72:75]\n"
  "v_exp_f32 v87, v87\n"
  "v_mfma_f32_16x16x32_bf16 v[72:75], a[156:159], v[28:31], v[72:75]\n"
  "v_add_f32 v84, 1.0, v84\n"
  "v_add_f32 v85, 1.0, v85\n"
  "v_mfma_f32_16x16x32_bf16 v[72:75], a[160:163], v[32:35], v[72:75]\n"
  "v_add_f32 v86, 1.0, v86\n"
  "v_add_f32 v87, 1.0, v87\n"
  "v_mfma_f32_16x16x32_bf16 v[72:75], a[164:167], v[36:39], v[72:75]\n"
  "v_rcp_f32 v84, v84\n"
  "v_mfma_f32_16x16x32_bf16 v[72:75], a[168:171], v[40:43], v[72:75]\n"
  "v_rcp_f32 v85, v85\n"
  "v_mfma_f32_16x16x32_bf16 v[72:75], a[172:175], v[44:47], v[72:75]\n"
  "v_rcp_f32 v86, v86\n"
  "v_mfma_f32_16x16x32_bf16 v[72:75], a[176:179], v[48:51], v[72:75]\n"
  "v_rcp_f32 v87, v87\n"
  "v_mfma_f32_16x16x32_bf16 v[72:75], a[180:183], v[52:55], v[72:75]\n"
  "v_mul_f32 v64, v64, v84\n"
  "v_mul_f32 v65, v65, v85\n"
  "v_mfma_f32_16x16x32_bf16 v[72:75], a[184:187], v[56:59], v[72:75]\n"
  "v_mul_f32 v66, v66, v86\n"
  "v_mul_f32 v67, v67, v87\n"
  "v_mfma_f32_16x16x32_bf16 v[72:75], a[188:191], v[60:63], v[72:75]\n"
  "v_mul_f32 v88, 0xbfb8aa3b, v68\n"
  "v_mul_f32 v89, 0xbfb8aa3b, v69\n"
  "v_mfma_f32_16x16x32_bf16 v[76:79], a[192:195], v[0:3], 0\n"
  "v_mul_f32 v90, 0xbfb8aa3b, v70\n"
  "v_mul_f32 v91, 0xbfb8aa3b, v71\n"
  "v_mfma_f32_16x16x32_bf16 v[76:79], a[196:199], v[4:7], v[76:79]\n"
  "v_exp_f32 v88, v88\n"
  "v_mfma_f32_16x16x32_bf16 v[76:79], a[200:203], v[8:11], v[76:79]\n"
  "v_exp_f32 v89, v89\n"
  "v_mfma_f32_16x16x32_bf16 v[76:79], a[204:207], v[12:15], v[76:79]\n"
  "v_exp_f32 v90, v90\n"
  "v_mfma_f32_16x16x32_bf16 v[76:79], a[208:211], v[16:19], v[76:79]\n"
  "v_exp_f32 v91, v91\n"
  "v_mfma_f32_16x16x32_bf16 v[76:79], a[212:215], v[20:23], v[76:79]\n"
  "v_add_f32 v88, 1.0, v88\n"
  "v_add_f32 v89, 1.0, v89\n"
  "v_mfma_f32_16x16x32_bf16 v[76:79], a[216:219], v[24:27], v[76:79]\n"
  "v_add_f32 v90, 1.0, v90\n"
  "v_add_f32 v91, 1.0, v91\n"
  "v_mfma_f32_16x16x32_bf16 v[76:79], a[220:223], v[28:31], v[76:79]\n"
  "v_rcp_f32 v88, v88\n"
  "v_mfma_f32_16x16x32_bf16 v[76:79], a[224:227], v[32:35], v[76:79]\n"
  "v_rcp_f32 v89, v89\n"
  "v_mfma_f32_16x16x32_bf16 v[76:79], a[228:231], v[36:39], v[76:79]\n"
  "v_rcp_f32 v90, v90\n"
  "v_mfma_f32_16x16x32_bf16 v[76:79], a[232:235], v[40:43], v[76:79]\n"
  "v_rcp_f32 v91, v91\n"
  "v_mfma_f32_16x16x32_bf16 v[76:79], a[236:239], v[44:47], v[76:79]\n"
  "v_mul_f32 v68, v68, v88\n"
  "v_mul_f32 v69, v69, v89\n"
  "v_mfma_f32_16x16x32_bf16 v[76:79], a[240:243], v[48:51], v[76:79]\n"
  "v_mul_f32 v70, v70, v90\n"
  "v_mul_f32 v71, v71, v91\n"
  "v_mfma_f32_16x16x32_bf16 v[76:79], a[244:247], v[52:55], v[76:79]\n"
  "v_mul_f32 v64, v64, v72\n"
  "v_mul_f32 v65, v65, v73\n"
  "v_mfma_f32_16x16x32_bf16 v[76:79], a[248:251], v[56:59], v[76:79]\n"
  "s_nop 0\n"
  "v_mul_f32 v66, v66, v74\n"
  "v_mul_f32 v67, v67, v75\n"
  "v_mfma_f32_16x16x32_bf16 v[76:79], a[252:255], v[60:63], v[76:79]\n"
  "s_nop 0\n"
  "v_cvt_pk_bf16_f32 v80, v64, v65\n"
  "v_cvt_pk_bf16_f32 v81, v66, v67\n"
  "s_nop 4\n"
  "v_mul_f32 v68, v68, v76\n"
  "v_mul_f32 v69, v69, v77\n"
  "v_mul_f32 v70, v70, v78\n"
  "v_mul_f32 v71, v71, v79\n"
  "v_cvt_pk_bf16_f32 v82, v68, v69\n"
  "v_cvt_pk_bf16_f32 v83, v70, v71\n"
  "ds_write_b128 v102, v[80:83] offset:0\n"
  "s_nop 0\n"
  "v_mfma_f32_16x16x32_bf16 v[0:3], v[80:83], v[128:131], 0\n"
  "v_mfma_f32_16x16x32_bf16 v[4:7], v[80:83], v[132:135], 0\n"
  "v_mfma_f32_16x16x32_bf16 v[8:11], v[80:83], v[136:139], 0\n"
  "v_mov_b32 v115, s55\n"
  "buffer_store_dword v115, v105, s[36:39], 0 offen sc1\n"
  "v_mfma_f32_16x16x32_bf16 v[12:15], v[80:83], v[140:143], 0\n"
  "v_mfma_f32_16x16x32_bf16 v[16:19], v[80:83], v[144:147], 0\n"
  "s_waitcnt lgkmcnt(0)\n"
  "s_barrier\n"
  "ds_read_b128 v[84:87], v121 offset:0\n"
  "ds_read_b128 v[88:91], v122 offset:0\n"
  "ds_read_b128 v[92:95], v123 offset:0\n"
  "v_mfma_f32_16x16x32_bf16 v[20:23], v[80:83], v[148:151], 0\n"
  "v_mfma_f32_16x16x32_bf16 v[24:27], v[80:83], v[152:155], 0\n"
  "v_mfma_f32_16x16x32_bf16 v[28:31], v[80:83], v[156:159], 0\n"
  "s_waitcnt lgkmcnt(2)\n"
  "v_mfma_f32_16x16x32_bf16 v[0:3], v[84:87], v[160:163], v[0:3]\n"
  "v_mfma_f32_16x16x32_bf16 v[4:7], v[84:87], v[164:167], v[4:7]\n"
  "v_mfma_f32_16x16x32_bf16 v[8:11], v[84:87], v[168:171], v[8:11]\n"
  "v_mfma_f32_16x16x32_bf16 v[12:15], v[84:87], v[172:175], v[12:15]\n"
  "v_mfma_f32_16x16x32_bf16 v[16:19], v[84:87], v[176:179], v[16:19]\n"
  "v_mfma_f32_16x16x32_bf16 v[20:23], v[84:87], v[180:183], v[20:23]\n"
  "v_mfma_f32_16x16x32_bf16 v[24:27], v[84:87], v[184:187], v[24:27]\n"
  "v_mfma_f32_16x16x32_bf16 v[28:31], v[84:87], v[188:191], v[28:31]\n"
  "s_waitcnt lgkmcnt(1)\n"
  "v_mfma_f32_16x16x32_bf16 v[0:3], v[88:91], v[192:195], v[0:3]\n"
  "v_mfma_f32_16x16x32_bf16 v[4:7], v[88:91], v[196:199], v[4:7]\n"
  "v_mfma_f32_16x16x32_bf16 v[8:11], v[88:91], v[200:203], v[8:11]\n"
  "v_mfma_f32_16x16x32_bf16 v[12:15], v[88:91], v[204:207], v[12:15]\n"
  "v_mfma_f32_16x16x32_bf16 v[16:19], v[88:91], v[208:211], v[16:19]\n"
  "v_mfma_f32_16x16x32_bf16 v[20:23], v[88:91], v[212:215], v[20:23]\n"
  "v_mfma_f32_16x16x32_bf16 v[24:27], v[88:91], v[216:219], v[24:27]\n"
  "v_mfma_f32_16x16x32_bf16 v[28:31], v[88:91], v[220:223], v[28:31]\n"
  "s_waitcnt lgkmcnt(0)\n"
  "v_mfma_f32_16x16x32_bf16 v[0:3], v[92:95], v[224:227], v[0:3]\n"
  "v_mfma_f32_16x16x32_bf16 v[4:7], v[92:95], v[228:231], v[4:7]\n"
  "s_nop 1\n"
  "v_mfma_f32_16x16x32_bf16 v[8:11], v[92:95], v[232:235], v[8:11]\n"
  "s_nop 2\n"
  "v_mfma_f32_16x16x32_bf16 v[12:15], v[92:95], v[236:239], v[12:15]\n"
  "s_nop 0\n"
  "v_cvt_pk_bf16_f32 v32, v0, v4\n"
  "v_cvt_pk_bf16_f32 v36, v1, v5\n"
  "v_mfma_f32_16x16x32_bf16 v[16:19], v[92:95], v[240:243], v[16:19]\n"
  "v_cvt_pk_bf16_f32 v40, v2, v6\n"
  "v_cvt_pk_bf16_f32 v44, v3, v7\n"
  "v_mfma_f32_16x16x32_bf16 v[20:23], v[92:95], v[244:247], v[20:23]\n"
  "s_nop 0\n"
  "v_cvt_pk_bf16_f32 v33, v8, v12\n"
  "v_cvt_pk_bf16_f32 v37, v9, v13\n"
  "v_mfma_f32_16x16x32_bf16 v[24:27], v[92:95], v[248:251], v[24:27]\n"
  "v_cvt_pk_bf16_f32 v41, v10, v14\n"
  "v_cvt_pk_bf16_f32 v45, v11, v15\n"
  "v_mfma_f32_16x16x32_bf16 v[28:31], v[92:95], v[252:255], v[28:31]\n"
  "s_nop 0\n"
  "v_cvt_pk_bf16_f32 v34, v16, v20\n"
  "v_cvt_pk_bf16_f32 v38, v17, v21\n"
  "v_cvt_pk_bf16_f32 v42, v18, v22\n"
  "v_cvt_pk_bf16_f32 v46, v19, v23\n"
  "s_nop 2\n"
  "v_cvt_pk_bf16_f32 v35, v24, v28\n"
  "buffer_store_dwordx4 v[32:35], v103, s[36:39], s60 offen offset:0 sc1\n"
  "v_cvt_pk_bf16_f32 v39, v25, v29\n"
  "buffer_store_dwordx4 v[36:39], v103, s[36:39], s60 offen offset:1024 sc1\n"
  "v_cvt_pk_bf16_f32 v43, v26, v30\n"
  "buffer_store_dwordx4 v[40:43], v103, s[36:39], s60 offen offset:2048 sc1\n"
  "v_cvt_pk_bf16_f32 v47, v27, v31\n"
  "buffer_store_dwordx4 v[44:47], v103, s[36:39], s60 offen offset:3072 sc1\n"
  "buffer_load_dword v106, v104, s[36:39], 0 offen sc1\n"
  "s_add_u32 s51, s51, 1\n"
  "s_waitcnt vmcnt(0)\n"
  "s_cmp_lt_u32 s51, 64\n"
  "s_cbranch_scc1 .Lff_loop\n"
  ".Lff_end:\n"
  "s_endpgm\n"
  ".Lff_m:\n"
  "s_sub_u32 s52, s52, 1\n"
  "s_cbranch_scc1 .Lff_end\n"
  "s_sleep 1\n"
  "s_waitcnt lgkmcnt(0)\n"
  "buffer_load_dwordx4 v[80:83], v101, s[36:39], s59 offen offset:0 sc1\n"
  "buffer_load_dwordx4 v[84:87], v101, s[36:39], s59 offen offset:1024 sc1\n"
  "buffer_load_dwordx4 v[88:91], v101, s[36:39], s59 offen offset:2048 sc1\n"
  "buffer_load_dwordx4 v[92:95], v101, s[36:39], s59 offen offset:3072 sc1\n"
  "s_waitcnt vmcnt(0)\n"
  "s_branch .Lff_c\n"
  ".Lff_lim:\n"
      :: "{v[96:99]}"(xa), "{v100}"(w * 4096 + ((lane ^ (4 * w)) << 4)), "{v101}"(sp((16 * th + 4 * w) * 1024) + lane * 16), "{v102}"(32768 + w * 1024 + lane * 16),
        "{v103}"(sp(4 * g * 1024) + w * 256 + li * 16), "{v104}"(fl(l, F2_XF + f % RX * 8 + th * 4 + w)), "{v105}"(lane < RP ? fl(l, F2_FP + lane * 96 + th * 48 + f * 4 + w) : OOR),
        "{v107}"((lane >> 2) * 2752 + (lane & 3) * 16), "{v108}"(g * 1024 + li * 16), "{v109}"(w * 32768 + (4 * g + (li >> 2)) * 64 + (li & 3) * 8),
        "{v110}"(w * 32768 + (4 * g + (li >> 2)) * 256 + (li & 3) * 64),
        "{s[40:41]}"(PTR(Wg, c0)), "{s[42:43]}"(PTR(Wu, c0)), "{s[44:45]}"(PTR(Wd, RFL(256 * w))), "{s46}"(ro[0]), "{s47}"(ro[1]), "{s48}"(ro[2]), "{s49}"(ro[3]),
        "{s58}"(RFL(w * 32768)), "{s61}"(RFL((128u << 20) + f * 65536 + th * 32768)), "{s63}"(RFL(valid)), COMMON_IN NORET;
}

DEV void role_rf(Ctx& C, char* ws, int l, u32 v0, int row, int w, int lane, const u16* x, u16* out) {
  const int g = lane >> 4, li = lane & 15;
  const int nat = (128 * (li >> 3) + 32 * g + 4 * (li & 7)) * 2;
  if (l == 1) {
    const char* xp = (const char*)x + row * 1024 + nat;
    u4v cur[8], nxt[8];
#pragma unroll
    for (int j = 0; j < 8; ++j) cur[j] = ldnat(xp + j * 32768);
    if (LNF) {
#pragma unroll
      for (int j = 0; j < LNF; ++j) st128(C.ws, aX0(j) + sp(row * 1024) + lane * 16, lnorm(cur[j]));
      if (!hs_wait(C, v0, 1)) return;
      VM0();
      st32(C.ws, lane == 0 ? FL_X0 + row * 128 : OOR, LNF);
      MEMB();
    }
    for (int b = 0; b < 8; ++b) {
      const int bn = b < 7 ? b + 1 : 7;
#pragma unroll
      for (int j = 0; j < 8; ++j) nxt[j] = ldnat(xp + (bn * 8 + j) * 32768);
      MEMB();
#pragma unroll
      for (int j = 0; j < 8; ++j) st128(C.ws, j < LNF && b == 0 ? OOR : aX0(b * 8 + j) + sp(row * 1024) + lane * 16, lnorm(cur[j]));
      if (!LNF && b == 0 && !hs_wait(C, v0, 1)) return;
      VM0();
      st32(C.ws, lane == 0 ? FL_X0 + row * 128 : OOR, b * 8 + 8);
#pragma unroll
      for (int j = 0; j < 8; ++j) cur[j] = nxt[j];
    }
  }
  const u32 P = hs_wait(C, v0, 0), cl = (P ^ l) & 1;
  if (!P) return;
  const u4v sel = mksel(lane);
  const u32 lo = (g & 1) * 65536 + sp(row * 1024) + (g >> 1) * 256 + li * 16;
  const u32 fout = lane >= (l ? 1 : RN) ? OOR : l ? FL_X0 + row * 128 : fl(cl ^ XNC, F2_XN + lane * 32 + row);
  const u32 fin = fl(cl, lane < 44 || !(HSM & 2) ? F2_FP + row % RP * 96 + (row >> 4) * 48 + (lane < 44 ? lane : 0) : FL_XF + (row >> 2));
  static_assert(!(HSM & 2) || (F2_XF == 736 && RX == 1), "");
  u32 pv0 = 0, tn = now();
  const u32 ro = sp(row * 1024) + lane * 16;
  for (int s = 0; s < 64; ++s) {
    u32 ra = aY(s & RING, cl) + ro, o[6];
#pragma unroll
    for (int q = 0; q < 6; ++q) { o[q] = q == 5 && (g & 1) ? OOR : aFP(s & RING, cl) + lo + q * 131072; asm volatile("" : "+v"(o[q])); }
    asm volatile("" : "+v"(ra));
    if (RNP ? !wait_q<T1>(C, ws, fin, s + 1, pv0, tn) : !wait_p<T1>(C, fin, s + 1, pv0, tn)) return;
    TS(0);
    u4v y;
    while (!reduce_row(C, ra, o, sel, y)) {
      if (expired(C)) return;
      __builtin_amdgcn_s_sleep(1);
    }
    if (l == 0) {
      st128(C.ws, aXN(s, cl ^ XNC) + ro, lnorm(y));
      st128(C.ws, aX2(s & RING, cl ^ XNC) + ro, y);
      VM0();
      st32(C.ws, fout, s + 1);
    } else {
      char* op = (char*)out + (s * 32 + row) * 1024 + nat;
      asm volatile("global_store_dwordx2 %0, %1, off sc1\n\tglobal_store_dwordx2 %0, %2, off offset:512 sc1" :: "v"(op), "v"(u2v{y.x, y.y}), "v"(u2v{y.z, y.w}) : "memory");
      MEMB();
      if ((s & 3) == 3) st32(C.ws, fout + 4, s + 1);
    }
    TS(1); TF(s, 0);
  }
}

DEV void role_ra(Ctx& C, char* ws, int ly, u32 l, int j, int w, int lane, const u16* x) {
  const int g = lane >> 4, li = lane & 15, row = 4 * j + (li >> 2), q = li & 3;
  asm volatile(
  "s_nop 4\n"
  "v_mov_b32 v125, 0\n"
  "s_memtime s[64:65]\n"
  "s_waitcnt lgkmcnt(0)\n"
  "s_mov_b32 s56, s64\n"
  "v_mov_b32 v136, -1\n"
  "v_mov_b32 v137, -1\n"
  "v_mov_b32 v138, -1\n"
  "v_mov_b32 v139, -1\n"
  "s_mov_b32 s79, 180224\n"
  "s_lshr_b32 s53, s51, 3\n"
  "s_xor_b32 s53, s53, s50\n"
  "s_and_b32 s53, s53, 1\n"
  "s_lshl_b32 s53, s53, 13\n"
  "s_and_b32 s60, s51, 31\n"
  "s_lshl_b32 s60, s60, 18\n"
  "s_add_u32 s60, s60, 201326592\n"
  "s_or_b32 s60, s60, s53\n"
  "buffer_store_dwordx4 v[136:139], v121, s[36:39], s60 offen offset:0 sc1\n"
  "buffer_store_dwordx4 v[136:139], v121, s[36:39], s60 offen offset:256 sc1\n"
  "buffer_store_dwordx4 v[136:139], v121, s[36:39], s60 offen offset:512 sc1\n"
  "buffer_store_dwordx4 v[136:139], v121, s[36:39], s60 offen offset:768 sc1\n"
  ".Lra_loop:\n"
  "s_add_u32 s55, s51, 1\n"
  "v_cmp_gt_u32 vcc, s55, v125\n"
  "s_lshr_b32 s53, s51, 1\n"
  "s_xor_b32 s53, s53, s50\n"
  "s_and_b32 s53, s53, 1\n"
  "s_lshl_b32 s53, s53, 13\n"
  "s_and_b32 s59, s51, 31\n"
  "s_lshl_b32 s59, s59, 20\n"
  "s_add_u32 s59, s59, 134938624\n"
  "s_or_b32 s59, s59, s53\n"
  "s_lshr_b32 s53, s51, 3\n"
  "s_xor_b32 s53, s53, s50\n"
  "s_and_b32 s53, s53, 1\n"
  "s_lshl_b32 s53, s53, 13\n"
  "s_and_b32 s60, s51, 31\n"
  "s_lshl_b32 s60, s60, 18\n"
  "s_add_u32 s60, s60, 201326592\n"
  "s_or_b32 s60, s60, s53\n"
  "s_add_u32 s61, s60, 65536\n"
  "s_xor_b32 s62, s60, 131072\n"
  "s_mov_b32 s63, 0\n"
  "s_cmp_eq_u32 s58, 0\n"
  "s_cbranch_scc0 .Lra_r1\n"
  "global_load_dwordx2 v[96:97], v122, s[40:41] offset:0\n"
  "global_load_dwordx2 v[98:99], v122, s[40:41] offset:512\n"
  "global_load_dwordx2 v[100:101], v122, s[40:41] offset:64\n"
  "global_load_dwordx2 v[102:103], v122, s[40:41] offset:576\n"
  "global_load_dwordx2 v[104:105], v122, s[40:41] offset:128\n"
  "global_load_dwordx2 v[106:107], v122, s[40:41] offset:640\n"
  "global_load_dwordx2 v[108:109], v122, s[40:41] offset:192\n"
  "global_load_dwordx2 v[110:111], v122, s[40:41] offset:704\n"
  "s_add_u32 s40, s40, 32768\n"
  "s_addc_u32 s41, s41, 0\n"
  "s_branch .Lra_r2\n"
  ".Lra_r1:\n"
  "s_mov_b32 s63, vcc_hi\n"
  "s_cmp_eq_u32 s63, 0\n"
  "s_cbranch_scc0 .Lra_r2\n"
  "buffer_load_dwordx4 v[96:99], v121, s[36:39], s62 offen offset:0 sc1\n"
  "buffer_load_dwordx4 v[100:103], v121, s[36:39], s62 offen offset:256 sc1\n"
  "buffer_load_dwordx4 v[104:107], v121, s[36:39], s62 offen offset:512 sc1\n"
  "buffer_load_dwordx4 v[108:111], v121, s[36:39], s62 offen offset:768 sc1\n"
  ".Lra_r2:\n"
  "s_cbranch_vccz .Lra_go\n"
  ".Lra_p0:\n"
  "s_memtime s[64:65]\n"
  "s_waitcnt lgkmcnt(0)\n"
  "s_sub_u32 s53, s64, s56\n"
  "s_cmp_lt_i32 s53, 0\n"
  "s_cbranch_scc0 .Lra_p1\n"
  "s_sleep 1\n"
  "s_branch .Lra_p0\n"
  ".Lra_p1:\n"
  "buffer_load_dword v125, v123, s[36:39], 0 offen sc1\n"
  "s_memtime s[64:65]\n"
  "s_sub_u32 s52, s52, 1\n"
  "s_cbranch_scc1 .Lra_end\n"
  "s_waitcnt lgkmcnt(0)\n"
  "s_add_u32 s56, s64, 3400\n"
  "s_waitcnt vmcnt(0)\n"
  "v_cmp_gt_u32 vcc, s55, v125\n"
  "s_cbranch_vccz .Lra_go\n"
  "s_sleep 1\n"
  "s_branch .Lra_p1\n"
  ".Lra_go:\n"
  "s_cmp_eq_u32 s63, 0\n"
  "s_cbranch_scc1 .Lra_r3\n"
  "s_waitcnt vmcnt(3)\n"
  "buffer_load_dwordx4 v[96:99], v121, s[36:39], s62 offen offset:0 sc1\n"
  "s_waitcnt vmcnt(3)\n"
  "buffer_load_dwordx4 v[100:103], v121, s[36:39], s62 offen offset:256 sc1\n"
  "s_waitcnt vmcnt(3)\n"
  "buffer_load_dwordx4 v[104:107], v121, s[36:39], s62 offen offset:512 sc1\n"
  "s_waitcnt vmcnt(3)\n"
  "buffer_load_dwordx4 v[108:111], v121, s[36:39], s62 offen offset:768 sc1\n"
  ".Lra_r3:\n"
  "s_add_u32 s66, s59, 16384\n"
  "s_add_u32 s67, s59, 32768\n"
  "s_add_u32 s68, s59, 49152\n"
  "buffer_load_dwordx4 v[0:3], v120, s[36:39], s59 offen offset:0 sc1\n"
  "buffer_load_dwordx4 v[4:7], v120, s[36:39], s66 offen offset:0 sc1\n"
  "buffer_load_dwordx4 v[8:11], v120, s[36:39], s67 offen offset:0 sc1\n"
  "buffer_load_dwordx4 v[12:15], v120, s[36:39], s68 offen offset:0 sc1\n"
  "buffer_load_dwordx4 v[16:19], v120, s[36:39], s59 offen offset:256 sc1\n"
  "buffer_load_dwordx4 v[20:23], v120, s[36:39], s66 offen offset:256 sc1\n"
  "buffer_load_dwordx4 v[24:27], v120, s[36:39], s67 offen offset:256 sc1\n"
  "buffer_load_dwordx4 v[28:31], v120, s[36:39], s68 offen offset:256 sc1\n"
  "buffer_load_dwordx4 v[32:35], v120, s[36:39], s59 offen offset:512 sc1\n"
  "buffer_load_dwordx4 v[36:39], v120, s[36:39], s66 offen offset:512 sc1\n"
  "buffer_load_dwordx4 v[40:43], v120, s[36:39], s67 offen offset:512 sc1\n"
  "buffer_load_dwordx4 v[44:47], v120, s[36:39], s68 offen offset:512 sc1\n"
  "buffer_load_dwordx4 v[48:51], v120, s[36:39], s59 offen offset:768 sc1\n"
  "buffer_load_dwordx4 v[52:55], v120, s[36:39], s66 offen offset:768 sc1\n"
  "buffer_load_dwordx4 v[56:59], v120, s[36:39], s67 offen offset:768 sc1\n"
  "buffer_load_dwordx4 v[60:63], v120, s[36:39], s68 offen offset:768 sc1\n"
  "s_waitcnt vmcnt(16)\n"
  "v_lshlrev_b32 v64, 16, v96\n"
  "v_and_b32 v65, 0xffff0000, v96\n"
  "v_lshlrev_b32 v66, 16, v97\n"
  "v_and_b32 v67, 0xffff0000, v97\n"
  "v_lshlrev_b32 v68, 16, v98\n"
  "v_and_b32 v69, 0xffff0000, v98\n"
  "v_lshlrev_b32 v70, 16, v99\n"
  "v_and_b32 v71, 0xffff0000, v99\n"
  "v_lshlrev_b32 v72, 16, v100\n"
  "v_and_b32 v73, 0xffff0000, v100\n"
  "v_lshlrev_b32 v74, 16, v101\n"
  "v_and_b32 v75, 0xffff0000, v101\n"
  "v_lshlrev_b32 v76, 16, v102\n"
  "v_and_b32 v77, 0xffff0000, v102\n"
  "v_lshlrev_b32 v78, 16, v103\n"
  "v_and_b32 v79, 0xffff0000, v103\n"
  "v_lshlrev_b32 v80, 16, v104\n"
  "v_and_b32 v81, 0xffff0000, v104\n"
  "v_lshlrev_b32 v82, 16, v105\n"
  "v_and_b32 v83, 0xffff0000, v105\n"
  "v_lshlrev_b32 v84, 16, v106\n"
  "v_and_b32 v85, 0xffff0000, v106\n"
  "v_lshlrev_b32 v86, 16, v107\n"
  "v_and_b32 v87, 0xffff0000, v107\n"
  "v_lshlrev_b32 v88, 16, v108\n"
  "v_and_b32 v89, 0xffff0000, v108\n"
  "v_lshlrev_b32 v90, 16, v109\n"
  "v_and_b32 v91, 0xffff0000, v109\n"
  "v_lshlrev_b32 v92, 16, v110\n"
  "v_and_b32 v93, 0xffff0000, v110\n"
  "v_lshlrev_b32 v94, 16, v111\n"
  "v_and_b32 v95, 0xffff0000, v111\n"
  ".Lra_c0:\n"
  "s_waitcnt vmcnt(14)\n"
  "v_max3_u32 v128, v0, v2, v4\n"
  "s_waitcnt vmcnt(13)\n"
  "v_max3_u32 v128, v128, v6, v8\n"
  "s_waitcnt vmcnt(12)\n"
  "v_max3_u32 v128, v128, v10, v12\n"
  "v_max_u32 v128, v128, v14\n"
  "v_cmp_eq_u32 vcc, -1, v128\n"
  "s_cbranch_vccnz .Lra_m0\n"
  "v_mfma_f32_16x16x32_bf16 v[64:67], v[112:115], v[0:3], v[64:67]\n"
  "v_mfma_f32_16x16x32_bf16 v[68:71], v[112:115], v[8:11], v[68:71]\n"
  "v_mfma_f32_16x16x32_bf16 v[64:67], v[116:119], v[4:7], v[64:67]\n"
  "v_mfma_f32_16x16x32_bf16 v[68:71], v[116:119], v[12:15], v[68:71]\n"
  ".Lra_c1:\n"
  "s_waitcnt vmcnt(10)\n"
  "v_max3_u32 v128, v16, v18, v20\n"
  "s_waitcnt vmcnt(9)\n"
  "v_max3_u32 v128, v128, v22, v24\n"
  "s_waitcnt vmcnt(8)\n"
  "v_max3_u32 v128, v128, v26, v28\n"
  "v_max_u32 v128, v128, v30\n"
  "v_cmp_eq_u32 vcc, -1, v128\n"
  "s_cbranch_vccnz .Lra_m1\n"
  "v_mfma_f32_16x16x32_bf16 v[72:75], v[112:115], v[16:19], v[72:75]\n"
  "v_mfma_f32_16x16x32_bf16 v[76:79], v[112:115], v[24:27], v[76:79]\n"
  "v_mfma_f32_16x16x32_bf16 v[72:75], v[116:119], v[20:23], v[72:75]\n"
  "v_mfma_f32_16x16x32_bf16 v[76:79], v[116:119], v[28:31], v[76:79]\n"
  "v_pk_mul_f32 v[100:101], v[64:65], v[64:65]\n"
  "v_pk_mul_f32 v[102:103], v[66:67], v[66:67]\n"
  "v_pk_fma_f32 v[100:101], v[68:69], v[68:69], v[100:101]\n"
  "v_pk_add_f32 v[96:97], v[68:69], v[64:65]\n"
  "v_pk_fma_f32 v[102:103], v[70:71], v[70:71], v[102:103]\n"
  "v_pk_add_f32 v[98:99], v[70:71], v[66:67]\n"
  "v_cvt_pk_bf16_f32 v0, v64, v65\n"
  "v_cvt_pk_bf16_f32 v1, v66, v67\n"
  "v_cvt_pk_bf16_f32 v2, v68, v69\n"
  "v_cvt_pk_bf16_f32 v3, v70, v71\n"
  "buffer_store_dwordx4 v[0:3], v121, s[36:39], s61 offen offset:0 sc1\n"
  ".Lra_c2:\n"
  "s_waitcnt vmcnt(7)\n"
  "v_max3_u32 v128, v32, v34, v36\n"
  "s_waitcnt vmcnt(6)\n"
  "v_max3_u32 v128, v128, v38, v40\n"
  "s_waitcnt vmcnt(5)\n"
  "v_max3_u32 v128, v128, v42, v44\n"
  "v_max_u32 v128, v128, v46\n"
  "v_cmp_eq_u32 vcc, -1, v128\n"
  "s_cbranch_vccnz .Lra_m2\n"
  "v_mfma_f32_16x16x32_bf16 v[80:83], v[112:115], v[32:35], v[80:83]\n"
  "v_mfma_f32_16x16x32_bf16 v[84:87], v[112:115], v[40:43], v[84:87]\n"
  "v_mfma_f32_16x16x32_bf16 v[80:83], v[116:119], v[36:39], v[80:83]\n"
  "v_mfma_f32_16x16x32_bf16 v[84:87], v[116:119], v[44:47], v[84:87]\n"
  "v_pk_fma_f32 v[100:101], v[72:73], v[72:73], v[100:101]\n"
  "v_pk_add_f32 v[96:97], v[72:73], v[96:97]\n"
  "v_pk_fma_f32 v[102:103], v[74:75], v[74:75], v[102:103]\n"
  "v_pk_add_f32 v[98:99], v[74:75], v[98:99]\n"
  "v_pk_fma_f32 v[100:101], v[76:77], v[76:77], v[100:101]\n"
  "v_pk_add_f32 v[96:97], v[76:77], v[96:97]\n"
  "v_pk_fma_f32 v[102:103], v[78:79], v[78:79], v[102:103]\n"
  "v_pk_add_f32 v[98:99], v[78:79], v[98:99]\n"
  "v_cvt_pk_bf16_f32 v16, v72, v73\n"
  "v_cvt_pk_bf16_f32 v17, v74, v75\n"
  "v_cvt_pk_bf16_f32 v18, v76, v77\n"
  "v_cvt_pk_bf16_f32 v19, v78, v79\n"
  "buffer_store_dwordx4 v[16:19], v121, s[36:39], s61 offen offset:256 sc1\n"
  ".Lra_c3:\n"
  "s_waitcnt vmcnt(4)\n"
  "v_max3_u32 v128, v48, v50, v52\n"
  "s_waitcnt vmcnt(3)\n"
  "v_max3_u32 v128, v128, v54, v56\n"
  "s_waitcnt vmcnt(2)\n"
  "v_max3_u32 v128, v128, v58, v60\n"
  "v_max_u32 v128, v128, v62\n"
  "v_cmp_eq_u32 vcc, -1, v128\n"
  "s_cbranch_vccnz .Lra_m3\n"
  "v_mfma_f32_16x16x32_bf16 v[88:91], v[112:115], v[48:51], v[88:91]\n"
  "v_mfma_f32_16x16x32_bf16 v[92:95], v[112:115], v[56:59], v[92:95]\n"
  "v_mfma_f32_16x16x32_bf16 v[88:91], v[116:119], v[52:55], v[88:91]\n"
  "v_mfma_f32_16x16x32_bf16 v[92:95], v[116:119], v[60:63], v[92:95]\n"
  "v_mov_b32 v131, s55\n"
  "buffer_store_dword v131, v124, s[36:39], s79 offen sc1\n"
  "v_pk_fma_f32 v[100:101], v[80:81], v[80:81], v[100:101]\n"
  "v_pk_add_f32 v[96:97], v[80:81], v[96:97]\n"
  "v_pk_fma_f32 v[102:103], v[82:83], v[82:83], v[102:103]\n"
  "v_pk_add_f32 v[98:99], v[82:83], v[98:99]\n"
  "v_pk_fma_f32 v[100:101], v[84:85], v[84:85], v[100:101]\n"
  "v_pk_add_f32 v[96:97], v[84:85], v[96:97]\n"
  "v_pk_fma_f32 v[102:103], v[86:87], v[86:87], v[102:103]\n"
  "v_pk_add_f32 v[98:99], v[86:87], v[98:99]\n"
  "v_cvt_pk_bf16_f32 v32, v80, v81\n"
  "v_cvt_pk_bf16_f32 v33, v82, v83\n"
  "v_cvt_pk_bf16_f32 v34, v84, v85\n"
  "v_cvt_pk_bf16_f32 v35, v86, v87\n"
  "buffer_store_dwordx4 v[32:35], v121, s[36:39], s61 offen offset:512 sc1\n"
  "v_pk_fma_f32 v[100:101], v[88:89], v[88:89], v[100:101]\n"
  "v_pk_add_f32 v[96:97], v[88:89], v[96:97]\n"
  "v_pk_fma_f32 v[102:103], v[90:91], v[90:91], v[102:103]\n"
  "v_pk_add_f32 v[98:99], v[90:91], v[98:99]\n"
  "v_pk_fma_f32 v[100:101], v[92:93], v[92:93], v[100:101]\n"
  "v_pk_add_f32 v[96:97], v[92:93], v[96:97]\n"
  "v_pk_fma_f32 v[102:103], v[94:95], v[94:95], v[102:103]\n"
  "v_pk_add_f32 v[98:99], v[94:95], v[98:99]\n"
  "v_pk_add_f32 v[96:97], v[96:97], v[98:99]\n"
  "v_pk_add_f32 v[100:101], v[100:101], v[102:103]\n"
  "v_add_f32 v96, v96, v97\n"
  "v_add_f32 v100, v100, v101\n"
  "v_cvt_pk_bf16_f32 v48, v88, v89\n"
  "v_add_f32_dpp v96, v96, v96 quad_perm:[1,0,3,2] row_mask:0xf bank_mask:0xf\n"
  "v_add_f32_dpp v100, v100, v100 quad_perm:[1,0,3,2] row_mask:0xf bank_mask:0xf\n"
  "v_cvt_pk_bf16_f32 v49, v90, v91\n"
  "v_add_f32_dpp v96, v96, v96 quad_perm:[2,3,0,1] row_mask:0xf bank_mask:0xf\n"
  "v_add_f32_dpp v100, v100, v100 quad_perm:[2,3,0,1] row_mask:0xf bank_mask:0xf\n"
  "v_mov_b32 v97, v96\n"
  "v_mov_b32 v101, v100\n"
  "v_cvt_pk_bf16_f32 v50, v92, v93\n"
  "v_permlane16_swap_b32 v96, v97\n"
  "v_permlane16_swap_b32 v100, v101\n"
  "v_add_f32 v96, v96, v97\n"
  "v_add_f32 v100, v100, v101\n"
  "v_mov_b32 v97, v96\n"
  "v_mov_b32 v101, v100\n"
  "v_cvt_pk_bf16_f32 v51, v94, v95\n"
  "v_permlane32_swap_b32 v96, v97\n"
  "v_permlane32_swap_b32 v100, v101\n"
  "v_add_f32 v96, v96, v97\n"
  "v_add_f32 v100, v100, v101\n"
  "buffer_store_dwordx4 v[48:51], v121, s[36:39], s61 offen offset:768 sc1\n"
  "v_mul_f32 v96, 0x3b000000, v96\n"
  "v_mul_f32 v100, 0x3b000000, v100\n"
  "v_fma_f32 v100, -v96, v96, v100\n"
  "v_max_f32 v100, 0, v100\n"
  "v_add_f32 v100, 0x3727c5ac, v100\n"
  "v_rsq_f32 v100, v100\n"
  "s_nop 0\n"
  "v_mul_f32 v96, v96, v100\n"
  "v_pk_fma_f32 v[64:65], v[64:65], v[100:101], v[96:97] op_sel_hi:[1,0,0] neg_lo:[0,0,1] neg_hi:[0,0,1]\n"
  "v_pk_fma_f32 v[66:67], v[66:67], v[100:101], v[96:97] op_sel_hi:[1,0,0] neg_lo:[0,0,1] neg_hi:[0,0,1]\n"
  "v_pk_fma_f32 v[68:69], v[68:69], v[100:101], v[96:97] op_sel_hi:[1,0,0] neg_lo:[0,0,1] neg_hi:[0,0,1]\n"
  "v_pk_fma_f32 v[70:71], v[70:71], v[100:101], v[96:97] op_sel_hi:[1,0,0] neg_lo:[0,0,1] neg_hi:[0,0,1]\n"
  "v_cvt_pk_bf16_f32 v4, v64, v65\n"
  "v_cvt_pk_bf16_f32 v5, v66, v67\n"
  "v_cvt_pk_bf16_f32 v6, v68, v69\n"
  "v_cvt_pk_bf16_f32 v7, v70, v71\n"
  "buffer_store_dwordx4 v[4:7], v121, s[36:39], s60 offen offset:0 sc1\n"
  "v_pk_fma_f32 v[72:73], v[72:73], v[100:101], v[96:97] op_sel_hi:[1,0,0] neg_lo:[0,0,1] neg_hi:[0,0,1]\n"
  "v_pk_fma_f32 v[74:75], v[74:75], v[100:101], v[96:97] op_sel_hi:[1,0,0] neg_lo:[0,0,1] neg_hi:[0,0,1]\n"
  "v_pk_fma_f32 v[76:77], v[76:77], v[100:101], v[96:97] op_sel_hi:[1,0,0] neg_lo:[0,0,1] neg_hi:[0,0,1]\n"
  "v_pk_fma_f32 v[78:79], v[78:79], v[100:101], v[96:97] op_sel_hi:[1,0,0] neg_lo:[0,0,1] neg_hi:[0,0,1]\n"
  "v_cvt_pk_bf16_f32 v20, v72, v73\n"
  "v_cvt_pk_bf16_f32 v21, v74, v75\n"
  "v_cvt_pk_bf16_f32 v22, v76, v77\n"
  "v_cvt_pk_bf16_f32 v23, v78, v79\n"
  "buffer_store_dwordx4 v[20:23], v121, s[36:39], s60 offen offset:256 sc1\n"
  "v_pk_fma_f32 v[80:81], v[80:81], v[100:101], v[96:97] op_sel_hi:[1,0,0] neg_lo:[0,0,1] neg_hi:[0,0,1]\n"
  "v_pk_fma_f32 v[82:83], v[82:83], v[100:101], v[96:97] op_sel_hi:[1,0,0] neg_lo:[0,0,1] neg_hi:[0,0,1]\n"
  "v_pk_fma_f32 v[84:85], v[84:85], v[100:101], v[96:97] op_sel_hi:[1,0,0] neg_lo:[0,0,1] neg_hi:[0,0,1]\n"
  "v_pk_fma_f32 v[86:87], v[86:87], v[100:101], v[96:97] op_sel_hi:[1,0,0] neg_lo:[0,0,1] neg_hi:[0,0,1]\n"
  "v_cvt_pk_bf16_f32 v36, v80, v81\n"
  "v_cvt_pk_bf16_f32 v37, v82, v83\n"
  "v_cvt_pk_bf16_f32 v38, v84, v85\n"
  "v_cvt_pk_bf16_f32 v39, v86, v87\n"
  "buffer_store_dwordx4 v[36:39], v121, s[36:39], s60 offen offset:512 sc1\n"
  "v_pk_fma_f32 v[88:89], v[88:89], v[100:101], v[96:97] op_sel_hi:[1,0,0] neg_lo:[0,0,1] neg_hi:[0,0,1]\n"
  "v_pk_fma_f32 v[90:91], v[90:91], v[100:101], v[96:97] op_sel_hi:[1,0,0] neg_lo:[0,0,1] neg_hi:[0,0,1]\n"
  "v_pk_fma_f32 v[92:93], v[92:93], v[100:101], v[96:97] op_sel_hi:[1,0,0] neg_lo:[0,0,1] neg_hi:[0,0,1]\n"
  "v_pk_fma_f32 v[94:95], v[94:95], v[100:101], v[96:97] op_sel_hi:[1,0,0] neg_lo:[0,0,1] neg_hi:[0,0,1]\n"
  "v_cvt_pk_bf16_f32 v52, v88, v89\n"
  "v_cvt_pk_bf16_f32 v53, v90, v91\n"
  "v_cvt_pk_bf16_f32 v54, v92, v93\n"
  "v_cvt_pk_bf16_f32 v55, v94, v95\n"
  "buffer_store_dwordx4 v[52:55], v121, s[36:39], s60 offen offset:768 sc1\n"
  "v_mov_b32 v128, s55\n"
  "s_waitcnt vmcnt(0)\n"
  "buffer_store_dword v128, v124, s[36:39], 0 offen sc1\n"
  "s_add_u32 s51, s51, 1\n"
  "s_lshr_b32 s53, s51, 3\n"
  "s_xor_b32 s53, s53, s50\n"
  "s_and_b32 s53, s53, 1\n"
  "s_lshl_b32 s53, s53, 13\n"
  "s_and_b32 s60, s51, 31\n"
  "s_lshl_b32 s60, s60, 18\n"
  "s_add_u32 s60, s60, 201326592\n"
  "s_or_b32 s60, s60, s53\n"
  "buffer_store_dwordx4 v[136:139], v121, s[36:39], s60 offen offset:0 sc1\n"
  "buffer_store_dwordx4 v[136:139], v121, s[36:39], s60 offen offset:256 sc1\n"
  "buffer_store_dwordx4 v[136:139], v121, s[36:39], s60 offen offset:512 sc1\n"
  "buffer_store_dwordx4 v[136:139], v121, s[36:39], s60 offen offset:768 sc1\n"
  "s_cmp_lt_u32 s51, 64\n"
  "s_cbranch_scc1 .Lra_loop\n"
  ".Lra_end:\n"
  "s_endpgm\n"
  ".Lra_m0:\n"
  "s_sub_u32 s52, s52, 1\n"
  "s_cbranch_scc1 .Lra_end\n"
  "s_sleep 1\n"
  "s_waitcnt lgkmcnt(0)\n"
  "buffer_load_dwordx4 v[0:3], v120, s[36:39], s59 offen offset:0 sc1\n"
  "buffer_load_dwordx4 v[4:7], v120, s[36:39], s66 offen offset:0 sc1\n"
  "buffer_load_dwordx4 v[8:11], v120, s[36:39], s67 offen offset:0 sc1\n"
  "buffer_load_dwordx4 v[12:15], v120, s[36:39], s68 offen offset:0 sc1\n"
  "s_waitcnt vmcnt(0)\n"
  "s_branch .Lra_c0\n"
  ".Lra_m1:\n"
  "s_sub_u32 s52, s52, 1\n"
  "s_cbranch_scc1 .Lra_end\n"
  "s_sleep 1\n"
  "s_waitcnt lgkmcnt(0)\n"
  "buffer_load_dwordx4 v[16:19], v120, s[36:39], s59 offen offset:256 sc1\n"
  "buffer_load_dwordx4 v[20:23], v120, s[36:39], s66 offen offset:256 sc1\n"
  "buffer_load_dwordx4 v[24:27], v120, s[36:39], s67 offen offset:256 sc1\n"
  "buffer_load_dwordx4 v[28:31], v120, s[36:39], s68 offen offset:256 sc1\n"
  "s_waitcnt vmcnt(0)\n"
  "s_branch .Lra_c1\n"
  ".Lra_m2:\n"
  "s_sub_u32 s52, s52, 1\n"
  "s_cbranch_scc1 .Lra_end\n"
  "s_sleep 1\n"
  "s_waitcnt lgkmcnt(0)\n"
  "buffer_load_dwordx4 v[32:35], v120, s[36:39], s59 offen offset:512 sc1\n"
  "buffer_load_dwordx4 v[36:39], v120, s[36:39], s66 offen offset:512 sc1\n"
  "buffer_load_dwordx4 v[40:43], v120, s[36:39], s67 offen offset:512 sc1\n"
  "buffer_load_dwordx4 v[44:47], v120, s[36:39], s68 offen offset:512 sc1\n"
  "s_waitcnt vmcnt(0)\n"
  "s_branch .Lra_c2\n"
  ".Lra_m3:\n"
  "s_sub_u32 s52, s52, 1\n"
  "s_cbranch_scc1 .Lra_end\n"
  "s_sleep 1\n"
  "s_waitcnt lgkmcnt(0)\n"
  "buffer_load_dwordx4 v[48:51], v120, s[36:39], s59 offen offset:768 sc1\n"
  "buffer_load_dwordx4 v[52:55], v120, s[36:39], s66 offen offset:768 sc1\n"
  "buffer_load_dwordx4 v[56:59], v120, s[36:39], s67 offen offset:768 sc1\n"
  "buffer_load_dwordx4 v[60:63], v120, s[36:39], s68 offen offset:768 sc1\n"
  "s_waitcnt vmcnt(0)\n"
  "s_branch .Lra_c3\n"
  ".Lra_lim:\n"
      :: "{v[112:115]}"(mksel(li)), "{v[116:119]}"(mksel(li + 32)), "{v120}"(g * 65536 + j * 1024 + li * 16), "{v121}"(sp(row * 1024) + (4 * g + q) * 16),
        "{v122}"(row * 1024 + (128 * (g >> 1) + 16 * (g & 1) + 4 * q) * 2), "{v123}"(ly && lane >= 32 ? fl(l ^ 1 ^ XNC, F2_XN + 4 * j + q) : fl(l, FL_OP + j * 16 + li)),
        "{v124}"(lane < RX ? fl(l, (HSM & 2 ? FL_XF : F2_XF) + lane * 8 + j) : OOR), "{s[40:41]}"((u64)x), "{s58}"(RFL(ly)), COMMON_IN NORET;
}

DEV void role_chain(Ctx& C, char* ws, int l, int h, int w, int lane, const u16* mem0, const u16* Wq, const u16* Wk, const u16* Wv, const u16* Wo) {
  const int g = lane >> 4, li = lane & 15, c = w & 1;
  const u32 cl = l, sel = l;
  const u32 cpbase = OFF_HOP + l * (4u << 20), mbase = cpbase + (1u << 20) + sel * 8192;
#define INBOX(r) ((r) * 32768 + ((sel ^ (((r) >> 4) * 2) ^ (SPREAD * ((r) & 1) * 2)) * 8192))
  constexpr u32 LQK = 24576;
  const u32 reg = 32768 + w * 32768, q = li >> 2, p = li & 3;
  const u32 rw = 16 * (q & 1) + 4 * (g >> 1) + 2 * (q >> 1) + (g & 1), ro = 8 * (g >> 1) + 4 * (g & 1) + (q & 1);
  u32 g64 = (lane >> 2) * 8192 + p * 16, g128 = (lane >> 3) * 1024 + (lane & 7) * 16;
  u32 ta = reg + rw * 64 + p * 8, tv = reg + rw * 64 + p * 16, to = reg + ro * 128 + (q >> 1) * 32 + p * 8 + c * 4096;
  u32 m0o = ((2 * q + (li & 1)) * 512 + 128 * w + 4 * g + (li & 2)) * 2;
  u32 fcp = FL_C0 + (l * 32 + (lane & 31)) * 128;
  const u32 idx = (lane & 2) ? 0xEEEE : 0x4444, ll = lane * 16, lw = reg + ll;
  const u32 qw = LQK + (g & 1) * 1056 + (g >> 1) * 2176 + (32 * w + li) * 8, qr = LQK + (p & 1) * 1056 + (p >> 1) * 2176 + (8 * g + q) * 8;
  const u32 ow = to ^ 4096;
  const u32 pst = INBOX(8 * w + q) + (g >> 1) * 4096 + h * 128 + (g & 1) * 64 + p * 16;
#define LKVS cl
  const bool master = h == 0 && w == 1;
  const u32 fa = lane >= 48 ? fl(cl, FL_CTL) + (lane & 1) * 4 : master && lane >= 16 ? fl(cl, FL_CLEAN + lane - 16) + 4 : fl(LKVS, FL_KV + h * 8 + (w >> 1) * 4 + (lane & 3));
  const u32 w2 = ll + w * 2048, vmw = w * 512 + (lane >> 2) * 32 + (lane & 1) * 16 + (lane & 2) * 4;
  u4v r4; u32 rx;
  if (w < 2) {
#pragma unroll
    for (int d = 0; d < 4; ++d) r4[d] = ((li & 7) == 2 * d) ? 0x3f80u : ((li & 7) == 2 * d + 1) ? 0x3f800000u : 0u;
    rx = (g < 2 && li < 8) ? h * 256 + 128 * c + 64 * q + 16 * (li & 2) + 16 * g + 8 * (li & 1) : OOR;
  } else {
#pragma unroll
    for (int d = 0; d < 4; ++d) r4[d] = (4 * g + d <= li) ? 0u : asu(-3.0e38f);
    rx = lane == 0 ? fl(cl, FL_O + h * 2 + c) : OOR;
  }
  const u64 wsa = (u64)ws;
  const i4v rs = {(int)wsa, (int)(wsa >> 32), (int)WS_USED, 0x00020000};
  const u32 wo = RFL(h * 256 + 64 * w);
  u64 pk_ = PTR(Wk, wo), pv_ = PTR(Wv, wo), pq_ = PTR(Wq, wo), po_ = PTR(Wo, RFL(h * 131072 + 256 * w));
  u32 skv = RFL(aQKV(0, LKVS) + h * 49152 + (w < 2 ? 16384 : c * 4096));
  u32 sm = RFL(mbase), so = RFL(aO(0, cl) + h * 16384 + c * 4096), need = RFL(w < 2 ? 2 : 1), st = 0, bud = C.budget;
  u32 dbg = RFL(OFF_DBG + (BID * 4 + w) * 64 * 64), r1 = 0, r2 = 0;
  asm volatile(
  "s_nop 4\n"
  "v_mov_b32 v120, 0\n"
  "buffer_load_dword v16, off, s[36:39], s92 sc1\n"
  "s_cmp_eq_u32 s93, 0\n"
  "s_cbranch_scc1 .Lch_d1\n"
  "s_memrealtime s[70:71]\n"
  "s_waitcnt lgkmcnt(0)\n"
  "s_add_u32 s93, s93, s70\n"
  ".Lch_d0:\n"
  "s_sleep 8\n"
  "s_memrealtime s[70:71]\n"
  "s_waitcnt lgkmcnt(0)\n"
  "s_sub_u32 s70, s70, s93\n"
  "s_cmp_lt_i32 s70, 0\n"
  "s_cbranch_scc1 .Lch_d0\n"
  ".Lch_d1:\n"
  "s_add_u32 m0, s51, 0\n"
  "s_xor_b32 s61, s50, 0\n"
  "s_lshl_b32 s61, s61, 20\n"
  "s_add_u32 s70, s40, s61\n"
  "s_addc_u32 s71, s41, 0\n"
  "global_load_lds_dwordx4 v96, s[70:71]\n"
  "s_add_u32 s70, s70, 131072\n"
  "s_addc_u32 s71, s71, 0\n"
  "s_add_u32 m0, m0, 1024\n"
  "s_nop 0\n"
  "global_load_lds_dwordx4 v96, s[70:71]\n"
  "s_add_u32 s70, s70, 131072\n"
  "s_addc_u32 s71, s71, 0\n"
  "s_add_u32 m0, m0, 1024\n"
  "s_nop 0\n"
  "global_load_lds_dwordx4 v96, s[70:71]\n"
  "s_add_u32 s70, s70, 131072\n"
  "s_addc_u32 s71, s71, 0\n"
  "s_add_u32 m0, m0, 1024\n"
  "s_nop 0\n"
  "global_load_lds_dwordx4 v96, s[70:71]\n"
  "s_add_u32 s70, s70, 131072\n"
  "s_addc_u32 s71, s71, 0\n"
  "s_add_u32 m0, m0, 1024\n"
  "s_nop 0\n"
  "global_load_lds_dwordx4 v96, s[70:71]\n"
  "s_add_u32 s70, s70, 131072\n"
  "s_addc_u32 s71, s71, 0\n"
  "s_add_u32 m0, m0, 1024\n"
  "s_nop 0\n"
  "global_load_lds_dwordx4 v96, s[70:71]\n"
  "s_add_u32 s70, s70, 131072\n"
  "s_addc_u32 s71, s71, 0\n"
  "s_add_u32 m0, m0, 1024\n"
  "s_nop 0\n"
  "global_load_lds_dwordx4 v96, s[70:71]\n"
  "s_add_u32 s70, s70, 131072\n"
  "s_addc_u32 s71, s71, 0\n"
  "s_add_u32 m0, m0, 1024\n"
  "s_nop 0\n"
  "global_load_lds_dwordx4 v96, s[70:71]\n"
  "s_add_u32 m0, s51, 8192\n"
  "s_xor_b32 s61, s50, 1\n"
  "s_lshl_b32 s61, s61, 20\n"
  "s_add_u32 s70, s40, s61\n"
  "s_addc_u32 s71, s41, 0\n"
  "global_load_lds_dwordx4 v96, s[70:71]\n"
  "s_add_u32 s70, s70, 131072\n"
  "s_addc_u32 s71, s71, 0\n"
  "s_add_u32 m0, m0, 1024\n"
  "s_nop 0\n"
  "global_load_lds_dwordx4 v96, s[70:71]\n"
  "s_add_u32 s70, s70, 131072\n"
  "s_addc_u32 s71, s71, 0\n"
  "s_add_u32 m0, m0, 1024\n"
  "s_nop 0\n"
  "global_load_lds_dwordx4 v96, s[70:71]\n"
  "s_add_u32 s70, s70, 131072\n"
  "s_addc_u32 s71, s71, 0\n"
  "s_add_u32 m0, m0, 1024\n"
  "s_nop 0\n"
  "global_load_lds_dwordx4 v96, s[70:71]\n"
  "s_add_u32 s70, s70, 131072\n"
  "s_addc_u32 s71, s71, 0\n"
  "s_add_u32 m0, m0, 1024\n"
  "s_nop 0\n"
  "global_load_lds_dwordx4 v96, s[70:71]\n"
  "s_add_u32 s70, s70, 131072\n"
  "s_addc_u32 s71, s71, 0\n"
  "s_add_u32 m0, m0, 1024\n"
  "s_nop 0\n"
  "global_load_lds_dwordx4 v96, s[70:71]\n"
  "s_add_u32 s70, s70, 131072\n"
  "s_addc_u32 s71, s71, 0\n"
  "s_add_u32 m0, m0, 1024\n"
  "s_nop 0\n"
  "global_load_lds_dwordx4 v96, s[70:71]\n"
  "s_add_u32 s70, s70, 131072\n"
  "s_addc_u32 s71, s71, 0\n"
  "s_add_u32 m0, m0, 1024\n"
  "s_nop 0\n"
  "global_load_lds_dwordx4 v96, s[70:71]\n"
  "s_add_u32 m0, s51, 16384\n"
  "s_xor_b32 s61, s50, 2\n"
  "s_lshl_b32 s61, s61, 20\n"
  "s_add_u32 s70, s40, s61\n"
  "s_addc_u32 s71, s41, 0\n"
  "global_load_lds_dwordx4 v96, s[70:71]\n"
  "s_add_u32 s70, s70, 131072\n"
  "s_addc_u32 s71, s71, 0\n"
  "s_add_u32 m0, m0, 1024\n"
  "s_nop 0\n"
  "global_load_lds_dwordx4 v96, s[70:71]\n"
  "s_add_u32 s70, s70, 131072\n"
  "s_addc_u32 s71, s71, 0\n"
  "s_add_u32 m0, m0, 1024\n"
  "s_nop 0\n"
  "global_load_lds_dwordx4 v96, s[70:71]\n"
  "s_add_u32 s70, s70, 131072\n"
  "s_addc_u32 s71, s71, 0\n"
  "s_add_u32 m0, m0, 1024\n"
  "s_nop 0\n"
  "global_load_lds_dwordx4 v96, s[70:71]\n"
  "s_add_u32 s70, s70, 131072\n"
  "s_addc_u32 s71, s71, 0\n"
  "s_add_u32 m0, m0, 1024\n"
  "s_nop 0\n"
  "global_load_lds_dwordx4 v96, s[70:71]\n"
  "s_add_u32 s70, s70, 131072\n"
  "s_addc_u32 s71, s71, 0\n"
  "s_add_u32 m0, m0, 1024\n"
  "s_nop 0\n"
  "global_load_lds_dwordx4 v96, s[70:71]\n"
  "s_add_u32 s70, s70, 131072\n"
  "s_addc_u32 s71, s71, 0\n"
  "s_add_u32 m0, m0, 1024\n"
  "s_nop 0\n"
  "global_load_lds_dwordx4 v96, s[70:71]\n"
  "s_add_u32 s70, s70, 131072\n"
  "s_addc_u32 s71, s71, 0\n"
  "s_add_u32 m0, m0, 1024\n"
  "s_nop 0\n"
  "global_load_lds_dwordx4 v96, s[70:71]\n"
  "s_add_u32 m0, s51, 24576\n"
  "s_xor_b32 s61, s50, 3\n"
  "s_lshl_b32 s61, s61, 20\n"
  "s_add_u32 s70, s40, s61\n"
  "s_addc_u32 s71, s41, 0\n"
  "global_load_lds_dwordx4 v96, s[70:71]\n"
  "s_add_u32 s70, s70, 131072\n"
  "s_addc_u32 s71, s71, 0\n"
  "s_add_u32 m0, m0, 1024\n"
  "s_nop 0\n"
  "global_load_lds_dwordx4 v96, s[70:71]\n"
  "s_add_u32 s70, s70, 131072\n"
  "s_addc_u32 s71, s71, 0\n"
  "s_add_u32 m0, m0, 1024\n"
  "s_nop 0\n"
  "global_load_lds_dwordx4 v96, s[70:71]\n"
  "s_add_u32 s70, s70, 131072\n"
  "s_addc_u32 s71, s71, 0\n"
  "s_add_u32 m0, m0, 1024\n"
  "s_nop 0\n"
  "global_load_lds_dwordx4 v96, s[70:71]\n"
  "s_add_u32 s70, s70, 131072\n"
  "s_addc_u32 s71, s71, 0\n"
  "s_add_u32 m0, m0, 1024\n"
  "s_nop 0\n"
  "global_load_lds_dwordx4 v96, s[70:71]\n"
  "s_add_u32 s70, s70, 131072\n"
  "s_addc_u32 s71, s71, 0\n"
  "s_add_u32 m0, m0, 1024\n"
  "s_nop 0\n"
  "global_load_lds_dwordx4 v96, s[70:71]\n"
  "s_add_u32 s70, s70, 131072\n"
  "s_addc_u32 s71, s71, 0\n"
  "s_add_u32 m0, m0, 1024\n"
  "s_nop 0\n"
  "global_load_lds_dwordx4 v96, s[70:71]\n"
  "s_add_u32 s70, s70, 131072\n"
  "s_addc_u32 s71, s71, 0\n"
  "s_add_u32 m0, m0, 1024\n"
  "s_nop 0\n"
  "global_load_lds_dwordx4 v96, s[70:71]\n"
  "global_load_dword v8, v101, s[48:49] offset:0\n"
  "global_load_dword v9, v101, s[48:49] offset:32\n"
  "global_load_dword v10, v101, s[48:49] offset:64\n"
  "global_load_dword v11, v101, s[48:49] offset:96\n"
  "global_load_dword v12, v101, s[48:49] offset:128\n"
  "global_load_dword v13, v101, s[48:49] offset:160\n"
  "global_load_dword v14, v101, s[48:49] offset:192\n"
  "global_load_dword v15, v101, s[48:49] offset:224\n"
  "ds_write_b32 v113, v120 offset:30912\n"
  "s_waitcnt vmcnt(32)\n"
  "v_readfirstlane_b32 s93, v16\n"
  "ds_read_b64_tr_b16 a[0:1], v98 offset:0\n"
  "ds_read_b64_tr_b16 a[2:3], v98 offset:2048\n"
  "ds_read_b64_tr_b16 a[4:5], v98 offset:512\n"
  "ds_read_b64_tr_b16 a[6:7], v98 offset:2560\n"
  "ds_read_b64_tr_b16 a[64:65], v98 offset:32\n"
  "ds_read_b64_tr_b16 a[66:67], v98 offset:2080\n"
  "ds_read_b64_tr_b16 a[68:69], v98 offset:544\n"
  "ds_read_b64_tr_b16 a[70:71], v98 offset:2592\n"
  "ds_read_b64_tr_b16 a[8:9], v98 offset:4096\n"
  "ds_read_b64_tr_b16 a[10:11], v98 offset:6144\n"
  "ds_read_b64_tr_b16 a[12:13], v98 offset:4608\n"
  "ds_read_b64_tr_b16 a[14:15], v98 offset:6656\n"
  "ds_read_b64_tr_b16 a[72:73], v98 offset:4128\n"
  "ds_read_b64_tr_b16 a[74:75], v98 offset:6176\n"
  "ds_read_b64_tr_b16 a[76:77], v98 offset:4640\n"
  "ds_read_b64_tr_b16 a[78:79], v98 offset:6688\n"
  "s_waitcnt lgkmcnt(0)\n"
  "s_add_u32 m0, s51, 0\n"
  "s_xor_b32 s61, s50, 0\n"
  "s_lshl_b32 s61, s61, 20\n"
  "s_add_u32 s70, s42, s61\n"
  "s_addc_u32 s71, s43, 0\n"
  "global_load_lds_dwordx4 v96, s[70:71]\n"
  "s_add_u32 s70, s70, 131072\n"
  "s_addc_u32 s71, s71, 0\n"
  "s_add_u32 m0, m0, 1024\n"
  "s_nop 0\n"
  "global_load_lds_dwordx4 v96, s[70:71]\n"
  "s_add_u32 s70, s70, 131072\n"
  "s_addc_u32 s71, s71, 0\n"
  "s_add_u32 m0, m0, 1024\n"
  "s_nop 0\n"
  "global_load_lds_dwordx4 v96, s[70:71]\n"
  "s_add_u32 s70, s70, 131072\n"
  "s_addc_u32 s71, s71, 0\n"
  "s_add_u32 m0, m0, 1024\n"
  "s_nop 0\n"
  "global_load_lds_dwordx4 v96, s[70:71]\n"
  "s_add_u32 s70, s70, 131072\n"
  "s_addc_u32 s71, s71, 0\n"
  "s_add_u32 m0, m0, 1024\n"
  "s_nop 0\n"
  "global_load_lds_dwordx4 v96, s[70:71]\n"
  "s_add_u32 s70, s70, 131072\n"
  "s_addc_u32 s71, s71, 0\n"
  "s_add_u32 m0, m0, 1024\n"
  "s_nop 0\n"
  "global_load_lds_dwordx4 v96, s[70:71]\n"
  "s_add_u32 s70, s70, 131072\n"
  "s_addc_u32 s71, s71, 0\n"
  "s_add_u32 m0, m0, 1024\n"
  "s_nop 0\n"
  "global_load_lds_dwordx4 v96, s[70:71]\n"
  "s_add_u32 s70, s70, 131072\n"
  "s_addc_u32 s71, s71, 0\n"
  "s_add_u32 m0, m0, 1024\n"
  "s_nop 0\n"
  "global_load_lds_dwordx4 v96, s[70:71]\n"
  "s_waitcnt vmcnt(32)\n"
  "ds_read_b64_tr_b16 a[16:17], v98 offset:8192\n"
  "ds_read_b64_tr_b16 a[18:19], v98 offset:10240\n"
  "ds_read_b64_tr_b16 a[20:21], v98 offset:8704\n"
  "ds_read_b64_tr_b16 a[22:23], v98 offset:10752\n"
  "ds_read_b64_tr_b16 a[80:81], v98 offset:8224\n"
  "ds_read_b64_tr_b16 a[82:83], v98 offset:10272\n"
  "ds_read_b64_tr_b16 a[84:85], v98 offset:8736\n"
  "ds_read_b64_tr_b16 a[86:87], v98 offset:10784\n"
  "ds_read_b64_tr_b16 a[24:25], v98 offset:12288\n"
  "ds_read_b64_tr_b16 a[26:27], v98 offset:14336\n"
  "ds_read_b64_tr_b16 a[28:29], v98 offset:12800\n"
  "ds_read_b64_tr_b16 a[30:31], v98 offset:14848\n"
  "ds_read_b64_tr_b16 a[88:89], v98 offset:12320\n"
  "ds_read_b64_tr_b16 a[90:91], v98 offset:14368\n"
  "ds_read_b64_tr_b16 a[92:93], v98 offset:12832\n"
  "ds_read_b64_tr_b16 a[94:95], v98 offset:14880\n"
  "s_waitcnt lgkmcnt(0)\n"
  "s_add_u32 m0, s51, 8192\n"
  "s_xor_b32 s61, s50, 1\n"
  "s_lshl_b32 s61, s61, 20\n"
  "s_add_u32 s70, s42, s61\n"
  "s_addc_u32 s71, s43, 0\n"
  "global_load_lds_dwordx4 v96, s[70:71]\n"
  "s_add_u32 s70, s70, 131072\n"
  "s_addc_u32 s71, s71, 0\n"
  "s_add_u32 m0, m0, 1024\n"
  "s_nop 0\n"
  "global_load_lds_dwordx4 v96, s[70:71]\n"
  "s_add_u32 s70, s70, 131072\n"
  "s_addc_u32 s71, s71, 0\n"
  "s_add_u32 m0, m0, 1024\n"
  "s_nop 0\n"
  "global_load_lds_dwordx4 v96, s[70:71]\n"
  "s_add_u32 s70, s70, 131072\n"
  "s_addc_u32 s71, s71, 0\n"
  "s_add_u32 m0, m0, 1024\n"
  "s_nop 0\n"
  "global_load_lds_dwordx4 v96, s[70:71]\n"
  "s_add_u32 s70, s70, 131072\n"
  "s_addc_u32 s71, s71, 0\n"
  "s_add_u32 m0, m0, 1024\n"
  "s_nop 0\n"
  "global_load_lds_dwordx4 v96, s[70:71]\n"
  "s_add_u32 s70, s70, 131072\n"
  "s_addc_u32 s71, s71, 0\n"
  "s_add_u32 m0, m0, 1024\n"
  "s_nop 0\n"
  "global_load_lds_dwordx4 v96, s[70:71]\n"
  "s_add_u32 s70, s70, 131072\n"
  "s_addc_u32 s71, s71, 0\n"
  "s_add_u32 m0, m0, 1024\n"
  "s_nop 0\n"
  "global_load_lds_dwordx4 v96, s[70:71]\n"
  "s_add_u32 s70, s70, 131072\n"
  "s_addc_u32 s71, s71, 0\n"
  "s_add_u32 m0, m0, 1024\n"
  "s_nop 0\n"
  "global_load_lds_dwordx4 v96, s[70:71]\n"
  "s_waitcnt vmcnt(32)\n"
  "ds_read_b64_tr_b16 a[32:33], v98 offset:16384\n"
  "ds_read_b64_tr_b16 a[34:35], v98 offset:18432\n"
  "ds_read_b64_tr_b16 a[36:37], v98 offset:16896\n"
  "ds_read_b64_tr_b16 a[38:39], v98 offset:18944\n"
  "ds_read_b64_tr_b16 a[96:97], v98 offset:16416\n"
  "ds_read_b64_tr_b16 a[98:99], v98 offset:18464\n"
  "ds_read_b64_tr_b16 a[100:101], v98 offset:16928\n"
  "ds_read_b64_tr_b16 a[102:103], v98 offset:18976\n"
  "ds_read_b64_tr_b16 a[40:41], v98 offset:20480\n"
  "ds_read_b64_tr_b16 a[42:43], v98 offset:22528\n"
  "ds_read_b64_tr_b16 a[44:45], v98 offset:20992\n"
  "ds_read_b64_tr_b16 a[46:47], v98 offset:23040\n"
  "ds_read_b64_tr_b16 a[104:105], v98 offset:20512\n"
  "ds_read_b64_tr_b16 a[106:107], v98 offset:22560\n"
  "ds_read_b64_tr_b16 a[108:109], v98 offset:21024\n"
  "ds_read_b64_tr_b16 a[110:111], v98 offset:23072\n"
  "s_waitcnt lgkmcnt(0)\n"
  "s_add_u32 m0, s51, 16384\n"
  "s_xor_b32 s61, s50, 2\n"
  "s_lshl_b32 s61, s61, 20\n"
  "s_add_u32 s70, s42, s61\n"
  "s_addc_u32 s71, s43, 0\n"
  "global_load_lds_dwordx4 v96, s[70:71]\n"
  "s_add_u32 s70, s70, 131072\n"
  "s_addc_u32 s71, s71, 0\n"
  "s_add_u32 m0, m0, 1024\n"
  "s_nop 0\n"
  "global_load_lds_dwordx4 v96, s[70:71]\n"
  "s_add_u32 s70, s70, 131072\n"
  "s_addc_u32 s71, s71, 0\n"
  "s_add_u32 m0, m0, 1024\n"
  "s_nop 0\n"
  "global_load_lds_dwordx4 v96, s[70:71]\n"
  "s_add_u32 s70, s70, 131072\n"
  "s_addc_u32 s71, s71, 0\n"
  "s_add_u32 m0, m0, 1024\n"
  "s_nop 0\n"
  "global_load_lds_dwordx4 v96, s[70:71]\n"
  "s_add_u32 s70, s70, 131072\n"
  "s_addc_u32 s71, s71, 0\n"
  "s_add_u32 m0, m0, 1024\n"
  "s_nop 0\n"
  "global_load_lds_dwordx4 v96, s[70:71]\n"
  "s_add_u32 s70, s70, 131072\n"
  "s_addc_u32 s71, s71, 0\n"
  "s_add_u32 m0, m0, 1024\n"
  "s_nop 0\n"
  "global_load_lds_dwordx4 v96, s[70:71]\n"
  "s_add_u32 s70, s70, 131072\n"
  "s_addc_u32 s71, s71, 0\n"
  "s_add_u32 m0, m0, 1024\n"
  "s_nop 0\n"
  "global_load_lds_dwordx4 v96, s[70:71]\n"
  "s_add_u32 s70, s70, 131072\n"
  "s_addc_u32 s71, s71, 0\n"
  "s_add_u32 m0, m0, 1024\n"
  "s_nop 0\n"
  "global_load_lds_dwordx4 v96, s[70:71]\n"
  "s_waitcnt vmcnt(32)\n"
  "ds_read_b64_tr_b16 a[48:49], v98 offset:24576\n"
  "ds_read_b64_tr_b16 a[50:51], v98 offset:26624\n"
  "ds_read_b64_tr_b16 a[52:53], v98 offset:25088\n"
  "ds_read_b64_tr_b16 a[54:55], v98 offset:27136\n"
  "ds_read_b64_tr_b16 a[112:113], v98 offset:24608\n"
  "ds_read_b64_tr_b16 a[114:115], v98 offset:26656\n"
  "ds_read_b64_tr_b16 a[116:117], v98 offset:25120\n"
  "ds_read_b64_tr_b16 a[118:119], v98 offset:27168\n"
  "ds_read_b64_tr_b16 a[56:57], v98 offset:28672\n"
  "ds_read_b64_tr_b16 a[58:59], v98 offset:30720\n"
  "ds_read_b64_tr_b16 a[60:61], v98 offset:29184\n"
  "ds_read_b64_tr_b16 a[62:63], v98 offset:31232\n"
  "ds_read_b64_tr_b16 a[120:121], v98 offset:28704\n"
  "ds_read_b64_tr_b16 a[122:123], v98 offset:30752\n"
  "ds_read_b64_tr_b16 a[124:125], v98 offset:29216\n"
  "ds_read_b64_tr_b16 a[126:127], v98 offset:31264\n"
  "s_waitcnt lgkmcnt(0)\n"
  "s_add_u32 m0, s51, 24576\n"
  "s_xor_b32 s61, s50, 3\n"
  "s_lshl_b32 s61, s61, 20\n"
  "s_add_u32 s70, s42, s61\n"
  "s_addc_u32 s71, s43, 0\n"
  "global_load_lds_dwordx4 v96, s[70:71]\n"
  "s_add_u32 s70, s70, 131072\n"
  "s_addc_u32 s71, s71, 0\n"
  "s_add_u32 m0, m0, 1024\n"
  "s_nop 0\n"
  "global_load_lds_dwordx4 v96, s[70:71]\n"
  "s_add_u32 s70, s70, 131072\n"
  "s_addc_u32 s71, s71, 0\n"
  "s_add_u32 m0, m0, 1024\n"
  "s_nop 0\n"
  "global_load_lds_dwordx4 v96, s[70:71]\n"
  "s_add_u32 s70, s70, 131072\n"
  "s_addc_u32 s71, s71, 0\n"
  "s_add_u32 m0, m0, 1024\n"
  "s_nop 0\n"
  "global_load_lds_dwordx4 v96, s[70:71]\n"
  "s_add_u32 s70, s70, 131072\n"
  "s_addc_u32 s71, s71, 0\n"
  "s_add_u32 m0, m0, 1024\n"
  "s_nop 0\n"
  "global_load_lds_dwordx4 v96, s[70:71]\n"
  "s_add_u32 s70, s70, 131072\n"
  "s_addc_u32 s71, s71, 0\n"
  "s_add_u32 m0, m0, 1024\n"
  "s_nop 0\n"
  "global_load_lds_dwordx4 v96, s[70:71]\n"
  "s_add_u32 s70, s70, 131072\n"
  "s_addc_u32 s71, s71, 0\n"
  "s_add_u32 m0, m0, 1024\n"
  "s_nop 0\n"
  "global_load_lds_dwordx4 v96, s[70:71]\n"
  "s_add_u32 s70, s70, 131072\n"
  "s_addc_u32 s71, s71, 0\n"
  "s_add_u32 m0, m0, 1024\n"
  "s_nop 0\n"
  "global_load_lds_dwordx4 v96, s[70:71]\n"
  "s_waitcnt vmcnt(24)\n"
  "s_mov_b32 s61, 0x05040100\n"
  "s_mov_b32 s65, 0x07060302\n"
  "v_perm_b32 v104, v9, v8, s61\n"
  "v_perm_b32 v105, v11, v10, s61\n"
  "v_perm_b32 v106, v9, v8, s65\n"
  "v_perm_b32 v107, v11, v10, s65\n"
  "v_perm_b32 v108, v13, v12, s61\n"
  "v_perm_b32 v109, v15, v14, s61\n"
  "v_perm_b32 v110, v13, v12, s65\n"
  "v_perm_b32 v111, v15, v14, s65\n"
  "ds_read_b64_tr_b16 a[128:129], v99 offset:0\n"
  "ds_read_b64_tr_b16 a[130:131], v99 offset:2048\n"
  "ds_read_b64_tr_b16 a[132:133], v99 offset:512\n"
  "ds_read_b64_tr_b16 a[134:135], v99 offset:2560\n"
  "ds_read_b64_tr_b16 a[192:193], v99 offset:8\n"
  "ds_read_b64_tr_b16 a[194:195], v99 offset:2056\n"
  "ds_read_b64_tr_b16 a[196:197], v99 offset:520\n"
  "ds_read_b64_tr_b16 a[198:199], v99 offset:2568\n"
  "ds_read_b64_tr_b16 a[136:137], v99 offset:4096\n"
  "ds_read_b64_tr_b16 a[138:139], v99 offset:6144\n"
  "ds_read_b64_tr_b16 a[140:141], v99 offset:4608\n"
  "ds_read_b64_tr_b16 a[142:143], v99 offset:6656\n"
  "ds_read_b64_tr_b16 a[200:201], v99 offset:4104\n"
  "ds_read_b64_tr_b16 a[202:203], v99 offset:6152\n"
  "ds_read_b64_tr_b16 a[204:205], v99 offset:4616\n"
  "ds_read_b64_tr_b16 a[206:207], v99 offset:6664\n"
  "s_waitcnt lgkmcnt(0)\n"
  "s_add_u32 m0, s51, 0\n"
  "s_xor_b32 s61, s50, 0\n"
  "s_lshl_b32 s61, s61, 20\n"
  "s_add_u32 s70, s44, s61\n"
  "s_addc_u32 s71, s45, 0\n"
  "global_load_lds_dwordx4 v96, s[70:71]\n"
  "s_add_u32 s70, s70, 131072\n"
  "s_addc_u32 s71, s71, 0\n"
  "s_add_u32 m0, m0, 1024\n"
  "s_nop 0\n"
  "global_load_lds_dwordx4 v96, s[70:71]\n"
  "s_add_u32 s70, s70, 131072\n"
  "s_addc_u32 s71, s71, 0\n"
  "s_add_u32 m0, m0, 1024\n"
  "s_nop 0\n"
  "global_load_lds_dwordx4 v96, s[70:71]\n"
  "s_add_u32 s70, s70, 131072\n"
  "s_addc_u32 s71, s71, 0\n"
  "s_add_u32 m0, m0, 1024\n"
  "s_nop 0\n"
  "global_load_lds_dwordx4 v96, s[70:71]\n"
  "s_add_u32 s70, s70, 131072\n"
  "s_addc_u32 s71, s71, 0\n"
  "s_add_u32 m0, m0, 1024\n"
  "s_nop 0\n"
  "global_load_lds_dwordx4 v96, s[70:71]\n"
  "s_add_u32 s70, s70, 131072\n"
  "s_addc_u32 s71, s71, 0\n"
  "s_add_u32 m0, m0, 1024\n"
  "s_nop 0\n"
  "global_load_lds_dwordx4 v96, s[70:71]\n"
  "s_add_u32 s70, s70, 131072\n"
  "s_addc_u32 s71, s71, 0\n"
  "s_add_u32 m0, m0, 1024\n"
  "s_nop 0\n"
  "global_load_lds_dwordx4 v96, s[70:71]\n"
  "s_add_u32 s70, s70, 131072\n"
  "s_addc_u32 s71, s71, 0\n"
  "s_add_u32 m0, m0, 1024\n"
  "s_nop 0\n"
  "global_load_lds_dwordx4 v96, s[70:71]\n"
  "s_waitcnt vmcnt(24)\n"
  "ds_read_b64_tr_b16 a[144:145], v99 offset:8192\n"
  "ds_read_b64_tr_b16 a[146:147], v99 offset:10240\n"
  "ds_read_b64_tr_b16 a[148:149], v99 offset:8704\n"
  "ds_read_b64_tr_b16 a[150:151], v99 offset:10752\n"
  "ds_read_b64_tr_b16 a[208:209], v99 offset:8200\n"
  "ds_read_b64_tr_b16 a[210:211], v99 offset:10248\n"
  "ds_read_b64_tr_b16 a[212:213], v99 offset:8712\n"
  "ds_read_b64_tr_b16 a[214:215], v99 offset:10760\n"
  "ds_read_b64_tr_b16 a[152:153], v99 offset:12288\n"
  "ds_read_b64_tr_b16 a[154:155], v99 offset:14336\n"
  "ds_read_b64_tr_b16 a[156:157], v99 offset:12800\n"
  "ds_read_b64_tr_b16 a[158:159], v99 offset:14848\n"
  "ds_read_b64_tr_b16 a[216:217], v99 offset:12296\n"
  "ds_read_b64_tr_b16 a[218:219], v99 offset:14344\n"
  "ds_read_b64_tr_b16 a[220:221], v99 offset:12808\n"
  "ds_read_b64_tr_b16 a[222:223], v99 offset:14856\n"
  "s_waitcnt lgkmcnt(0)\n"
  "s_add_u32 m0, s51, 8192\n"
  "s_xor_b32 s61, s50, 1\n"
  "s_lshl_b32 s61, s61, 20\n"
  "s_add_u32 s70, s44, s61\n"
  "s_addc_u32 s71, s45, 0\n"
  "global_load_lds_dwordx4 v96, s[70:71]\n"
  "s_add_u32 s70, s70, 131072\n"
  "s_addc_u32 s71, s71, 0\n"
  "s_add_u32 m0, m0, 1024\n"
  "s_nop 0\n"
  "global_load_lds_dwordx4 v96, s[70:71]\n"
  "s_add_u32 s70, s70, 131072\n"
  "s_addc_u32 s71, s71, 0\n"
  "s_add_u32 m0, m0, 1024\n"
  "s_nop 0\n"
  "global_load_lds_dwordx4 v96, s[70:71]\n"
  "s_add_u32 s70, s70, 131072\n"
  "s_addc_u32 s71, s71, 0\n"
  "s_add_u32 m0, m0, 1024\n"
  "s_nop 0\n"
  "global_load_lds_dwordx4 v96, s[70:71]\n"
  "s_add_u32 s70, s70, 131072\n"
  "s_addc_u32 s71, s71, 0\n"
  "s_add_u32 m0, m0, 1024\n"
  "s_nop 0\n"
  "global_load_lds_dwordx4 v96, s[70:71]\n"
  "s_add_u32 s70, s70, 131072\n"
  "s_addc_u32 s71, s71, 0\n"
  "s_add_u32 m0, m0, 1024\n"
  "s_nop 0\n"
  "global_load_lds_dwordx4 v96, s[70:71]\n"
  "s_add_u32 s70, s70, 131072\n"
  "s_addc_u32 s71, s71, 0\n"
  "s_add_u32 m0, m0, 1024\n"
  "s_nop 0\n"
  "global_load_lds_dwordx4 v96, s[70:71]\n"
  "s_add_u32 s70, s70, 131072\n"
  "s_addc_u32 s71, s71, 0\n"
  "s_add_u32 m0, m0, 1024\n"
  "s_nop 0\n"
  "global_load_lds_dwordx4 v96, s[70:71]\n"
  "s_waitcnt vmcnt(24)\n"
  "ds_read_b64_tr_b16 a[160:161], v99 offset:16384\n"
  "ds_read_b64_tr_b16 a[162:163], v99 offset:18432\n"
  "ds_read_b64_tr_b16 a[164:165], v99 offset:16896\n"
  "ds_read_b64_tr_b16 a[166:167], v99 offset:18944\n"
  "ds_read_b64_tr_b16 a[224:225], v99 offset:16392\n"
  "ds_read_b64_tr_b16 a[226:227], v99 offset:18440\n"
  "ds_read_b64_tr_b16 a[228:229], v99 offset:16904\n"
  "ds_read_b64_tr_b16 a[230:231], v99 offset:18952\n"
  "ds_read_b64_tr_b16 a[168:169], v99 offset:20480\n"
  "ds_read_b64_tr_b16 a[170:171], v99 offset:22528\n"
  "ds_read_b64_tr_b16 a[172:173], v99 offset:20992\n"
  "ds_read_b64_tr_b16 a[174:175], v99 offset:23040\n"
  "ds_read_b64_tr_b16 a[232:233], v99 offset:20488\n"
  "ds_read_b64_tr_b16 a[234:235], v99 offset:22536\n"
  "ds_read_b64_tr_b16 a[236:237], v99 offset:21000\n"
  "ds_read_b64_tr_b16 a[238:239], v99 offset:23048\n"
  "s_waitcnt lgkmcnt(0)\n"
  "s_add_u32 m0, s51, 16384\n"
  "s_xor_b32 s61, s50, 2\n"
  "s_lshl_b32 s61, s61, 20\n"
  "s_add_u32 s70, s44, s61\n"
  "s_addc_u32 s71, s45, 0\n"
  "global_load_lds_dwordx4 v96, s[70:71]\n"
  "s_add_u32 s70, s70, 131072\n"
  "s_addc_u32 s71, s71, 0\n"
  "s_add_u32 m0, m0, 1024\n"
  "s_nop 0\n"
  "global_load_lds_dwordx4 v96, s[70:71]\n"
  "s_add_u32 s70, s70, 131072\n"
  "s_addc_u32 s71, s71, 0\n"
  "s_add_u32 m0, m0, 1024\n"
  "s_nop 0\n"
  "global_load_lds_dwordx4 v96, s[70:71]\n"
  "s_add_u32 s70, s70, 131072\n"
  "s_addc_u32 s71, s71, 0\n"
  "s_add_u32 m0, m0, 1024\n"
  "s_nop 0\n"
  "global_load_lds_dwordx4 v96, s[70:71]\n"
  "s_add_u32 s70, s70, 131072\n"
  "s_addc_u32 s71, s71, 0\n"
  "s_add_u32 m0, m0, 1024\n"
  "s_nop 0\n"
  "global_load_lds_dwordx4 v96, s[70:71]\n"
  "s_add_u32 s70, s70, 131072\n"
  "s_addc_u32 s71, s71, 0\n"
  "s_add_u32 m0, m0, 1024\n"
  "s_nop 0\n"
  "global_load_lds_dwordx4 v96, s[70:71]\n"
  "s_add_u32 s70, s70, 131072\n"
  "s_addc_u32 s71, s71, 0\n"
  "s_add_u32 m0, m0, 1024\n"
  "s_nop 0\n"
  "global_load_lds_dwordx4 v96, s[70:71]\n"
  "s_add_u32 s70, s70, 131072\n"
  "s_addc_u32 s71, s71, 0\n"
  "s_add_u32 m0, m0, 1024\n"
  "s_nop 0\n"
  "global_load_lds_dwordx4 v96, s[70:71]\n"
  "s_waitcnt vmcnt(24)\n"
  "ds_read_b64_tr_b16 a[176:177], v99 offset:24576\n"
  "ds_read_b64_tr_b16 a[178:179], v99 offset:26624\n"
  "ds_read_b64_tr_b16 a[180:181], v99 offset:25088\n"
  "ds_read_b64_tr_b16 a[182:183], v99 offset:27136\n"
  "ds_read_b64_tr_b16 a[240:241], v99 offset:24584\n"
  "ds_read_b64_tr_b16 a[242:243], v99 offset:26632\n"
  "ds_read_b64_tr_b16 a[244:245], v99 offset:25096\n"
  "ds_read_b64_tr_b16 a[246:247], v99 offset:27144\n"
  "ds_read_b64_tr_b16 a[184:185], v99 offset:28672\n"
  "ds_read_b64_tr_b16 a[186:187], v99 offset:30720\n"
  "ds_read_b64_tr_b16 a[188:189], v99 offset:29184\n"
  "ds_read_b64_tr_b16 a[190:191], v99 offset:31232\n"
  "ds_read_b64_tr_b16 a[248:249], v99 offset:28680\n"
  "ds_read_b64_tr_b16 a[250:251], v99 offset:30728\n"
  "ds_read_b64_tr_b16 a[252:253], v99 offset:29192\n"
  "ds_read_b64_tr_b16 a[254:255], v99 offset:31240\n"
  "s_waitcnt lgkmcnt(0)\n"
  "buffer_load_dwordx2 v[18:19], off, s[36:39], s92 sc1\n"
  "s_add_u32 m0, s51, 24576\n"
  "s_xor_b32 s61, s50, 3\n"
  "s_lshl_b32 s61, s61, 20\n"
  "s_add_u32 s70, s44, s61\n"
  "s_addc_u32 s71, s45, 0\n"
  "global_load_lds_dwordx4 v96, s[70:71]\n"
  "s_add_u32 s70, s70, 131072\n"
  "s_addc_u32 s71, s71, 0\n"
  "s_add_u32 m0, m0, 1024\n"
  "s_nop 0\n"
  "global_load_lds_dwordx4 v96, s[70:71]\n"
  "s_add_u32 s70, s70, 131072\n"
  "s_addc_u32 s71, s71, 0\n"
  "s_add_u32 m0, m0, 1024\n"
  "s_nop 0\n"
  "global_load_lds_dwordx4 v96, s[70:71]\n"
  "s_add_u32 s70, s70, 131072\n"
  "s_addc_u32 s71, s71, 0\n"
  "s_add_u32 m0, m0, 1024\n"
  "s_nop 0\n"
  "global_load_lds_dwordx4 v96, s[70:71]\n"
  "s_add_u32 s70, s70, 131072\n"
  "s_addc_u32 s71, s71, 0\n"
  "s_add_u32 m0, m0, 1024\n"
  "s_nop 0\n"
  "global_load_lds_dwordx4 v96, s[70:71]\n"
  "s_add_u32 s70, s70, 131072\n"
  "s_addc_u32 s71, s71, 0\n"
  "s_add_u32 m0, m0, 1024\n"
  "s_nop 0\n"
  "global_load_lds_dwordx4 v96, s[70:71]\n"
  "s_add_u32 s70, s70, 131072\n"
  "s_addc_u32 s71, s71, 0\n"
  "s_add_u32 m0, m0, 1024\n"
  "s_nop 0\n"
  "global_load_lds_dwordx4 v96, s[70:71]\n"
  "s_add_u32 s70, s70, 131072\n"
  "s_addc_u32 s71, s71, 0\n"
  "s_add_u32 m0, m0, 1024\n"
  "s_nop 0\n"
  "global_load_lds_dwordx4 v96, s[70:71]\n"
  "s_waitcnt vmcnt(25)\n"
  "ds_read_b64_tr_b16 v[128:129], v98 offset:0\n"
  "ds_read_b64_tr_b16 v[130:131], v98 offset:2048\n"
  "ds_read_b64_tr_b16 v[132:133], v98 offset:512\n"
  "ds_read_b64_tr_b16 v[134:135], v98 offset:2560\n"
  "ds_read_b64_tr_b16 v[32:33], v98 offset:32\n"
  "ds_read_b64_tr_b16 v[34:35], v98 offset:2080\n"
  "ds_read_b64_tr_b16 v[36:37], v98 offset:544\n"
  "ds_read_b64_tr_b16 v[38:39], v98 offset:2592\n"
  "ds_read_b64_tr_b16 v[136:137], v98 offset:4096\n"
  "ds_read_b64_tr_b16 v[138:139], v98 offset:6144\n"
  "ds_read_b64_tr_b16 v[140:141], v98 offset:4608\n"
  "ds_read_b64_tr_b16 v[142:143], v98 offset:6656\n"
  "ds_read_b64_tr_b16 v[40:41], v98 offset:4128\n"
  "ds_read_b64_tr_b16 v[42:43], v98 offset:6176\n"
  "ds_read_b64_tr_b16 v[44:45], v98 offset:4640\n"
  "ds_read_b64_tr_b16 v[46:47], v98 offset:6688\n"
  "s_waitcnt lgkmcnt(0)\n"
  "s_add_u32 m0, s51, 0\n"
  "s_lshr_b32 s61, s50, 1\n"
  "s_xor_b32 s61, s61, 0\n"
  "s_lshl_b32 s61, s61, 16\n"
  "s_add_u32 s70, s46, s61\n"
  "s_addc_u32 s71, s47, 0\n"
  "global_load_lds_dwordx4 v97, s[70:71]\n"
  "s_add_u32 s70, s70, 8192\n"
  "s_addc_u32 s71, s71, 0\n"
  "s_add_u32 m0, m0, 1024\n"
  "s_nop 0\n"
  "global_load_lds_dwordx4 v97, s[70:71]\n"
  "s_add_u32 s70, s70, 8192\n"
  "s_addc_u32 s71, s71, 0\n"
  "s_add_u32 m0, m0, 1024\n"
  "s_nop 0\n"
  "global_load_lds_dwordx4 v97, s[70:71]\n"
  "s_add_u32 s70, s70, 8192\n"
  "s_addc_u32 s71, s71, 0\n"
  "s_add_u32 m0, m0, 1024\n"
  "s_nop 0\n"
  "global_load_lds_dwordx4 v97, s[70:71]\n"
  "s_add_u32 s70, s70, 8192\n"
  "s_addc_u32 s71, s71, 0\n"
  "s_add_u32 m0, m0, 1024\n"
  "s_nop 0\n"
  "global_load_lds_dwordx4 v97, s[70:71]\n"
  "s_add_u32 s70, s70, 8192\n"
  "s_addc_u32 s71, s71, 0\n"
  "s_add_u32 m0, m0, 1024\n"
  "s_nop 0\n"
  "global_load_lds_dwordx4 v97, s[70:71]\n"
  "s_add_u32 s70, s70, 8192\n"
  "s_addc_u32 s71, s71, 0\n"
  "s_add_u32 m0, m0, 1024\n"
  "s_nop 0\n"
  "global_load_lds_dwordx4 v97, s[70:71]\n"
  "s_add_u32 s70, s70, 8192\n"
  "s_addc_u32 s71, s71, 0\n"
  "s_add_u32 m0, m0, 1024\n"
  "s_nop 0\n"
  "global_load_lds_dwordx4 v97, s[70:71]\n"
  "s_waitcnt vmcnt(25)\n"
  "ds_read_b64_tr_b16 v[144:145], v98 offset:8192\n"
  "ds_read_b64_tr_b16 v[146:147], v98 offset:10240\n"
  "ds_read_b64_tr_b16 v[148:149], v98 offset:8704\n"
  "ds_read_b64_tr_b16 v[150:151], v98 offset:10752\n"
  "ds_read_b64_tr_b16 v[48:49], v98 offset:8224\n"
  "ds_read_b64_tr_b16 v[50:51], v98 offset:10272\n"
  "ds_read_b64_tr_b16 v[52:53], v98 offset:8736\n"
  "ds_read_b64_tr_b16 v[54:55], v98 offset:10784\n"
  "ds_read_b64_tr_b16 v[152:153], v98 offset:12288\n"
  "ds_read_b64_tr_b16 v[154:155], v98 offset:14336\n"
  "ds_read_b64_tr_b16 v[156:157], v98 offset:12800\n"
  "ds_read_b64_tr_b16 v[158:159], v98 offset:14848\n"
  "ds_read_b64_tr_b16 v[56:57], v98 offset:12320\n"
  "ds_read_b64_tr_b16 v[58:59], v98 offset:14368\n"
  "ds_read_b64_tr_b16 v[60:61], v98 offset:12832\n"
  "ds_read_b64_tr_b16 v[62:63], v98 offset:14880\n"
  "s_waitcnt lgkmcnt(0)\n"
  "s_add_u32 m0, s51, 8192\n"
  "s_lshr_b32 s61, s50, 1\n"
  "s_xor_b32 s61, s61, 1\n"
  "s_lshl_b32 s61, s61, 16\n"
  "s_add_u32 s70, s46, s61\n"
  "s_addc_u32 s71, s47, 0\n"
  "global_load_lds_dwordx4 v97, s[70:71]\n"
  "s_add_u32 s70, s70, 8192\n"
  "s_addc_u32 s71, s71, 0\n"
  "s_add_u32 m0, m0, 1024\n"
  "s_nop 0\n"
  "global_load_lds_dwordx4 v97, s[70:71]\n"
  "s_add_u32 s70, s70, 8192\n"
  "s_addc_u32 s71, s71, 0\n"
  "s_add_u32 m0, m0, 1024\n"
  "s_nop 0\n"
  "global_load_lds_dwordx4 v97, s[70:71]\n"
  "s_add_u32 s70, s70, 8192\n"
  "s_addc_u32 s71, s71, 0\n"
  "s_add_u32 m0, m0, 1024\n"
  "s_nop 0\n"
  "global_load_lds_dwordx4 v97, s[70:71]\n"
  "s_add_u32 s70, s70, 8192\n"
  "s_addc_u32 s71, s71, 0\n"
  "s_add_u32 m0, m0, 1024\n"
  "s_nop 0\n"
  "global_load_lds_dwordx4 v97, s[70:71]\n"
  "s_add_u32 s70, s70, 8192\n"
  "s_addc_u32 s71, s71, 0\n"
  "s_add_u32 m0, m0, 1024\n"
  "s_nop 0\n"
  "global_load_lds_dwordx4 v97, s[70:71]\n"
  "s_add_u32 s70, s70, 8192\n"
  "s_addc_u32 s71, s71, 0\n"
  "s_add_u32 m0, m0, 1024\n"
  "s_nop 0\n"
  "global_load_lds_dwordx4 v97, s[70:71]\n"
  "s_add_u32 s70, s70, 8192\n"
  "s_addc_u32 s71, s71, 0\n"
  "s_add_u32 m0, m0, 1024\n"
  "s_nop 0\n"
  "global_load_lds_dwordx4 v97, s[70:71]\n"
  "s_waitcnt vmcnt(24)\n"
  ".Lch_h0:\n"
  "v_readfirstlane_b32 s61, v18\n"
  "v_readfirstlane_b32 s65, v19\n"
  "s_cmp_lg_u32 s61, s93\n"
  "s_cselect_b32 s65, s65, 0\n"
  "s_cmp_lg_u32 s65, 0\n"
  "s_cbranch_scc1 .Lch_h1\n"
  "s_sub_u32 s59, s59, 1\n"
  "s_cbranch_scc1 .Lch_end\n"
  "s_sleep 4\n"
  "buffer_load_dwordx2 v[18:19], off, s[36:39], s92 sc1\n"
  "s_waitcnt vmcnt(0)\n"
  "s_branch .Lch_h0\n"
  ".Lch_h1:\n"
  "s_lshl_b32 s65, s65, 13\n"
  "s_and_b32 s61, s65, 0x2000\n"
  "s_and_b32 s65, s65, 0x6000\n"
  "s_xor_b32 s52, s52, s61\n"
  "s_xor_b32 s56, s56, s61\n"
  "s_xor_b32 s68, s68, s61\n"
  "s_xor_b32 s90, s90, s61\n"
  "s_xor_b32 s55, s55, s65\n"
  "s_xor_b32 s54, s54, s65\n"
  "v_xor_b32 v119, s61, v119\n"
  "v_xor_b32 v118, s65, v118\n"
  "s_cmp_gt_u32 s50, 1\n"
  "s_cselect_b32 s61, s61, 0\n"
  "v_xor_b32 v123, s61, v123\n"
  "s_cselect_b32 s86, 0, 1\n"
  "s_cselect_b32 s97, 0, 0x80000000\n"
  "v_readfirstlane_b32 s61, v123\n"
  "s_sub_u32 s61, s91, s61\n"
  "s_cmp_gt_u32 s50, 1\n"
  "s_cselect_b32 s43, s61, 0x80000000\n"
  "ds_read_b64_tr_b16 v[160:161], v98 offset:16384\n"
  "ds_read_b64_tr_b16 v[162:163], v98 offset:18432\n"
  "ds_read_b64_tr_b16 v[164:165], v98 offset:16896\n"
  "ds_read_b64_tr_b16 v[166:167], v98 offset:18944\n"
  "ds_read_b64_tr_b16 v[64:65], v98 offset:16416\n"
  "ds_read_b64_tr_b16 v[66:67], v98 offset:18464\n"
  "ds_read_b64_tr_b16 v[68:69], v98 offset:16928\n"
  "ds_read_b64_tr_b16 v[70:71], v98 offset:18976\n"
  "ds_read_b64_tr_b16 v[168:169], v98 offset:20480\n"
  "ds_read_b64_tr_b16 v[170:171], v98 offset:22528\n"
  "ds_read_b64_tr_b16 v[172:173], v98 offset:20992\n"
  "ds_read_b64_tr_b16 v[174:175], v98 offset:23040\n"
  "ds_read_b64_tr_b16 v[72:73], v98 offset:20512\n"
  "ds_read_b64_tr_b16 v[74:75], v98 offset:22560\n"
  "ds_read_b64_tr_b16 v[76:77], v98 offset:21024\n"
  "ds_read_b64_tr_b16 v[78:79], v98 offset:23072\n"
  "s_waitcnt lgkmcnt(0)\n"
  "s_add_u32 m0, s51, 16384\n"
  "s_lshr_b32 s61, s50, 1\n"
  "s_xor_b32 s61, s61, 0\n"
  "s_lshl_b32 s61, s61, 16\n"
  "s_add_u32 s61, s61, 128\n"
  "s_add_u32 s70, s46, s61\n"
  "s_addc_u32 s71, s47, 0\n"
  "global_load_lds_dwordx4 v97, s[70:71]\n"
  "s_add_u32 s70, s70, 8192\n"
  "s_addc_u32 s71, s71, 0\n"
  "s_add_u32 m0, m0, 1024\n"
  "s_nop 0\n"
  "global_load_lds_dwordx4 v97, s[70:71]\n"
  "s_add_u32 s70, s70, 8192\n"
  "s_addc_u32 s71, s71, 0\n"
  "s_add_u32 m0, m0, 1024\n"
  "s_nop 0\n"
  "global_load_lds_dwordx4 v97, s[70:71]\n"
  "s_add_u32 s70, s70, 8192\n"
  "s_addc_u32 s71, s71, 0\n"
  "s_add_u32 m0, m0, 1024\n"
  "s_nop 0\n"
  "global_load_lds_dwordx4 v97, s[70:71]\n"
  "s_add_u32 s70, s70, 8192\n"
  "s_addc_u32 s71, s71, 0\n"
  "s_add_u32 m0, m0, 1024\n"
  "s_nop 0\n"
  "global_load_lds_dwordx4 v97, s[70:71]\n"
  "s_add_u32 s70, s70, 8192\n"
  "s_addc_u32 s71, s71, 0\n"
  "s_add_u32 m0, m0, 1024\n"
  "s_nop 0\n"
  "global_load_lds_dwordx4 v97, s[70:71]\n"
  "s_add_u32 s70, s70, 8192\n"
  "s_addc_u32 s71, s71, 0\n"
  "s_add_u32 m0, m0, 1024\n"
  "s_nop 0\n"
  "global_load_lds_dwordx4 v97, s[70:71]\n"
  "s_add_u32 s70, s70, 8192\n"
  "s_addc_u32 s71, s71, 0\n"
  "s_add_u32 m0, m0, 1024\n"
  "s_nop 0\n"
  "global_load_lds_dwordx4 v97, s[70:71]\n"
  "s_waitcnt vmcnt(24)\n"
  "ds_read_b64_tr_b16 v[176:177], v98 offset:24576\n"
  "ds_read_b64_tr_b16 v[178:179], v98 offset:26624\n"
  "ds_read_b64_tr_b16 v[180:181], v98 offset:25088\n"
  "ds_read_b64_tr_b16 v[182:183], v98 offset:27136\n"
  "ds_read_b64_tr_b16 v[80:81], v98 offset:24608\n"
  "ds_read_b64_tr_b16 v[82:83], v98 offset:26656\n"
  "ds_read_b64_tr_b16 v[84:85], v98 offset:25120\n"
  "ds_read_b64_tr_b16 v[86:87], v98 offset:27168\n"
  "ds_read_b64_tr_b16 v[184:185], v98 offset:28672\n"
  "ds_read_b64_tr_b16 v[186:187], v98 offset:30720\n"
  "ds_read_b64_tr_b16 v[188:189], v98 offset:29184\n"
  "ds_read_b64_tr_b16 v[190:191], v98 offset:31232\n"
  "ds_read_b64_tr_b16 v[88:89], v98 offset:28704\n"
  "ds_read_b64_tr_b16 v[90:91], v98 offset:30752\n"
  "ds_read_b64_tr_b16 v[92:93], v98 offset:29216\n"
  "ds_read_b64_tr_b16 v[94:95], v98 offset:31264\n"
  "s_waitcnt lgkmcnt(0)\n"
  "buffer_load_dword v120, v119, s[36:39], 0 offen sc1\n"
  "buffer_load_dword v96, v103, s[36:39], 0 offen sc1\n"
  "s_add_u32 m0, s51, 24576\n"
  "s_lshr_b32 s61, s50, 1\n"
  "s_xor_b32 s61, s61, 1\n"
  "s_lshl_b32 s61, s61, 16\n"
  "s_add_u32 s61, s61, 128\n"
  "s_add_u32 s70, s46, s61\n"
  "s_addc_u32 s71, s47, 0\n"
  "global_load_lds_dwordx4 v97, s[70:71]\n"
  "s_add_u32 s70, s70, 8192\n"
  "s_addc_u32 s71, s71, 0\n"
  "s_add_u32 m0, m0, 1024\n"
  "s_nop 0\n"
  "global_load_lds_dwordx4 v97, s[70:71]\n"
  "s_add_u32 s70, s70, 8192\n"
  "s_addc_u32 s71, s71, 0\n"
  "s_add_u32 m0, m0, 1024\n"
  "s_nop 0\n"
  "global_load_lds_dwordx4 v97, s[70:71]\n"
  "s_add_u32 s70, s70, 8192\n"
  "s_addc_u32 s71, s71, 0\n"
  "s_add_u32 m0, m0, 1024\n"
  "s_nop 0\n"
  "global_load_lds_dwordx4 v97, s[70:71]\n"
  "s_add_u32 s70, s70, 8192\n"
  "s_addc_u32 s71, s71, 0\n"
  "s_add_u32 m0, m0, 1024\n"
  "s_nop 0\n"
  "global_load_lds_dwordx4 v97, s[70:71]\n"
  "s_add_u32 s70, s70, 8192\n"
  "s_addc_u32 s71, s71, 0\n"
  "s_add_u32 m0, m0, 1024\n"
  "s_nop 0\n"
  "global_load_lds_dwordx4 v97, s[70:71]\n"
  "s_add_u32 s70, s70, 8192\n"
  "s_addc_u32 s71, s71, 0\n"
  "s_add_u32 m0, m0, 1024\n"
  "s_nop 0\n"
  "global_load_lds_dwordx4 v97, s[70:71]\n"
  "s_add_u32 s70, s70, 8192\n"
  "s_addc_u32 s71, s71, 0\n"
  "s_add_u32 m0, m0, 1024\n"
  "s_nop 0\n"
  "global_load_lds_dwordx4 v97, s[70:71]\n"
  "s_waitcnt vmcnt(26)\n"
  "ds_read_b64_tr_b16 v[192:193], v100 offset:0\n"
  "ds_read_b64_tr_b16 v[194:195], v100 offset:256\n"
  "ds_read_b64_tr_b16 v[196:197], v100 offset:2048\n"
  "ds_read_b64_tr_b16 v[198:199], v100 offset:2304\n"
  "ds_read_b64_tr_b16 v[200:201], v100 offset:64\n"
  "ds_read_b64_tr_b16 v[202:203], v100 offset:320\n"
  "ds_read_b64_tr_b16 v[204:205], v100 offset:2112\n"
  "ds_read_b64_tr_b16 v[206:207], v100 offset:2368\n"
  "ds_read_b64_tr_b16 v[208:209], v117 offset:0\n"
  "ds_read_b64_tr_b16 v[210:211], v117 offset:256\n"
  "ds_read_b64_tr_b16 v[212:213], v117 offset:2048\n"
  "ds_read_b64_tr_b16 v[214:215], v117 offset:2304\n"
  "ds_read_b64_tr_b16 v[216:217], v117 offset:64\n"
  "ds_read_b64_tr_b16 v[218:219], v117 offset:320\n"
  "ds_read_b64_tr_b16 v[220:221], v117 offset:2112\n"
  "ds_read_b64_tr_b16 v[222:223], v117 offset:2368\n"
  "s_waitcnt lgkmcnt(0)\n"
  "ds_write_b128 v114, v[32:35] offset:0\n"
  "ds_write_b128 v114, v[36:39] offset:1024\n"
  "ds_write_b128 v114, v[40:43] offset:2048\n"
  "ds_write_b128 v114, v[44:47] offset:3072\n"
  "ds_write_b128 v114, v[48:51] offset:4096\n"
  "ds_write_b128 v114, v[52:55] offset:5120\n"
  "ds_write_b128 v114, v[56:59] offset:6144\n"
  "ds_write_b128 v114, v[60:63] offset:7168\n"
  "s_waitcnt vmcnt(18)\n"
  "ds_read_b64_tr_b16 v[224:225], v100 offset:8192\n"
  "ds_read_b64_tr_b16 v[226:227], v100 offset:8448\n"
  "ds_read_b64_tr_b16 v[228:229], v100 offset:10240\n"
  "ds_read_b64_tr_b16 v[230:231], v100 offset:10496\n"
  "ds_read_b64_tr_b16 v[232:233], v100 offset:8256\n"
  "ds_read_b64_tr_b16 v[234:235], v100 offset:8512\n"
  "ds_read_b64_tr_b16 v[236:237], v100 offset:10304\n"
  "ds_read_b64_tr_b16 v[238:239], v100 offset:10560\n"
  "ds_read_b64_tr_b16 v[240:241], v117 offset:8192\n"
  "ds_read_b64_tr_b16 v[242:243], v117 offset:8448\n"
  "ds_read_b64_tr_b16 v[244:245], v117 offset:10240\n"
  "ds_read_b64_tr_b16 v[246:247], v117 offset:10496\n"
  "ds_read_b64_tr_b16 v[248:249], v117 offset:8256\n"
  "ds_read_b64_tr_b16 v[250:251], v117 offset:8512\n"
  "ds_read_b64_tr_b16 v[252:253], v117 offset:10304\n"
  "ds_read_b64_tr_b16 v[254:255], v117 offset:10560\n"
  "s_waitcnt lgkmcnt(0)\n"
  "ds_write_b128 v114, v[64:67] offset:8192\n"
  "ds_write_b128 v114, v[68:71] offset:9216\n"
  "ds_write_b128 v114, v[72:75] offset:10240\n"
  "ds_write_b128 v114, v[76:79] offset:11264\n"
  "ds_write_b128 v114, v[80:83] offset:12288\n"
  "ds_write_b128 v114, v[84:87] offset:13312\n"
  "ds_write_b128 v114, v[88:91] offset:14336\n"
  "ds_write_b128 v114, v[92:95] offset:15360\n"
  "s_waitcnt vmcnt(8)\n"
  "v_cmp_gt_u32 vcc, s86, v120\n"
  "s_cmp_lt_u32 s50, 2\n"
  "s_cbranch_scc0 .Lch_p5\n"
  "s_cmp_eq_u64 vcc, 0\n"
  "s_cbranch_scc0 .Lch_p5\n"
  "s_lshl_b32 s65, s50, 11\n"
  "s_add_u32 m0, s65, 8192\n"
  "s_add_u32 s61, s52, 0\n"
  "buffer_load_dwordx4 v121, s[36:39], s61 offen offset:0 sc1 lds\n"
  "buffer_load_dwordx4 v121, s[36:39], s61 offen offset:1024 sc1 lds\n"
  "s_add_u32 m0, s65, 12288\n"
  "s_add_u32 s61, s52, 4096\n"
  "buffer_load_dwordx4 v121, s[36:39], s61 offen offset:0 sc1 lds\n"
  "buffer_load_dwordx4 v121, s[36:39], s61 offen offset:1024 sc1 lds\n"
  "s_add_u32 m0, s65, 16384\n"
  "s_add_u32 s61, s52, 16384\n"
  "buffer_load_dwordx4 v121, s[36:39], s61 offen offset:0 sc1 lds\n"
  "buffer_load_dwordx4 v121, s[36:39], s61 offen offset:1024 sc1 lds\n"
  "s_add_u32 m0, s65, 20480\n"
  "s_add_u32 s61, s52, 20480\n"
  "buffer_load_dwordx4 v121, s[36:39], s61 offen offset:0 sc1 lds\n"
  "buffer_load_dwordx4 v121, s[36:39], s61 offen offset:1024 sc1 lds\n"
  "s_cmp_eq_u32 s86, 1\n"
  ".Lch_p5:\n"
  "ds_read_b64_tr_b16 v[0:1], v100 offset:16384\n"
  "ds_read_b64_tr_b16 v[2:3], v100 offset:16640\n"
  "ds_read_b64_tr_b16 v[4:5], v100 offset:18432\n"
  "ds_read_b64_tr_b16 v[6:7], v100 offset:18688\n"
  "ds_read_b64_tr_b16 v[8:9], v100 offset:16448\n"
  "ds_read_b64_tr_b16 v[10:11], v100 offset:16704\n"
  "ds_read_b64_tr_b16 v[12:13], v100 offset:18496\n"
  "ds_read_b64_tr_b16 v[14:15], v100 offset:18752\n"
  "ds_read_b64_tr_b16 v[16:17], v117 offset:16384\n"
  "ds_read_b64_tr_b16 v[18:19], v117 offset:16640\n"
  "ds_read_b64_tr_b16 v[20:21], v117 offset:18432\n"
  "ds_read_b64_tr_b16 v[22:23], v117 offset:18688\n"
  "ds_read_b64_tr_b16 v[24:25], v117 offset:16448\n"
  "ds_read_b64_tr_b16 v[26:27], v117 offset:16704\n"
  "ds_read_b64_tr_b16 v[28:29], v117 offset:18496\n"
  "ds_read_b64_tr_b16 v[30:31], v117 offset:18752\n"
  "s_waitcnt lgkmcnt(0)\n"
  "ds_write_b128 v114, v[0:3] offset:16384\n"
  "ds_write_b128 v114, v[4:7] offset:17408\n"
  "ds_write_b128 v114, v[8:11] offset:18432\n"
  "ds_write_b128 v114, v[12:15] offset:19456\n"
  "ds_write_b128 v114, v[16:19] offset:20480\n"
  "ds_write_b128 v114, v[20:23] offset:21504\n"
  "ds_write_b128 v114, v[24:27] offset:22528\n"
  "ds_write_b128 v114, v[28:31] offset:23552\n"
  "s_waitcnt lgkmcnt(0)\n"
  "s_cbranch_scc1 .Lch_p7\n"
  "s_waitcnt vmcnt(0)\n"
  ".Lch_p7:\n"
  "s_waitcnt vmcnt(8)\n"
  "ds_read_b64_tr_b16 v[0:1], v100 offset:24576\n"
  "ds_read_b64_tr_b16 v[2:3], v100 offset:24832\n"
  "ds_read_b64_tr_b16 v[4:5], v100 offset:26624\n"
  "ds_read_b64_tr_b16 v[6:7], v100 offset:26880\n"
  "ds_read_b64_tr_b16 v[8:9], v100 offset:24640\n"
  "ds_read_b64_tr_b16 v[10:11], v100 offset:24896\n"
  "ds_read_b64_tr_b16 v[12:13], v100 offset:26688\n"
  "ds_read_b64_tr_b16 v[14:15], v100 offset:26944\n"
  "ds_read_b64_tr_b16 v[16:17], v117 offset:24576\n"
  "ds_read_b64_tr_b16 v[18:19], v117 offset:24832\n"
  "ds_read_b64_tr_b16 v[20:21], v117 offset:26624\n"
  "ds_read_b64_tr_b16 v[22:23], v117 offset:26880\n"
  "ds_read_b64_tr_b16 v[24:25], v117 offset:24640\n"
  "ds_read_b64_tr_b16 v[26:27], v117 offset:24896\n"
  "ds_read_b64_tr_b16 v[28:29], v117 offset:26688\n"
  "ds_read_b64_tr_b16 v[30:31], v117 offset:26944\n"
  "s_waitcnt lgkmcnt(0)\n"
  "ds_write_b128 v114, v[0:3] offset:24576\n"
  "ds_write_b128 v114, v[4:7] offset:25600\n"
  "ds_write_b128 v114, v[8:11] offset:26624\n"
  "ds_write_b128 v114, v[12:15] offset:27648\n"
  "ds_write_b128 v114, v[16:19] offset:28672\n"
  "ds_write_b128 v114, v[20:23] offset:29696\n"
  "ds_write_b128 v114, v[24:27] offset:30720\n"
  "ds_write_b128 v114, v[28:31] offset:31744\n"
  "s_waitcnt lgkmcnt(0)\n"
  "s_cbranch_vccz .Lch_p1\n"
  ".Lch_p0:\n"
  "buffer_load_dword v120, v119, s[36:39], 0 offen sc1\n"
  "s_waitcnt vmcnt(0)\n"
  "v_cmp_gt_u32 vcc, 1, v120\n"
  "s_cbranch_vccz .Lch_p0_ok\n"
  "s_sub_u32 s59, s59, 1\n"
  "s_cbranch_scc1 .Lch_end\n"
  "s_sleep 2\n"
  "s_branch .Lch_p0\n"
  ".Lch_p0_ok:\n"
  "s_lshl_b32 s65, s50, 11\n"
  "s_add_u32 m0, s65, 8192\n"
  "s_add_u32 s61, s52, 0\n"
  "buffer_load_dwordx4 v121, s[36:39], s61 offen offset:0 sc1 lds\n"
  "buffer_load_dwordx4 v121, s[36:39], s61 offen offset:1024 sc1 lds\n"
  "s_add_u32 m0, s65, 12288\n"
  "s_add_u32 s61, s52, 4096\n"
  "buffer_load_dwordx4 v121, s[36:39], s61 offen offset:0 sc1 lds\n"
  "buffer_load_dwordx4 v121, s[36:39], s61 offen offset:1024 sc1 lds\n"
  "s_add_u32 m0, s65, 16384\n"
  "s_add_u32 s61, s52, 16384\n"
  "buffer_load_dwordx4 v121, s[36:39], s61 offen offset:0 sc1 lds\n"
  "buffer_load_dwordx4 v121, s[36:39], s61 offen offset:1024 sc1 lds\n"
  "s_add_u32 m0, s65, 20480\n"
  "s_add_u32 s61, s52, 20480\n"
  "buffer_load_dwordx4 v121, s[36:39], s61 offen offset:0 sc1 lds\n"
  "buffer_load_dwordx4 v121, s[36:39], s61 offen offset:1024 sc1 lds\n"
  ".Lch_p1:\n"
  "s_mov_b32 s86, 0\n"
  "s_mov_b32 s80, 0\n"
  "s_mov_b32 s81, 0\n"
  "s_mov_b32 s4, 858993459\n"
  "s_mov_b32 s5, 858993459\n"
  "s_mov_b32 s26, 3435973836\n"
  "s_mov_b32 s27, 3435973836\n"
  "s_mov_b32 s6, 512\n"
  "s_mov_b32 s7, 1024\n"
  "s_mov_b32 s24, 1536\n"
  "s_mov_b32 s25, 2147483648\n"
  "s_mov_b32 s40, 0x3f803f80\n"
  "s_mov_b32 s41, 0x3f803f80\n"
  "s_mov_b32 s42, 1317011456\n"
  "s_mov_b32 s30, s60\n"
  "s_mov_b32 s31, s60\n"
  "s_mov_b32 s44, s90\n"
  "s_mov_b32 s46, 148\n"
  "s_mov_b32 s92, 1000\n"
  "v_cmp_gt_u32 vcc, 1, v96\n"
  "s_cmp_gt_u32 s50, 1\n"
  "s_cbranch_scc1 .Lch_p6\n"
  "s_add_u32 s52, s52, 0x200000\n"
  "s_xor_b32 s52, s52, 8192\n"
  "s_bitset0_b32 s52, 26\n"
  ".Lch_p6:\n"
  "s_cbranch_vccz .Lch_p2_ok\n"
  ".Lch_p2:\n"
  "buffer_load_dword v96, v103, s[36:39], 0 offen sc1\n"
  "s_waitcnt vmcnt(0)\n"
  "v_cmp_gt_u32 vcc, 1, v96\n"
  "s_cbranch_vccz .Lch_p2_ok\n"
  "s_sub_u32 s59, s59, 1\n"
  "s_cbranch_scc1 .Lch_end\n"
  "s_sleep 2\n"
  "s_branch .Lch_p2\n"
  ".Lch_p2_ok:\n"
  "v_readfirstlane_b32 s84, v96\n"
  "v_mov_b64 v[0:1], v[104:105]\n"
  "v_mov_b64 v[2:3], v[106:107]\n"
  "v_mov_b64 v[4:5], v[108:109]\n"
  "v_mov_b64 v[6:7], v[110:111]\n"
  "v_mov_b64 v[88:89], 0\n"
  "v_mov_b64 v[90:91], 0\n"
  "v_mov_b64 v[92:93], 0\n"
  "v_mov_b64 v[94:95], 0\n"
  "v_mov_b64 v[96:97], 0\n"
  "v_mov_b64 v[98:99], 0\n"
  "v_mov_b64 v[100:101], 0\n"
  "v_mov_b64 v[102:103], 0\n"
  "v_mov_b64 v[104:105], 0\n"
  "v_mov_b64 v[106:107], 0\n"
  "v_mov_b64 v[108:109], 0\n"
  "v_mov_b64 v[110:111], 0\n"
  "v_xor_b32 v12, 2048, v121\n"
  "v_xor_b32 v20, 4096, v121\n"
  "v_xor_b32 v28, 6144, v121\n"
  "s_waitcnt vmcnt(0)\n"
  "s_waitcnt lgkmcnt(0)\n"
  "s_barrier\n"
  "s_memrealtime s[82:83]\n"
  "s_waitcnt lgkmcnt(0)\n"
  "s_sub_u32 s85, s82, s84\n"
  "s_or_b32 s91, s85, 1\n"
  ".Lch_loop:\n"
  "s_waitcnt vmcnt(1)\n"
  "ds_write_b128 v121, v[0:3] offset:0\n"
  "v_smfmac_f32_16x16x64_bf16 v[100:103], v[0:3], v[32:39], v112\n"
  "s_waitcnt vmcnt(0)\n"
  "ds_write_b128 v121, v[4:7] offset:1024\n"
  "v_smfmac_f32_16x16x64_bf16 v[96:99], v[0:3], v[128:135], v112\n"
  "s_and_b32 s87, s58, 1\n"
  "s_add_u32 s65, s53, 131072\n"
  "v_smfmac_f32_16x16x64_bf16 v[104:107], v[0:3], a[0:7], v112\n"
  "buffer_load_dword v120, v119, s[36:39], 0 offen sc1\n"
  "v_mov_b32 v117, s84\n"
  "v_smfmac_f32_16x16x64_bf16 v[108:111], v[0:3], a[64:71], v112\n"
  "ds_max_u32 v113, v117 offset:30912\n"
  "v_smfmac_f32_16x16x64_bf16 v[88:91], v[0:3], a[128:135], v112\n"
  "s_or_b32 s47, s87, 48\n"
  "v_max3_u32 v32, v0, v2, v4\n"
  "v_smfmac_f32_16x16x64_bf16 v[92:95], v[0:3], a[192:199], v112\n"
  "v_max_u32 v32, v32, v6\n"
  "v_mov_b32 v1, s91\n"
  "v_smfmac_f32_16x16x64_bf16 v[100:103], v[4:7], v[40:47], v112\n"
  "v_cmp_eq_u32 vcc, -1, v32\n"
  "buffer_store_dword v1, v123, s[36:39], s43 offen sc1\n"
  "v_smfmac_f32_16x16x64_bf16 v[96:99], v[4:7], v[136:143], v112\n"
  "s_cbranch_vccnz .Lch_retry\n"
  "s_waitcnt lgkmcnt(0)\n"
  "s_barrier\n"
  "ds_read_b128 v[8:11], v12 offset:0\n"
  "ds_read_b128 v[12:15], v12 offset:1024\n"
  "v_smfmac_f32_16x16x64_bf16 v[104:107], v[4:7], a[8:15], v112\n"
  "ds_read_b128 v[16:19], v20 offset:0\n"
  "v_smfmac_f32_16x16x64_bf16 v[108:111], v[4:7], a[72:79], v112\n"
  "ds_read_b128 v[20:23], v20 offset:1024\n"
  "v_smfmac_f32_16x16x64_bf16 v[88:91], v[4:7], a[136:143], v112\n"
  "ds_read_b128 v[24:27], v28 offset:0\n"
  "v_smfmac_f32_16x16x64_bf16 v[92:95], v[4:7], a[200:207], v112\n"
  "ds_read_b128 v[28:31], v28 offset:1024\n"
  "s_waitcnt lgkmcnt(5)\n"
  "v_smfmac_f32_16x16x64_bf16 v[100:103], v[8:11], v[48:55], v112\n"
  "s_waitcnt lgkmcnt(4)\n"
  "v_smfmac_f32_16x16x64_bf16 v[100:103], v[12:15], v[56:63], v112\n"
  "ds_read_b128 v[32:35], v114 offset:14336\n"
  "v_smfmac_f32_16x16x64_bf16 v[96:99], v[8:11], v[144:151], v112\n"
  "ds_read_b128 v[36:39], v114 offset:15360\n"
  "s_waitcnt lgkmcnt(5)\n"
  "v_smfmac_f32_16x16x64_bf16 v[100:103], v[16:19], v[64:71], v112\n"
  "ds_read_b128 v[40:43], v113 offset:10240\n"
  "s_waitcnt lgkmcnt(5)\n"
  "v_smfmac_f32_16x16x64_bf16 v[100:103], v[20:23], v[72:79], v112\n"
  "ds_read_b128 v[44:47], v113 offset:11264\n"
  "s_waitcnt lgkmcnt(5)\n"
  "v_smfmac_f32_16x16x64_bf16 v[100:103], v[24:27], v[80:87], v112\n"
  "ds_read_b128 v[48:51], v113 offset:12288\n"
  "s_waitcnt lgkmcnt(3)\n"
  "v_smfmac_f32_16x16x64_bf16 v[100:103], v[28:31], v[32:39], v112\n"
  "ds_read_b128 v[52:55], v113 offset:13312\n"
  "s_nop 0\n"
  "v_smfmac_f32_16x16x64_bf16 v[96:99], v[12:15], v[152:159], v112\n"
  "ds_read_b128 v[0:3], v114 offset:16384\n"
  "s_nop 0\n"
  "v_smfmac_f32_16x16x64_bf16 v[96:99], v[16:19], v[160:167], v112\n"
  "ds_read_b128 v[4:7], v114 offset:17408\n"
  "s_nop 0\n"
  "v_add_f32 v100, v100, v102\n"
  "v_smfmac_f32_16x16x64_bf16 v[96:99], v[20:23], v[168:175], v112\n"
  "ds_read_b128 v[32:35], v113 offset:8192\n"
  "v_add_f32 v101, v101, v103\n"
  "v_smfmac_f32_16x16x64_bf16 v[96:99], v[24:27], v[176:183], v112\n"
  "ds_read_b128 v[36:39], v113 offset:9216\n"
  "v_mul_f32 v100, s60, v100\n"
  "v_smfmac_f32_16x16x64_bf16 v[96:99], v[28:31], v[184:191], v112\n"
  "s_nop 0\n"
  "v_mul_f32 v101, s60, v101\n"
  "v_smfmac_f32_16x16x64_bf16 v[104:107], v[8:11], a[16:23], v112\n"
  "ds_read_b32 v117, v113 offset:30912\n"
  "v_cvt_pk_bf16_f32 v100, v100, v101\n"
  "v_smfmac_f32_16x16x64_bf16 v[104:107], v[12:15], a[24:31], v112\n"
  "s_nop 1\n"
  "v_add_f32 v96, v96, v98\n"
  "v_smfmac_f32_16x16x64_bf16 v[104:107], v[16:19], a[32:39], v112\n"
  "ds_read_b128 v[56:59], v113 offset:14336\n"
  "v_add_f32 v97, v97, v99\n"
  "v_smfmac_f32_16x16x64_bf16 v[104:107], v[20:23], a[40:47], v112\n"
  "ds_read_b128 v[60:63], v113 offset:15360\n"
  "v_mul_f32 v96, s60, v96\n"
  "v_smfmac_f32_16x16x64_bf16 v[104:107], v[24:27], a[48:55], v112\n"
  "s_add_u32 s55, s55, 32768\n"
  "s_andn2_b32 s55, s55, 131072\n"
  "v_mul_f32 v97, s60, v97\n"
  "v_smfmac_f32_16x16x64_bf16 v[104:107], v[28:31], a[56:63], v112\n"
  "s_nop 0\n"
  "v_cvt_pk_bf16_f32 v96, v96, v97\n"
  "v_smfmac_f32_16x16x64_bf16 v[108:111], v[8:11], a[80:87], v112\n"
  "ds_write2_b32 v115, v96, v100 offset0:0 offset1:32\n"
  "s_nop 0\n"
  "v_smfmac_f32_16x16x64_bf16 v[108:111], v[12:15], a[88:95], v112\n"
  "s_nop 1\n"
  "v_add_f32 v104, v104, v106\n"
  "v_smfmac_f32_16x16x64_bf16 v[108:111], v[16:19], a[96:103], v112\n"
  "v_add_f32 v105, v105, v107\n"
  "v_smfmac_f32_16x16x64_bf16 v[108:111], v[20:23], a[104:111], v112\n"
  "v_cvt_pk_bf16_f32 v104, v104, v105\n"
  "v_smfmac_f32_16x16x64_bf16 v[108:111], v[24:27], a[112:119], v112\n"
  "s_waitcnt lgkmcnt(0)\n"
  "s_barrier\n"
  "v_smfmac_f32_16x16x64_bf16 v[108:111], v[28:31], a[120:127], v112\n"
  "ds_read_b64_tr_b16 v[64:65], v116 offset:0\n"
  "v_mov_b64 v[96:97], 0\n"
  "v_smfmac_f32_16x16x64_bf16 v[88:91], v[8:11], a[144:151], v112\n"
  "ds_read_b64_tr_b16 v[66:67], v116 offset:32\n"
  "v_mov_b64 v[98:99], 0\n"
  "v_smfmac_f32_16x16x64_bf16 v[88:91], v[12:15], a[152:159], v112\n"
  "ds_read_b64_tr_b16 v[68:69], v116 offset:256\n"
  "v_mov_b64 v[100:101], 0\n"
  "v_smfmac_f32_16x16x64_bf16 v[88:91], v[16:19], a[160:167], v112\n"
  "ds_read_b64_tr_b16 v[70:71], v116 offset:288\n"
  "v_add_f32 v108, v108, v110\n"
  "v_smfmac_f32_16x16x64_bf16 v[88:91], v[20:23], a[168:175], v112\n"
  "ds_read_b64_tr_b16 v[72:73], v116 offset:512\n"
  "v_add_f32 v109, v109, v111\n"
  "v_smfmac_f32_16x16x64_bf16 v[88:91], v[24:27], a[176:183], v112\n"
  "ds_read_b64_tr_b16 v[74:75], v116 offset:544\n"
  "v_cvt_pk_bf16_f32 v108, v108, v109\n"
  "s_waitcnt lgkmcnt(4)\n"
  "v_mfma_f32_16x16x32_bf16 v[80:83], v[32:35], v[64:67], 0\n"
  "ds_read_b64_tr_b16 v[76:77], v116 offset:768\n"
  "v_mov_b64 v[102:103], 0\n"
  "v_mfma_f32_16x16x32_bf16 v[84:87], v[36:39], v[64:67], 0\n"
  "ds_read_b64_tr_b16 v[78:79], v116 offset:800\n"
  "v_mov_b64 v[106:107], 0\n"
  "s_waitcnt lgkmcnt(4)\n"
  "v_mfma_f32_16x16x32_bf16 v[80:83], v[40:43], v[68:71], v[80:83]\n"
  "ds_write2_b32 v115, v104, v108 offset0:1 offset1:33\n"
  "v_mov_b64 v[110:111], 0\n"
  "v_mfma_f32_16x16x32_bf16 v[84:87], v[44:47], v[68:71], v[84:87]\n"
  "v_mov_b64 v[32:33], s[40:41]\n"
  "v_mov_b64 v[34:35], s[40:41]\n"
  "s_waitcnt lgkmcnt(3)\n"
  "v_mfma_f32_16x16x32_bf16 v[80:83], v[48:51], v[72:75], v[80:83]\n"
  "ds_read_b128 v[40:43], v121 offset:16384\n"
  "v_mov_b64 v[104:105], 0\n"
  "v_mfma_f32_16x16x32_bf16 v[84:87], v[52:55], v[72:75], v[84:87]\n"
  "ds_read_b128 v[44:47], v121 offset:17408\n"
  "v_mov_b64 v[108:109], 0\n"
  "s_waitcnt lgkmcnt(3)\n"
  "v_mfma_f32_16x16x32_bf16 v[80:83], v[56:59], v[76:79], v[80:83]\n"
  "v_mov_b64 v[36:37], 0\n"
  "v_bitop3_b32 v38, v122, -9, s6 bitop3:0x6a\n"
  "v_mfma_f32_16x16x32_bf16 v[84:87], v[60:63], v[76:79], v[84:87]\n"
  "s_waitcnt lgkmcnt(2)\n"
  "s_barrier\n"
  "v_smfmac_f32_16x16x64_bf16 v[88:91], v[28:31], a[184:191], v112\n"
  "ds_read_b64_tr_b16 v[64:65], v116 offset:0\n"
  "ds_read_b64_tr_b16 v[66:67], v116 offset:32\n"
  "v_smfmac_f32_16x16x64_bf16 v[92:95], v[8:11], a[208:215], v112\n"
  "ds_read_b64_tr_b16 v[68:69], v116 offset:256\n"
  "v_exp_f32_dpp v80, v80 quad_perm:[0,1,0,1] row_mask:0xf bank_mask:0xf\n"
  "v_smfmac_f32_16x16x64_bf16 v[92:95], v[12:15], a[216:223], v112\n"
  "ds_read_b64_tr_b16 v[70:71], v116 offset:288\n"
  "v_exp_f32_dpp v81, v81 quad_perm:[0,1,0,1] row_mask:0xf bank_mask:0xf\n"
  "v_smfmac_f32_16x16x64_bf16 v[92:95], v[16:19], a[224:231], v112\n"
  "ds_read_b64_tr_b16 v[72:73], v116 offset:512\n"
  "v_exp_f32_dpp v82, v82 quad_perm:[0,1,0,1] row_mask:0xf bank_mask:0xf\n"
  "v_smfmac_f32_16x16x64_bf16 v[92:95], v[20:23], a[232:239], v112\n"
  "ds_read_b64_tr_b16 v[74:75], v116 offset:544\n"
  "v_exp_f32_dpp v83, v83 quad_perm:[0,1,0,1] row_mask:0xf bank_mask:0xf\n"
  "v_smfmac_f32_16x16x64_bf16 v[92:95], v[24:27], a[240:247], v112\n"
  "ds_read_b64_tr_b16 v[76:77], v116 offset:768\n"
  "v_exp_f32_dpp v84, v84 quad_perm:[0,1,0,1] row_mask:0xf bank_mask:0xf\n"
  "v_smfmac_f32_16x16x64_bf16 v[92:95], v[28:31], a[248:255], v112\n"
  "ds_read_b64_tr_b16 v[78:79], v116 offset:800\n"
  "v_exp_f32_dpp v85, v85 quad_perm:[0,1,0,1] row_mask:0xf bank_mask:0xf\n"
  "s_waitcnt lgkmcnt(6)\n"
  "v_mfma_f32_16x16x32_bf16 v[60:63], v[64:67], v[64:67], 0\n"
  "v_exp_f32_dpp v86, v86 quad_perm:[0,1,0,1] row_mask:0xf bank_mask:0xf\n"
  "v_cvt_pk_bf16_f32 v80, v80, v81\n"
  "s_waitcnt lgkmcnt(4)\n"
  "v_mfma_f32_16x16x32_bf16 v[60:63], v[68:71], v[68:71], v[60:63]\n"
  "v_exp_f32_dpp v87, v87 quad_perm:[0,1,0,1] row_mask:0xf bank_mask:0xf\n"
  "v_cvt_pk_bf16_f32 v81, v82, v83\n"
  "s_waitcnt lgkmcnt(2)\n"
  "v_mfma_f32_16x16x32_bf16 v[60:63], v[72:75], v[72:75], v[60:63]\n"
  "v_cvt_pk_bf16_f32 v82, v84, v85\n"
  "v_cvt_pk_bf16_f32 v83, v86, v87\n"
  "s_waitcnt lgkmcnt(0)\n"
  "v_mfma_f32_16x16x32_bf16 v[60:63], v[76:79], v[76:79], v[60:63]\n"
  "v_add_f32 v88, v88, v90\n"
  "v_add_f32 v89, v89, v91\n"
  "v_mfma_f32_16x16x32_bf16 v[48:51], v[40:43], v[80:83], 0\n"
  "v_add_f32 v92, v92, v94\n"
  "v_add_f32 v93, v93, v95\n"
  "v_mfma_f32_16x16x32_bf16 v[52:55], v[44:47], v[80:83], 0\n"
  "s_waitcnt vmcnt(1)\n"
  "v_cmp_gt_u32 s[98:99], s57, v120\n"
  "v_exp_f32_dpp v62, v62 quad_perm:[0,1,0,1] row_mask:0xf bank_mask:0xf\n"
  "v_mfma_f32_16x16x32_bf16 v[56:59], v[32:35], v[80:83], 0\n"
  "v_exp_f32_dpp v63, v63 quad_perm:[0,1,0,1] row_mask:0xf bank_mask:0xf\n"
  "s_mov_b64 exec, s[4:5]\n"
  "v_cvt_pk_bf16_f32 v36, v62, v63\n"
  "s_mov_b64 exec, s[26:27]\n"
  "v_cvt_pk_bf16_f32 v37, v62, v63\n"
  "v_mov_b64 v[48:49], v[52:53]\n"
  "v_mov_b64 v[50:51], v[54:55]\n"
  "s_mov_b64 exec, -1\n"
  "v_mfma_f32_16x16x16_bf16 v[56:59], v[32:33], v[36:37], v[56:59]\n"
  "v_cvt_pk_bf16_f32 v88, v88, v89\n"
  "v_cvt_pk_bf16_f32 v89, v92, v93\n"
  "s_cmp_eq_u64 s[98:99], 0\n"
  "s_cselect_b32 s99, s52, 0x80000000\n"
  "v_mfma_f32_16x16x16_bf16 v[48:51], v[88:89], v[36:37], v[48:51]\n"
  "ds_write_b64 v122, v[88:89] offset:28864\n"
  "v_bitop3_b32 v39, v122, -9, s7 bitop3:0x6a\n"
  "v_bitop3_b32 v61, v122, -9, s24 bitop3:0x6a\n"
  "v_rcp_f32 v58, v56\n"
  "s_or_b32 s98, s99, s97\n"
  "s_cmp_eq_u32 s58, 63\n"
  "s_nop 1\n"
  "v_pk_mul_f32 v[48:49], v[48:49], v[58:59] op_sel_hi:[1,0]\n"
  "v_pk_mul_f32 v[50:51], v[50:51], v[58:59] op_sel_hi:[1,0]\n"
  "v_cvt_pk_bf16_f32 v48, v48, v49\n"
  "v_cvt_pk_bf16_f32 v49, v50, v51\n"
  "ds_write_b64 v122, v[48:49] offset:0\n"
  "v_max_f32 v57, v56, v58\n"
  "v_cmp_ngt_f32 vcc, s42, v57\n"
  "v_mov_b32_dpp v84, v48 quad_perm:[0,1,0,1] row_mask:0xf bank_mask:0xf\n"
  "v_mov_b32_dpp v85, v49 quad_perm:[0,1,0,1] row_mask:0xf bank_mask:0xf\n"
  "v_mov_b32_dpp v86, v48 quad_perm:[2,3,2,3] row_mask:0xf bank_mask:0xf\n"
  "v_mov_b32_dpp v87, v49 quad_perm:[2,3,2,3] row_mask:0xf bank_mask:0xf\n"
  "ds_read_b128 v[8:11], v114 offset:18432\n"
  "ds_read_b128 v[12:15], v114 offset:19456\n"
  "v_smfmac_f32_16x16x64_bf16 v[96:99], v[84:87], v[192:199], v112\n"
  "s_cbranch_vccnz .Lch_slow\n"
  ".Lch_join:\n"
  "s_waitcnt lgkmcnt(2)\n"
  "s_barrier\n"
  "ds_read_b128 v[64:67], v38 offset:0\n"
  "ds_read_b128 v[68:71], v39 offset:0\n"
  "ds_read_b128 v[72:75], v61 offset:0\n"
  "s_cbranch_scc1 .Lch_last\n"
  "v_smfmac_f32_16x16x64_bf16 v[100:103], v[84:87], v[200:207], v112\n"
  "buffer_load_dwordx4 v[80:83], v113, s[36:39], s98 offen offset:0 sc1\n"
  "v_smfmac_f32_16x16x64_bf16 v[104:107], v[84:87], v[0:7], v112\n"
  "ds_read_b128 v[16:19], v114 offset:20480\n"
  "ds_read_b128 v[20:23], v114 offset:21504\n"
  "s_waitcnt lgkmcnt(5)\n"
  "v_smfmac_f32_16x16x64_bf16 v[108:111], v[84:87], v[8:15], v112\n"
  "buffer_load_dwordx4 v[92:95], v113, s[36:39], s98 offen offset:3072 sc1\n"
  "s_waitcnt lgkmcnt(4)\n"
  "v_smfmac_f32_16x16x64_bf16 v[96:99], v[64:67], v[208:215], v112\n"
  "ds_read_b128 v[32:35], v114 offset:24576\n"
  "ds_read_b128 v[36:39], v114 offset:25600\n"
  "s_waitcnt lgkmcnt(5)\n"
  "v_smfmac_f32_16x16x64_bf16 v[96:99], v[68:71], v[224:231], v112\n"
  "ds_read_b128 v[48:51], v114 offset:28672\n"
  "ds_read_b128 v[52:55], v114 offset:29696\n"
  "s_waitcnt lgkmcnt(6)\n"
  "v_smfmac_f32_16x16x64_bf16 v[96:99], v[72:75], v[240:247], v112\n"
  "buffer_load_dwordx4 v[88:91], v113, s[36:39], s98 offen offset:2048 sc1\n"
  "s_nop 0\n"
  "v_smfmac_f32_16x16x64_bf16 v[100:103], v[64:67], v[216:223], v112\n"
  "ds_read_b128 v[24:27], v114 offset:22528\n"
  "ds_read_b128 v[28:31], v114 offset:23552\n"
  "v_smfmac_f32_16x16x64_bf16 v[100:103], v[68:71], v[232:239], v112\n"
  "ds_read_b128 v[40:43], v114 offset:26624\n"
  "s_nop 0\n"
  "v_cvt_pk_bf16_f32 v0, v96, v98\n"
  "v_smfmac_f32_16x16x64_bf16 v[100:103], v[72:75], v[248:255], v112\n"
  "ds_read_b128 v[44:47], v114 offset:27648\n"
  "v_cvt_pk_bf16_f32 v2, v97, v99\n"
  "s_waitcnt lgkmcnt(8)\n"
  "v_smfmac_f32_16x16x64_bf16 v[104:107], v[64:67], v[16:23], v112\n"
  "ds_read_b128 v[56:59], v114 offset:30720\n"
  "v_and_or_b32 v0, v0, -2, s87\n"
  "s_waitcnt lgkmcnt(7)\n"
  "v_smfmac_f32_16x16x64_bf16 v[104:107], v[68:71], v[32:39], v112\n"
  "ds_read_b128 v[60:63], v114 offset:31744\n"
  "v_and_or_b32 v2, v2, -2, s87\n"
  "s_waitcnt lgkmcnt(6)\n"
  "v_smfmac_f32_16x16x64_bf16 v[104:107], v[72:75], v[48:55], v112\n"
  "v_cvt_pk_bf16_f32 v1, v100, v102\n"
  "s_waitcnt lgkmcnt(4)\n"
  "v_smfmac_f32_16x16x64_bf16 v[108:111], v[64:67], v[24:31], v112\n"
  "s_nop 0\n"
  "v_cvt_pk_bf16_f32 v3, v101, v103\n"
  "s_waitcnt lgkmcnt(2)\n"
  "v_smfmac_f32_16x16x64_bf16 v[108:111], v[68:71], v[40:47], v112\n"
  "buffer_store_dwordx4 v[0:3], v118, s[36:39], s53 offen offset:0 sc1\n"
  "v_cvt_pk_bf16_f32 v4, v104, v106\n"
  "s_waitcnt lgkmcnt(0)\n"
  "v_smfmac_f32_16x16x64_bf16 v[108:111], v[72:75], v[56:63], v112\n"
  "s_nop 1\n"
  "v_cvt_pk_bf16_f32 v6, v105, v107\n"
  "v_and_or_b32 v4, v4, -2, s87\n"
  "v_and_or_b32 v6, v6, -2, s87\n"
  "s_nop 2\n"
  "v_cvt_pk_bf16_f32 v5, v108, v110\n"
  "v_cvt_pk_bf16_f32 v7, v109, v111\n"
  "buffer_store_dwordx4 v[4:7], v118, s[36:39], s65 offen offset:0 sc1\n"
  "buffer_load_dwordx4 v[84:87], v113, s[36:39], s98 offen offset:1024 sc1\n"
  "v_readfirstlane_b32 s84, v117\n"
  "v_readlane_b32 s45, v120, s47\n"
  "s_add_u32 s84, s84, s45\n"
  "s_cmp_gt_u32 s50, 1\n"
  "s_nop 2\n"
  "s_cbranch_scc1 .Lch_x\n"
  "buffer_load_dword v120, v119, s[36:39], 0 offen sc1\n"
  "s_add_u32 s62, s53, s54\n"
  "s_cmp_lg_u32 s58, 0\n"
  "s_cbranch_scc1 .Lch_s0_e\n"
  "v_lshlrev_b32 v104, 3, v113\n"
  "v_and_b32 v104, 0xf80, v104\n"
  "v_add_u32 v104, s95, v104\n"
  "s_nop 1\n"
  ".Lch_s0:\n"
  "buffer_load_dword v105, v104, s[36:39], 0 offen sc1\n"
  "s_waitcnt vmcnt(0)\n"
  "v_cmp_gt_u32 vcc, 1, v105\n"
  "s_cbranch_vccz .Lch_s0_ok\n"
  "s_sub_u32 s59, s59, 1\n"
  "s_cbranch_scc1 .Lch_end\n"
  "s_sleep 2\n"
  "s_branch .Lch_s0\n"
  ".Lch_s0_ok:\n"
  "v_max_u32_dpp v105, v105, v105 row_ror:8 row_mask:0xf bank_mask:0xf\n"
  "s_nop 1\n"
  "v_max_u32_dpp v105, v105, v105 row_ror:4 row_mask:0xf bank_mask:0xf\n"
  "s_nop 1\n"
  "v_max_u32_dpp v105, v105, v105 row_ror:2 row_mask:0xf bank_mask:0xf\n"
  "s_nop 1\n"
  "v_max_u32_dpp v105, v105, v105 row_ror:1 row_mask:0xf bank_mask:0xf\n"
  "s_nop 0\n"
  "v_readlane_b32 s61, v105, 0\n"
  "v_readlane_b32 s65, v105, 16\n"
  "s_nop 1\n"
  "s_max_u32 s61, s61, s65\n"
  "s_add_u32 s84, s84, s61\n"
  "s_add_u32 s84, s84, s94\n"
  "s_waitcnt lgkmcnt(0)\n"
  "s_memrealtime s[82:83]\n"
  "s_waitcnt lgkmcnt(0)\n"
  ".Lch_u1a:\n"
  "s_sub_u32 s85, s82, s84\n"
  ".Lch_u1b:\n"
  "s_cmp_ge_i32 s85, 0\n"
  "s_cbranch_scc1 .Lch_u1c\n"
  "s_memrealtime s[82:83]\n"
  "s_waitcnt lgkmcnt(0)\n"
  "s_sub_u32 s85, s82, s84\n"
  "s_branch .Lch_u1b\n"
  ".Lch_u1c:\n"
  "buffer_load_dword v120, v119, s[36:39], 0 offen sc1\n"
  "s_add_u32 s84, s84, 36\n"
  "s_waitcnt lgkmcnt(0)\n"
  "s_memrealtime s[82:83]\n"
  "s_waitcnt lgkmcnt(0)\n"
  ".Lch_u2a:\n"
  "s_sub_u32 s85, s82, s84\n"
  ".Lch_u2b:\n"
  "s_cmp_ge_i32 s85, 0\n"
  "s_cbranch_scc1 .Lch_u2c\n"
  "s_memrealtime s[82:83]\n"
  "s_waitcnt lgkmcnt(0)\n"
  "s_sub_u32 s85, s82, s84\n"
  "s_branch .Lch_u2b\n"
  ".Lch_u2c:\n"
  "s_add_u32 s84, s84, s96\n"
  "s_mov_b32 s48, 100\n"
  "s_branch .Lch_s0_f\n"
  ".Lch_s0_e:\n"
  "s_waitcnt lgkmcnt(0)\n"
  "s_memrealtime s[82:83]\n"
  "s_waitcnt lgkmcnt(0)\n"
  "s_sub_u32 s48, s82, s84\n"
  ".Lch_u3a:\n"
  "s_sub_u32 s85, s82, s84\n"
  "s_cmp_lt_i32 s85, 4\n"
  "s_cbranch_scc1 .Lch_u3b\n"
  "s_add_u32 s84, s84, 48\n"
  "s_branch .Lch_u3a\n"
  ".Lch_u3b:\n"
  "s_cmp_ge_i32 s85, 0\n"
  "s_cbranch_scc1 .Lch_u3c\n"
  "s_memrealtime s[82:83]\n"
  "s_waitcnt lgkmcnt(0)\n"
  "s_sub_u32 s85, s82, s84\n"
  "s_branch .Lch_u3b\n"
  ".Lch_u3c:\n"
  ".Lch_s0_f:\n"
  "buffer_load_dwordx4 v[96:99], v113, s[36:39], s62 offen offset:0 sc1\n"
  "buffer_load_dwordx4 v[100:103], v113, s[36:39], s62 offen offset:1024 sc1\n"
  "buffer_load_dwordx4 v[104:107], v113, s[36:39], s62 offen offset:2048 sc1\n"
  "buffer_load_dwordx4 v[108:111], v113, s[36:39], s62 offen offset:3072 sc1\n"
  "s_add_u32 s61, s84, 52\n"
  "v_mov_b32 v5, s61\n"
  "ds_max_u32 v113, v5 offset:30912\n"
  "ds_read_b128 v[32:35], v114 offset:0\n"
  "ds_read_b128 v[36:39], v114 offset:1024\n"
  "ds_read_b128 v[40:43], v114 offset:2048\n"
  "ds_read_b128 v[44:47], v114 offset:3072\n"
  "ds_read_b128 v[48:51], v114 offset:4096\n"
  "ds_read_b128 v[52:55], v114 offset:5120\n"
  "ds_read_b128 v[56:59], v114 offset:6144\n"
  "ds_read_b128 v[60:63], v114 offset:7168\n"
  "ds_read_b128 v[64:67], v114 offset:8192\n"
  "ds_read_b128 v[68:71], v114 offset:9216\n"
  "ds_read_b128 v[72:75], v114 offset:10240\n"
  "ds_read_b128 v[76:79], v114 offset:11264\n"
  "s_cmp_lt_i32 s48, -7\n"
  "s_cselect_b32 s61, s68, 0x80000000\n"
  "s_add_u32 s48, s48, s45\n"
  "v_mov_b32 v4, s48\n"
  "s_mov_b64 exec, 1\n"
  "buffer_store_dword v4, off, s[36:39], s61 sc1\n"
  "s_mov_b64 exec, -1\n"
  "s_waitcnt vmcnt(5)\n"
  "v_cmp_gt_u32 vcc, s57, v120\n"
  "v_max_u32_dpp v4, v120, v120 row_ror:8 row_mask:0xf bank_mask:0xf\n"
  "s_nop 1\n"
  "s_cmp_eq_u64 vcc, 0\n"
  "s_cselect_b32 s63, 0, 0x80000000\n"
  "v_or_b32 v84, s63, v121\n"
  "buffer_load_dwordx4 v[8:11], v84, s[36:39], s52 offen offset:0 sc1\n"
  "buffer_load_dwordx4 v[12:15], v84, s[36:39], s52 offen offset:1024 sc1\n"
  "s_add_u32 s61, s52, 4096\n"
  "buffer_load_dwordx4 v[16:19], v84, s[36:39], s61 offen offset:0 sc1\n"
  "buffer_load_dwordx4 v[20:23], v84, s[36:39], s61 offen offset:1024 sc1\n"
  "s_add_u32 s61, s52, 16384\n"
  "buffer_load_dwordx4 v[24:27], v84, s[36:39], s61 offen offset:0 sc1\n"
  "buffer_load_dwordx4 v[28:31], v84, s[36:39], s61 offen offset:1024 sc1\n"
  "s_add_u32 s61, s52, 20480\n"
  "buffer_load_dwordx4 v[80:83], v84, s[36:39], s61 offen offset:0 sc1\n"
  "buffer_load_dwordx4 v[84:87], v84, s[36:39], s61 offen offset:1024 sc1\n"
  "s_cbranch_vccz .Lch_k1\n"
  "buffer_load_dword v120, v119, s[36:39], 0 offen sc1\n"
  ".Lch_k1:\n"
  "v_max_u32_dpp v4, v4, v4 row_ror:4 row_mask:0xf bank_mask:0xf\n"
  "s_nop 1\n"
  "v_max_u32_dpp v4, v4, v4 row_ror:2 row_mask:0xf bank_mask:0xf\n"
  "s_nop 1\n"
  "v_max_u32_dpp v4, v4, v4 row_ror:1 row_mask:0xf bank_mask:0xf\n"
  "s_nop 0\n"
  "v_readlane_b32 s61, v4, 16\n"
  "v_readlane_b32 s65, v4, 32\n"
  "s_nop 1\n"
  "s_max_u32 s61, s61, s65\n"
  "s_min_u32 s65, s61, s92\n"
  "s_mov_b32 s92, s61\n"
  "s_mov_b32 s61, s65\n"
  "s_add_u32 s61, s61, 18\n"
  "s_sub_u32 s46, s46, 1\n"
  "s_max_u32 s46, s46, s61\n"
  "s_max_u32 s46, s46, 120\n"
  "s_min_u32 s46, s46, 200\n"
  ".Lch_c2:\n"
  "s_waitcnt vmcnt(12)\n"
  "v_mfma_f32_16x16x32_bf16 v[0:3], v[124:127], v[96:99], 0\n"
  "v_bitop3_b32 v4, v96, v98, s87 bitop3:0x7e\n"
  "s_waitcnt vmcnt(11)\n"
  "v_mfma_f32_16x16x32_bf16 v[0:3], v[124:127], v[100:103], v[0:3]\n"
  "v_bitop3_b32 v5, v100, v102, s87 bitop3:0x7e\n"
  "s_waitcnt vmcnt(10)\n"
  "v_mfma_f32_16x16x32_bf16 v[0:3], v[124:127], v[104:107], v[0:3]\n"
  "v_bitop3_b32 v6, v104, v106, s87 bitop3:0x7e\n"
  "s_waitcnt vmcnt(9)\n"
  "v_mfma_f32_16x16x32_bf16 v[0:3], v[124:127], v[108:111], v[0:3]\n"
  "s_nop 0\n"
  "v_bitop3_b32 v7, v108, v110, s87 bitop3:0x7e\n"
  "v_or3_b32 v4, v4, v5, v6\n"
  "v_bitop3_b32 v4, v4, v7, 1 bitop3:0xa8\n"
  "v_cmp_ne_u32 vcc, 0, v4\n"
  "s_nop 2\n"
  "v_add_f32_dpp v4, v0, v0 row_ror:8 row_mask:0xf bank_mask:0xf\n"
  "v_add_f32_dpp v5, v1, v1 row_ror:8 row_mask:0xf bank_mask:0xf\n"
  "v_add_f32_dpp v6, v2, v2 row_ror:8 row_mask:0xf bank_mask:0xf\n"
  "v_add_f32_dpp v7, v3, v3 row_ror:8 row_mask:0xf bank_mask:0xf\n"
  "v_cvt_pk_bf16_f32 v0, v4, v5\n"
  "v_cvt_pk_bf16_f32 v1, v6, v7\n"
  "s_cbranch_vccnz .Lch_c2m\n"
  ".Lch_c3:\n"
  "buffer_store_dwordx2 v[0:1], v123, s[36:39], s55 offen sc1\n"
  "s_xor_b32 s61, s55, 65536\n"
  "v_mov_b64 v[2:3], -1\n"
  "buffer_store_dwordx2 v[2:3], v123, s[36:39], s61 offen sc1\n"
  "s_lshl_b32 s61, s87, 2\n"
  "s_add_u32 s61, s61, s44\n"
  "v_mov_b32 v4, s46\n"
  "s_mov_b64 exec, 1\n"
  "buffer_store_dword v4, off, s[36:39], s61 sc1\n"
  "s_mov_b64 exec, -1\n"
  "v_mov_b64 v[96:97], 0\n"
  "v_mov_b64 v[98:99], 0\n"
  "v_mov_b64 v[100:101], 0\n"
  "v_mov_b64 v[102:103], 0\n"
  "v_mov_b64 v[104:105], 0\n"
  "v_mov_b64 v[106:107], 0\n"
  "v_mov_b64 v[108:109], 0\n"
  "v_mov_b64 v[110:111], 0\n"
  "s_cmp_eq_u32 s63, 0\n"
  "s_cbranch_scc0 .Lch_k2\n"
  "s_barrier\n"
  ".Lch_c4:\n"
  "s_waitcnt vmcnt(10)\n"
  "ds_write_b128 v121, v[8:11] offset:8192\n"
  "s_waitcnt vmcnt(9)\n"
  "ds_write_b128 v121, v[12:15] offset:9216\n"
  "s_waitcnt vmcnt(8)\n"
  "ds_write_b128 v121, v[16:19] offset:12288\n"
  "s_waitcnt vmcnt(7)\n"
  "ds_write_b128 v121, v[20:23] offset:13312\n"
  "s_add_u32 s84, s84, 52\n"
  "s_waitcnt lgkmcnt(0)\n"
  "s_memrealtime s[82:83]\n"
  "s_waitcnt lgkmcnt(0)\n"
  ".Lch_u4a:\n"
  "s_sub_u32 s85, s82, s84\n"
  ".Lch_u4b:\n"
  "s_cmp_ge_i32 s85, 0\n"
  "s_cbranch_scc1 .Lch_u4c\n"
  "s_memrealtime s[82:83]\n"
  "s_waitcnt lgkmcnt(0)\n"
  "s_sub_u32 s85, s82, s84\n"
  "s_branch .Lch_u4b\n"
  ".Lch_u4c:\n"
  "buffer_load_dwordx4 v[0:3], v121, s[36:39], s55 offen offset:0 sc1\n"
  "buffer_load_dwordx4 v[4:7], v121, s[36:39], s55 offen offset:1024 sc1\n"
  "s_waitcnt vmcnt(8)\n"
  "ds_write_b128 v121, v[24:27] offset:16384\n"
  "s_waitcnt vmcnt(7)\n"
  "ds_write_b128 v121, v[28:31] offset:17408\n"
  "s_waitcnt vmcnt(6)\n"
  "ds_write_b128 v121, v[80:83] offset:20480\n"
  "s_waitcnt vmcnt(5)\n"
  "ds_write_b128 v121, v[84:87] offset:21504\n"
  "ds_read_b128 v[80:83], v114 offset:12288\n"
  "ds_read_b128 v[84:87], v114 offset:13312\n"
  ".Lch_next:\n"
  "s_add_u32 s52, s52, 0x200000\n"
  "s_xor_b32 s52, s52, 8192\n"
  "s_bitset0_b32 s52, 26\n"
  "s_add_u32 s57, s57, 1\n"
  "s_add_u32 s58, s58, 1\n"
  "s_mov_b32 s91, s58\n"
  ".Lch_go:\n"
  "v_xor_b32 v12, 2048, v121\n"
  "v_xor_b32 v20, 4096, v121\n"
  "v_xor_b32 v28, 6144, v121\n"
  "s_waitcnt lgkmcnt(0)\n"
  "s_branch .Lch_loop\n"
  ".Lch_c2m:\n"
  "s_sub_u32 s59, s59, 1\n"
  "s_cbranch_scc1 .Lch_end\n"
  "s_add_u32 s61, s84, 100\n"
  "v_mov_b32 v5, s61\n"
  "ds_max_u32 v113, v5 offset:30912\n"
  "s_cmp_eq_u32 s63, 0\n"
  "s_cbranch_scc1 .Lch_k4\n"
  "s_waitcnt vmcnt(0)\n"
  "v_cmp_gt_u32 vcc, s57, v120\n"
  "s_cbranch_vccz .Lch_k3\n"
  "buffer_load_dword v120, v119, s[36:39], 0 offen sc1\n"
  ".Lch_k4:\n"
  "s_waitcnt lgkmcnt(0)\n"
  "s_memrealtime s[82:83]\n"
  "s_waitcnt lgkmcnt(0)\n"
  ".Lch_u5a:\n"
  "s_sub_u32 s85, s82, s84\n"
  "s_cmp_lt_i32 s85, 21\n"
  "s_cbranch_scc1 .Lch_u5b\n"
  "s_add_u32 s84, s84, 48\n"
  "s_branch .Lch_u5a\n"
  ".Lch_u5b:\n"
  "s_cmp_ge_i32 s85, 0\n"
  "s_cbranch_scc1 .Lch_u5c\n"
  "s_memrealtime s[82:83]\n"
  "s_waitcnt lgkmcnt(0)\n"
  "s_sub_u32 s85, s82, s84\n"
  "s_branch .Lch_u5b\n"
  ".Lch_u5c:\n"
  "buffer_load_dwordx4 v[96:99], v113, s[36:39], s62 offen offset:0 sc1\n"
  "buffer_load_dwordx4 v[100:103], v113, s[36:39], s62 offen offset:1024 sc1\n"
  "buffer_load_dwordx4 v[104:107], v113, s[36:39], s62 offen offset:2048 sc1\n"
  "buffer_load_dwordx4 v[108:111], v113, s[36:39], s62 offen offset:3072 sc1\n"
  "s_add_u32 s61, s84, 52\n"
  "v_mov_b32 v5, s61\n"
  "ds_max_u32 v113, v5 offset:30912\n"
  "s_waitcnt vmcnt(0)\n"
  "s_branch .Lch_c2\n"
  ".Lch_k3:\n"
  "s_waitcnt lgkmcnt(0)\n"
  "s_memrealtime s[82:83]\n"
  "s_waitcnt lgkmcnt(0)\n"
  ".Lch_u6a:\n"
  "s_sub_u32 s85, s82, s84\n"
  "s_cmp_lt_i32 s85, 21\n"
  "s_cbranch_scc1 .Lch_u6b\n"
  "s_add_u32 s84, s84, 48\n"
  "s_branch .Lch_u6a\n"
  ".Lch_u6b:\n"
  "s_cmp_ge_i32 s85, 0\n"
  "s_cbranch_scc1 .Lch_u6c\n"
  "s_memrealtime s[82:83]\n"
  "s_waitcnt lgkmcnt(0)\n"
  "s_sub_u32 s85, s82, s84\n"
  "s_branch .Lch_u6b\n"
  ".Lch_u6c:\n"
  "buffer_load_dwordx4 v[96:99], v113, s[36:39], s62 offen offset:0 sc1\n"
  "buffer_load_dwordx4 v[100:103], v113, s[36:39], s62 offen offset:1024 sc1\n"
  "buffer_load_dwordx4 v[104:107], v113, s[36:39], s62 offen offset:2048 sc1\n"
  "buffer_load_dwordx4 v[108:111], v113, s[36:39], s62 offen offset:3072 sc1\n"
  "s_add_u32 s61, s84, 52\n"
  "v_mov_b32 v5, s61\n"
  "ds_max_u32 v113, v5 offset:30912\n"
  "buffer_load_dwordx4 v[8:11], v121, s[36:39], s52 offen offset:0 sc1\n"
  "buffer_load_dwordx4 v[12:15], v121, s[36:39], s52 offen offset:1024 sc1\n"
  "s_add_u32 s61, s52, 4096\n"
  "buffer_load_dwordx4 v[16:19], v121, s[36:39], s61 offen offset:0 sc1\n"
  "buffer_load_dwordx4 v[20:23], v121, s[36:39], s61 offen offset:1024 sc1\n"
  "s_add_u32 s61, s52, 16384\n"
  "buffer_load_dwordx4 v[24:27], v121, s[36:39], s61 offen offset:0 sc1\n"
  "buffer_load_dwordx4 v[28:31], v121, s[36:39], s61 offen offset:1024 sc1\n"
  "s_add_u32 s61, s52, 20480\n"
  "buffer_load_dwordx4 v[80:83], v121, s[36:39], s61 offen offset:0 sc1\n"
  "buffer_load_dwordx4 v[84:87], v121, s[36:39], s61 offen offset:1024 sc1\n"
  "s_mov_b32 s63, 0\n"
  "s_waitcnt vmcnt(8)\n"
  "s_branch .Lch_c2\n"
  ".Lch_retry:\n"
  "s_sub_u32 s59, s59, 1\n"
  "s_cbranch_scc1 .Lch_end\n"
  "s_waitcnt lgkmcnt(0)\n"
  "s_memrealtime s[82:83]\n"
  "s_waitcnt lgkmcnt(0)\n"
  ".Lch_u7a:\n"
  "s_sub_u32 s85, s82, s84\n"
  "s_cmp_lt_i32 s85, 21\n"
  "s_cbranch_scc1 .Lch_u7b\n"
  "s_add_u32 s84, s84, 48\n"
  "s_branch .Lch_u7a\n"
  ".Lch_u7b:\n"
  "s_cmp_ge_i32 s85, 0\n"
  "s_cbranch_scc1 .Lch_u7c\n"
  "s_memrealtime s[82:83]\n"
  "s_waitcnt lgkmcnt(0)\n"
  "s_sub_u32 s85, s82, s84\n"
  "s_branch .Lch_u7b\n"
  ".Lch_u7c:\n"
  "buffer_load_dwordx4 v[0:3], v121, s[36:39], s55 offen offset:0 sc1\n"
  "buffer_load_dwordx4 v[4:7], v121, s[36:39], s55 offen offset:1024 sc1\n"
  "s_nop 7\n"
  "v_mov_b64 v[88:89], 0\n"
  "v_mov_b64 v[90:91], 0\n"
  "v_mov_b64 v[92:93], 0\n"
  "v_mov_b64 v[94:95], 0\n"
  "v_mov_b64 v[96:97], 0\n"
  "v_mov_b64 v[98:99], 0\n"
  "v_mov_b64 v[100:101], 0\n"
  "v_mov_b64 v[102:103], 0\n"
  "v_mov_b64 v[104:105], 0\n"
  "v_mov_b64 v[106:107], 0\n"
  "v_mov_b64 v[108:109], 0\n"
  "v_mov_b64 v[110:111], 0\n"
  "ds_read_b128 v[32:35], v114 offset:0\n"
  "ds_read_b128 v[36:39], v114 offset:1024\n"
  "s_branch .Lch_go\n"
  ".Lch_k2:\n"
  "s_waitcnt vmcnt(3)\n"
  "v_cmp_gt_u32 vcc, s57, v120\n"
  "s_cbranch_vccz .Lch_p3_ok\n"
  ".Lch_p3:\n"
  "buffer_load_dword v120, v119, s[36:39], 0 offen sc1\n"
  "s_waitcnt vmcnt(0)\n"
  "v_cmp_gt_u32 vcc, s57, v120\n"
  "s_cbranch_vccz .Lch_p3_ok\n"
  "s_sub_u32 s59, s59, 1\n"
  "s_cbranch_scc1 .Lch_end\n"
  "s_sleep 2\n"
  "s_branch .Lch_p3\n"
  ".Lch_p3_ok:\n"
  "buffer_load_dwordx4 v[8:11], v121, s[36:39], s52 offen offset:0 sc1\n"
  "buffer_load_dwordx4 v[12:15], v121, s[36:39], s52 offen offset:1024 sc1\n"
  "s_add_u32 s61, s52, 4096\n"
  "buffer_load_dwordx4 v[16:19], v121, s[36:39], s61 offen offset:0 sc1\n"
  "buffer_load_dwordx4 v[20:23], v121, s[36:39], s61 offen offset:1024 sc1\n"
  "s_add_u32 s61, s52, 16384\n"
  "buffer_load_dwordx4 v[24:27], v121, s[36:39], s61 offen offset:0 sc1\n"
  "buffer_load_dwordx4 v[28:31], v121, s[36:39], s61 offen offset:1024 sc1\n"
  "s_add_u32 s61, s52, 20480\n"
  "buffer_load_dwordx4 v[80:83], v121, s[36:39], s61 offen offset:0 sc1\n"
  "buffer_load_dwordx4 v[84:87], v121, s[36:39], s61 offen offset:1024 sc1\n"
  "s_barrier\n"
  "s_waitcnt vmcnt(0)\n"
  "s_branch .Lch_c4\n"
  ".Lch_slow:\n"
  "s_nop 7\n"
  "s_waitcnt vmcnt(0) lgkmcnt(0)\n"
  "ds_read_b128 v[96:99], v113 offset:8192\n"
  "ds_read_b128 v[100:103], v113 offset:10240\n"
  "ds_read_b128 v[104:107], v113 offset:12288\n"
  "ds_read_b128 v[108:111], v113 offset:14336\n"
  "s_waitcnt lgkmcnt(3)\n"
  "v_mfma_f32_16x16x32_bf16 v[80:83], v[96:99], v[64:67], 0\n"
  "s_waitcnt lgkmcnt(2)\n"
  "v_mfma_f32_16x16x32_bf16 v[80:83], v[100:103], v[68:71], v[80:83]\n"
  "s_waitcnt lgkmcnt(1)\n"
  "v_mfma_f32_16x16x32_bf16 v[80:83], v[104:107], v[72:75], v[80:83]\n"
  "s_waitcnt lgkmcnt(0)\n"
  "v_mfma_f32_16x16x32_bf16 v[80:83], v[108:111], v[76:79], v[80:83]\n"
  "s_nop 3\n"
  "ds_read_b128 v[96:99], v113 offset:9216\n"
  "ds_read_b128 v[100:103], v113 offset:11264\n"
  "ds_read_b128 v[104:107], v113 offset:13312\n"
  "ds_read_b128 v[108:111], v113 offset:15360\n"
  "s_waitcnt lgkmcnt(3)\n"
  "v_mfma_f32_16x16x32_bf16 v[84:87], v[96:99], v[64:67], 0\n"
  "s_waitcnt lgkmcnt(2)\n"
  "v_mfma_f32_16x16x32_bf16 v[84:87], v[100:103], v[68:71], v[84:87]\n"
  "s_waitcnt lgkmcnt(1)\n"
  "v_mfma_f32_16x16x32_bf16 v[84:87], v[104:107], v[72:75], v[84:87]\n"
  "s_waitcnt lgkmcnt(0)\n"
  "v_mfma_f32_16x16x32_bf16 v[84:87], v[108:111], v[76:79], v[84:87]\n"
  "s_nop 3\n"
  "v_mfma_f32_16x16x32_bf16 v[60:63], v[64:67], v[64:67], 0\n"
  "v_mfma_f32_16x16x32_bf16 v[60:63], v[68:71], v[68:71], v[60:63]\n"
  "v_mfma_f32_16x16x32_bf16 v[60:63], v[72:75], v[72:75], v[60:63]\n"
  "v_mfma_f32_16x16x32_bf16 v[60:63], v[76:79], v[76:79], v[60:63]\n"
  "v_mov_b64 v[96:97], 0\n"
  "v_mov_b64 v[98:99], 0\n"
  "v_mov_b64 v[100:101], 0\n"
  "v_mov_b64 v[102:103], 0\n"
  "v_mov_b64 v[104:105], 0\n"
  "v_mov_b64 v[106:107], 0\n"
  "v_mov_b64 v[108:109], 0\n"
  "v_mov_b64 v[110:111], 0\n"
  "v_max3_f32 v48, v80, v81, v82\n"
  "v_max3_f32 v49, v83, v84, v85\n"
  "v_max3_f32 v48, v48, v86, v87\n"
  "v_max3_f32 v49, v49, v62, v63\n"
  "v_max_f32 v48, v48, v49\n"
  "v_mov_b32 v49, v48\n"
  "s_nop 1\n"
  "v_permlane16_swap_b32 v48, v49\n"
  "v_max_f32 v48, v48, v49\n"
  "v_mov_b32 v49, v48\n"
  "s_nop 1\n"
  "v_permlane32_swap_b32 v48, v49\n"
  "v_max_f32 v48, v48, v49\n"
  "v_sub_f32 v80, v80, v48\n"
  "v_sub_f32 v81, v81, v48\n"
  "v_sub_f32 v82, v82, v48\n"
  "v_sub_f32 v83, v83, v48\n"
  "v_sub_f32 v84, v84, v48\n"
  "v_sub_f32 v85, v85, v48\n"
  "v_sub_f32 v86, v86, v48\n"
  "v_sub_f32 v87, v87, v48\n"
  "v_sub_f32 v62, v62, v48\n"
  "v_sub_f32 v63, v63, v48\n"
  "v_exp_f32_dpp v80, v80 quad_perm:[0,1,0,1] row_mask:0xf bank_mask:0xf\n"
  "v_exp_f32_dpp v81, v81 quad_perm:[0,1,0,1] row_mask:0xf bank_mask:0xf\n"
  "v_exp_f32_dpp v82, v82 quad_perm:[0,1,0,1] row_mask:0xf bank_mask:0xf\n"
  "v_exp_f32_dpp v83, v83 quad_perm:[0,1,0,1] row_mask:0xf bank_mask:0xf\n"
  "v_exp_f32_dpp v84, v84 quad_perm:[0,1,0,1] row_mask:0xf bank_mask:0xf\n"
  "v_exp_f32_dpp v85, v85 quad_perm:[0,1,0,1] row_mask:0xf bank_mask:0xf\n"
  "v_exp_f32_dpp v86, v86 quad_perm:[0,1,0,1] row_mask:0xf bank_mask:0xf\n"
  "v_exp_f32_dpp v87, v87 quad_perm:[0,1,0,1] row_mask:0xf bank_mask:0xf\n"
  "v_exp_f32_dpp v62, v62 quad_perm:[0,1,0,1] row_mask:0xf bank_mask:0xf\n"
  "v_exp_f32_dpp v63, v63 quad_perm:[0,1,0,1] row_mask:0xf bank_mask:0xf\n"
  "v_cvt_pk_bf16_f32 v80, v80, v81\n"
  "v_cvt_pk_bf16_f32 v81, v82, v83\n"
  "v_cvt_pk_bf16_f32 v82, v84, v85\n"
  "v_cvt_pk_bf16_f32 v83, v86, v87\n"
  "s_nop 1\n"
  "v_mfma_f32_16x16x32_bf16 v[48:51], v[40:43], v[80:83], 0\n"
  "v_mfma_f32_16x16x32_bf16 v[52:55], v[44:47], v[80:83], 0\n"
  "s_nop 2\n"
  "v_mfma_f32_16x16x32_bf16 v[56:59], v[32:35], v[80:83], 0\n"
  "s_mov_b64 exec, s[4:5]\n"
  "v_cvt_pk_bf16_f32 v36, v62, v63\n"
  "s_mov_b64 exec, s[26:27]\n"
  "v_cvt_pk_bf16_f32 v37, v62, v63\n"
  "v_mov_b64 v[48:49], v[52:53]\n"
  "v_mov_b64 v[50:51], v[54:55]\n"
  "s_mov_b64 exec, -1\n"
  "v_mfma_f32_16x16x16_bf16 v[56:59], v[32:33], v[36:37], v[56:59]\n"
  "s_nop 2\n"
  "v_mfma_f32_16x16x16_bf16 v[48:51], v[88:89], v[36:37], v[48:51]\n"
  "s_nop 3\n"
  "v_rcp_f32 v58, v56\n"
  "s_nop 2\n"
  "v_pk_mul_f32 v[48:49], v[48:49], v[58:59] op_sel_hi:[1,0]\n"
  "v_pk_mul_f32 v[50:51], v[50:51], v[58:59] op_sel_hi:[1,0]\n"
  "v_cvt_pk_bf16_f32 v48, v48, v49\n"
  "v_cvt_pk_bf16_f32 v49, v50, v51\n"
  "ds_write_b64 v122, v[48:49] offset:0\n"
  "v_bitop3_b32 v38, v122, -9, s6 bitop3:0x6a\n"
  "v_bitop3_b32 v39, v122, -9, s7 bitop3:0x6a\n"
  "v_bitop3_b32 v61, v122, -9, s24 bitop3:0x6a\n"
  "v_mov_b32_dpp v84, v48 quad_perm:[0,1,0,1] row_mask:0xf bank_mask:0xf\n"
  "v_mov_b32_dpp v85, v49 quad_perm:[0,1,0,1] row_mask:0xf bank_mask:0xf\n"
  "v_mov_b32_dpp v86, v48 quad_perm:[2,3,2,3] row_mask:0xf bank_mask:0xf\n"
  "v_mov_b32_dpp v87, v49 quad_perm:[2,3,2,3] row_mask:0xf bank_mask:0xf\n"
  "s_nop 1\n"
  "v_smfmac_f32_16x16x64_bf16 v[96:99], v[84:87], v[192:199], v112\n"
  "s_cmp_eq_u32 s58, 63\n"
  "s_nop 7\n"
  "s_waitcnt lgkmcnt(0)\n"
  "s_branch .Lch_join\n"
  ".Lch_last:\n"
  "s_cmp_lt_u32 s50, 2\n"
  "s_cbranch_scc1 .Lch_end\n"
  "buffer_load_dwordx4 v[80:83], v113, s[36:39], s98 offen offset:0 sc1\n"
  "buffer_load_dwordx4 v[92:95], v113, s[36:39], s98 offen offset:3072 sc1\n"
  "buffer_load_dwordx4 v[88:91], v113, s[36:39], s98 offen offset:2048 sc1\n"
  "buffer_load_dwordx4 v[84:87], v113, s[36:39], s98 offen offset:1024 sc1\n"
  "s_waitcnt lgkmcnt(0)\n"
  ".Lch_x:\n"
  "s_nop 7\n"
  "ds_read_b128 v[0:3], v113 offset:8192\n"
  "ds_read_b128 v[4:7], v113 offset:9216\n"
  "ds_read_b128 v[8:11], v113 offset:10240\n"
  "ds_read_b128 v[12:15], v113 offset:11264\n"
  "ds_read_b128 v[16:19], v113 offset:12288\n"
  "ds_read_b128 v[20:23], v113 offset:13312\n"
  "ds_read_b128 v[24:27], v113 offset:14336\n"
  "ds_read_b128 v[28:31], v113 offset:15360\n"
  "ds_read_b64_tr_b16 v[64:65], v116 offset:0\n"
  "ds_read_b64_tr_b16 v[66:67], v116 offset:32\n"
  "ds_read_b64_tr_b16 v[68:69], v116 offset:256\n"
  "ds_read_b64_tr_b16 v[70:71], v116 offset:288\n"
  "ds_read_b64_tr_b16 v[72:73], v116 offset:512\n"
  "ds_read_b64_tr_b16 v[74:75], v116 offset:544\n"
  "ds_read_b64_tr_b16 v[76:77], v116 offset:768\n"
  "ds_read_b64_tr_b16 v[78:79], v116 offset:800\n"
  "ds_read_b128 v[32:35], v113 offset:16384\n"
  "ds_read_b128 v[36:39], v113 offset:17408\n"
  "ds_read_b128 v[40:43], v113 offset:18432\n"
  "ds_read_b128 v[44:47], v113 offset:19456\n"
  "ds_read_b128 v[48:51], v113 offset:20480\n"
  "ds_read_b128 v[52:55], v113 offset:21504\n"
  "ds_read_b128 v[56:59], v113 offset:22528\n"
  "ds_read_b128 v[60:63], v113 offset:23552\n"
  "v_mov_b64 v[108:109], 0\n"
  "v_mov_b64 v[110:111], 0\n"
  "s_mov_b32 s49, 0\n"
  "s_bitcmp1_b32 s98, 31\n"
  "s_cbranch_scc0 .Lch_x2\n"
  ".Lch_p4:\n"
  "buffer_load_dword v120, v119, s[36:39], 0 offen sc1\n"
  "s_waitcnt vmcnt(0)\n"
  "v_cmp_gt_u32 vcc, s57, v120\n"
  "s_cbranch_vccz .Lch_p4_ok\n"
  "s_sub_u32 s59, s59, 1\n"
  "s_cbranch_scc1 .Lch_end\n"
  "s_sleep 2\n"
  "s_branch .Lch_p4\n"
  ".Lch_p4_ok:\n"
  "buffer_load_dwordx4 v[80:83], v113, s[36:39], s52 offen offset:0 sc1\n"
  "buffer_load_dwordx4 v[84:87], v113, s[36:39], s52 offen offset:1024 sc1\n"
  "buffer_load_dwordx4 v[88:91], v113, s[36:39], s52 offen offset:2048 sc1\n"
  "buffer_load_dwordx4 v[92:95], v113, s[36:39], s52 offen offset:3072 sc1\n"
  "s_waitcnt vmcnt(0)\n"
  ".Lch_x2:\n"
  "s_cmp_eq_u32 s58, 63\n"
  "s_cbranch_scc0 .Lch_x5\n"
  "s_waitcnt vmcnt(0)\n"
  ".Lch_x5:\n"
  "s_cmp_eq_u32 s50, 3\n"
  "s_cbranch_scc1 .Lch_x3\n"
  "s_waitcnt vmcnt(5) lgkmcnt(15)\n"
  "v_mfma_f32_16x16x32_bf16 v[96:99], v[0:3], v[80:83], v[124:127]\n"
  "s_waitcnt lgkmcnt(14)\n"
  "v_mfma_f32_16x16x32_bf16 v[104:107], v[64:67], v[80:83], 0\n"
  "s_waitcnt vmcnt(4)\n"
  "v_mfma_f32_16x16x32_bf16 v[96:99], v[24:27], v[92:95], v[96:99]\n"
  "s_waitcnt lgkmcnt(8)\n"
  "v_mfma_f32_16x16x32_bf16 v[104:107], v[76:79], v[92:95], v[104:107]\n"
  "s_waitcnt vmcnt(3)\n"
  "v_mfma_f32_16x16x32_bf16 v[96:99], v[16:19], v[88:91], v[96:99]\n"
  "v_mfma_f32_16x16x32_bf16 v[104:107], v[72:75], v[88:91], v[104:107]\n"
  "s_waitcnt vmcnt(0)\n"
  "v_mfma_f32_16x16x32_bf16 v[96:99], v[8:11], v[84:87], v[96:99]\n"
  "v_mfma_f32_16x16x32_bf16 v[104:107], v[68:71], v[84:87], v[104:107]\n"
  "v_and_b32 v84, 0x1ff, v122\n"
  "ds_read_b64 v[64:65], v84 offset:28864\n"
  "ds_read_b64 v[66:67], v84 offset:29376\n"
  "ds_read_b64 v[68:69], v84 offset:29888\n"
  "ds_read_b64 v[70:71], v84 offset:30400\n"
  "v_mov_b64 v[72:73], s[40:41]\n"
  "v_mov_b64 v[74:75], s[40:41]\n"
  "v_max3_f32 v86, v96, v97, v98\n"
  "v_max3_f32 v87, v99, v106, v107\n"
  "v_max_f32 v86, v86, v87\n"
  "v_mov_b32 v87, v86\n"
  "s_nop 1\n"
  "v_permlane16_swap_b32 v86, v87\n"
  "v_max_f32 v86, v86, v87\n"
  "v_mov_b32 v87, v86\n"
  "s_nop 1\n"
  "v_permlane32_swap_b32 v86, v87\n"
  "v_max_f32 v86, v86, v87\n"
  "v_mul_f32 v86, s60, v86\n"
  "v_fma_f32 v96, v96, s60, -v86\n"
  "v_fma_f32 v97, v97, s60, -v86\n"
  "v_fma_f32 v98, v98, s60, -v86\n"
  "v_fma_f32 v99, v99, s60, -v86\n"
  "v_fma_f32 v106, v106, s60, -v86\n"
  "v_fma_f32 v107, v107, s60, -v86\n"
  "v_exp_f32 v96, v96\n"
  "v_exp_f32 v97, v97\n"
  "v_exp_f32 v98, v98\n"
  "v_exp_f32 v99, v99\n"
  "v_cvt_pk_bf16_f32 v80, v96, v97\n"
  "v_cvt_pk_bf16_f32 v81, v98, v99\n"
  "s_nop 1\n"
  "v_mfma_f32_16x16x16_bf16 v[76:79], v[72:73], v[80:81], 0\n"
  "v_exp_f32 v106, v106\n"
  "s_waitcnt lgkmcnt(11)\n"
  "v_mfma_f32_16x16x16_bf16 v[0:3], v[32:33], v[80:81], 0\n"
  "v_exp_f32 v107, v107\n"
  "s_waitcnt lgkmcnt(10)\n"
  "v_mfma_f32_16x16x16_bf16 v[4:7], v[36:37], v[80:81], 0\n"
  "v_cvt_pk_bf16_f32 v108, v106, v107\n"
  "v_cvt_pk_bf16_f32 v111, v106, v107\n"
  "s_waitcnt lgkmcnt(9)\n"
  "v_mfma_f32_16x16x16_bf16 v[8:11], v[40:41], v[80:81], 0\n"
  "v_mfma_f32_16x16x16_bf16 v[76:79], v[72:73], v[108:109], v[76:79]\n"
  "s_waitcnt lgkmcnt(8)\n"
  "v_mfma_f32_16x16x16_bf16 v[12:15], v[44:45], v[80:81], 0\n"
  "ds_read_b128 v[32:35], v114 offset:0\n"
  "s_waitcnt lgkmcnt(8)\n"
  "v_mfma_f32_16x16x16_bf16 v[16:19], v[48:49], v[80:81], 0\n"
  "ds_read_b128 v[36:39], v114 offset:1024\n"
  "s_waitcnt lgkmcnt(8)\n"
  "v_mfma_f32_16x16x16_bf16 v[20:23], v[52:53], v[80:81], 0\n"
  "ds_read_b128 v[40:43], v114 offset:2048\n"
  "s_waitcnt lgkmcnt(8)\n"
  "v_mfma_f32_16x16x16_bf16 v[24:27], v[56:57], v[80:81], 0\n"
  "ds_read_b128 v[44:47], v114 offset:3072\n"
  "v_rcp_f32 v86, v76\n"
  "s_waitcnt lgkmcnt(8)\n"
  "v_mfma_f32_16x16x16_bf16 v[28:31], v[60:61], v[80:81], 0\n"
  "ds_read_b128 v[48:51], v114 offset:4096\n"
  "s_waitcnt lgkmcnt(8)\n"
  "v_mfma_f32_16x16x16_bf16 v[0:3], v[64:65], v[108:109], v[0:3]\n"
  "ds_read_b128 v[52:55], v114 offset:5120\n"
  "s_nop 0\n"
  "v_mfma_f32_16x16x16_bf16 v[4:7], v[64:65], v[110:111], v[4:7]\n"
  "ds_read_b128 v[56:59], v114 offset:6144\n"
  "s_waitcnt lgkmcnt(9)\n"
  "v_mfma_f32_16x16x16_bf16 v[8:11], v[66:67], v[108:109], v[8:11]\n"
  "ds_read_b128 v[60:63], v114 offset:7168\n"
  "s_nop 0\n"
  "v_mul_f32 v0, v0, v86\n"
  "v_mul_f32 v1, v1, v86\n"
  "v_mul_f32 v2, v2, v86\n"
  "v_mul_f32 v3, v3, v86\n"
  "v_cvt_pk_bf16_f32 v88, v0, v1\n"
  "v_cvt_pk_bf16_f32 v89, v2, v3\n"
  "v_mfma_f32_16x16x16_bf16 v[12:15], v[66:67], v[110:111], v[12:15]\n"
  "v_mul_f32 v4, v4, v86\n"
  "v_mul_f32 v5, v5, v86\n"
  "v_mul_f32 v6, v6, v86\n"
  "v_mul_f32 v7, v7, v86\n"
  "v_cvt_pk_bf16_f32 v90, v4, v5\n"
  "v_cvt_pk_bf16_f32 v91, v6, v7\n"
  "buffer_store_dwordx4 v[88:91], v113, s[36:39], s56 offen offset:0 sc1\n"
  "s_waitcnt lgkmcnt(9)\n"
  "v_mfma_f32_16x16x16_bf16 v[16:19], v[68:69], v[108:109], v[16:19]\n"
  "v_mul_f32 v8, v8, v86\n"
  "v_mul_f32 v9, v9, v86\n"
  "v_mul_f32 v10, v10, v86\n"
  "v_mul_f32 v11, v11, v86\n"
  "v_cvt_pk_bf16_f32 v92, v8, v9\n"
  "v_cvt_pk_bf16_f32 v93, v10, v11\n"
  "v_mfma_f32_16x16x16_bf16 v[20:23], v[68:69], v[110:111], v[20:23]\n"
  "v_mul_f32 v12, v12, v86\n"
  "v_mul_f32 v13, v13, v86\n"
  "v_mul_f32 v14, v14, v86\n"
  "v_mul_f32 v15, v15, v86\n"
  "v_cvt_pk_bf16_f32 v94, v12, v13\n"
  "v_cvt_pk_bf16_f32 v95, v14, v15\n"
  "buffer_store_dwordx4 v[92:95], v113, s[36:39], s56 offen offset:1024 sc1\n"
  "s_waitcnt lgkmcnt(8)\n"
  "v_mfma_f32_16x16x16_bf16 v[24:27], v[70:71], v[108:109], v[24:27]\n"
  "v_mul_f32 v16, v16, v86\n"
  "v_mul_f32 v17, v17, v86\n"
  "v_mul_f32 v18, v18, v86\n"
  "v_mul_f32 v19, v19, v86\n"
  "v_cvt_pk_bf16_f32 v96, v16, v17\n"
  "v_cvt_pk_bf16_f32 v97, v18, v19\n"
  "v_mfma_f32_16x16x16_bf16 v[28:31], v[70:71], v[110:111], v[28:31]\n"
  "v_mul_f32 v20, v20, v86\n"
  "v_mul_f32 v21, v21, v86\n"
  "v_mul_f32 v22, v22, v86\n"
  "v_mul_f32 v23, v23, v86\n"
  "v_cvt_pk_bf16_f32 v98, v20, v21\n"
  "v_cvt_pk_bf16_f32 v99, v22, v23\n"
  "buffer_store_dwordx4 v[96:99], v113, s[36:39], s56 offen offset:2048 sc1\n"
  "ds_read_b128 v[64:67], v114 offset:8192\n"
  "ds_read_b128 v[68:71], v114 offset:9216\n"
  "ds_read_b128 v[72:75], v114 offset:10240\n"
  "ds_read_b128 v[76:79], v114 offset:11264\n"
  "v_mul_f32 v24, v24, v86\n"
  "v_mul_f32 v25, v25, v86\n"
  "v_mul_f32 v26, v26, v86\n"
  "v_mul_f32 v27, v27, v86\n"
  "v_cvt_pk_bf16_f32 v100, v24, v25\n"
  "v_cvt_pk_bf16_f32 v101, v26, v27\n"
  "v_mul_f32 v28, v28, v86\n"
  "v_mul_f32 v29, v29, v86\n"
  "v_mul_f32 v30, v30, v86\n"
  "v_mul_f32 v31, v31, v86\n"
  "v_cvt_pk_bf16_f32 v102, v28, v29\n"
  "v_cvt_pk_bf16_f32 v103, v30, v31\n"
  "buffer_store_dwordx4 v[100:103], v113, s[36:39], s56 offen offset:3072 sc1\n"
  "s_cmp_eq_u32 s49, 0\n"
  "s_cbranch_scc0 .Lch_xb0\n"
  "s_barrier\n"
  ".Lch_xb0:\n"
  "s_branch .Lch_x4\n"
  ".Lch_x3:\n"
  "s_waitcnt vmcnt(5) lgkmcnt(15)\n"
  "v_mfma_f32_16x16x32_bf16 v[96:99], v[0:3], v[80:83], 0\n"
  "v_mfma_f32_16x16x32_bf16 v[100:103], v[4:7], v[80:83], v[124:127]\n"
  "s_waitcnt lgkmcnt(14)\n"
  "v_mfma_f32_16x16x32_bf16 v[104:107], v[64:67], v[80:83], 0\n"
  "s_waitcnt vmcnt(4)\n"
  "v_mfma_f32_16x16x32_bf16 v[96:99], v[24:27], v[92:95], v[96:99]\n"
  "v_mfma_f32_16x16x32_bf16 v[100:103], v[28:31], v[92:95], v[100:103]\n"
  "s_waitcnt lgkmcnt(8)\n"
  "v_mfma_f32_16x16x32_bf16 v[104:107], v[76:79], v[92:95], v[104:107]\n"
  "s_waitcnt vmcnt(3)\n"
  "v_mfma_f32_16x16x32_bf16 v[96:99], v[16:19], v[88:91], v[96:99]\n"
  "v_mfma_f32_16x16x32_bf16 v[100:103], v[20:23], v[88:91], v[100:103]\n"
  "v_mfma_f32_16x16x32_bf16 v[104:107], v[72:75], v[88:91], v[104:107]\n"
  "s_waitcnt vmcnt(0)\n"
  "v_mfma_f32_16x16x32_bf16 v[96:99], v[8:11], v[84:87], v[96:99]\n"
  "v_mfma_f32_16x16x32_bf16 v[100:103], v[12:15], v[84:87], v[100:103]\n"
  "v_mfma_f32_16x16x32_bf16 v[104:107], v[68:71], v[84:87], v[104:107]\n"
  "v_and_b32 v84, 0x1ff, v122\n"
  "ds_read_b64 v[64:65], v84 offset:28864\n"
  "ds_read_b64 v[66:67], v84 offset:29376\n"
  "ds_read_b64 v[68:69], v84 offset:29888\n"
  "ds_read_b64 v[70:71], v84 offset:30400\n"
  "v_mov_b64 v[72:73], s[40:41]\n"
  "v_mov_b64 v[74:75], s[40:41]\n"
  "v_max3_f32 v86, v96, v97, v98\n"
  "v_max3_f32 v86, v86, v100, v101\n"
  "v_max3_f32 v86, v86, v102, v103\n"
  "v_max3_f32 v87, v99, v106, v107\n"
  "v_max_f32 v86, v86, v87\n"
  "v_mov_b32 v87, v86\n"
  "s_nop 1\n"
  "v_permlane16_swap_b32 v86, v87\n"
  "v_max_f32 v86, v86, v87\n"
  "v_mov_b32 v87, v86\n"
  "s_nop 1\n"
  "v_permlane32_swap_b32 v86, v87\n"
  "v_max_f32 v86, v86, v87\n"
  "v_mul_f32 v86, s60, v86\n"
  "v_fma_f32 v96, v96, s60, -v86\n"
  "v_fma_f32 v97, v97, s60, -v86\n"
  "v_fma_f32 v98, v98, s60, -v86\n"
  "v_fma_f32 v99, v99, s60, -v86\n"
  "v_fma_f32 v100, v100, s60, -v86\n"
  "v_fma_f32 v101, v101, s60, -v86\n"
  "v_fma_f32 v102, v102, s60, -v86\n"
  "v_fma_f32 v103, v103, s60, -v86\n"
  "v_fma_f32 v106, v106, s60, -v86\n"
  "v_fma_f32 v107, v107, s60, -v86\n"
  "v_exp_f32 v96, v96\n"
  "v_exp_f32 v97, v97\n"
  "v_exp_f32 v98, v98\n"
  "v_exp_f32 v99, v99\n"
  "v_exp_f32 v100, v100\n"
  "v_exp_f32 v101, v101\n"
  "v_exp_f32 v102, v102\n"
  "v_exp_f32 v103, v103\n"
  "v_cvt_pk_bf16_f32 v80, v96, v97\n"
  "v_cvt_pk_bf16_f32 v81, v98, v99\n"
  "v_cvt_pk_bf16_f32 v82, v100, v101\n"
  "v_cvt_pk_bf16_f32 v83, v102, v103\n"
  "s_nop 1\n"
  "v_mfma_f32_16x16x32_bf16 v[76:79], v[72:75], v[80:83], 0\n"
  "v_exp_f32 v106, v106\n"
  "s_waitcnt lgkmcnt(11)\n"
  "v_mfma_f32_16x16x32_bf16 v[0:3], v[32:35], v[80:83], 0\n"
  "v_exp_f32 v107, v107\n"
  "s_waitcnt lgkmcnt(10)\n"
  "v_mfma_f32_16x16x32_bf16 v[4:7], v[36:39], v[80:83], 0\n"
  "v_cvt_pk_bf16_f32 v108, v106, v107\n"
  "v_cvt_pk_bf16_f32 v111, v106, v107\n"
  "s_waitcnt lgkmcnt(9)\n"
  "v_mfma_f32_16x16x32_bf16 v[8:11], v[40:43], v[80:83], 0\n"
  "v_mfma_f32_16x16x16_bf16 v[76:79], v[72:73], v[108:109], v[76:79]\n"
  "s_waitcnt lgkmcnt(8)\n"
  "v_mfma_f32_16x16x32_bf16 v[12:15], v[44:47], v[80:83], 0\n"
  "ds_read_b128 v[32:35], v114 offset:0\n"
  "s_waitcnt lgkmcnt(8)\n"
  "v_mfma_f32_16x16x32_bf16 v[16:19], v[48:51], v[80:83], 0\n"
  "ds_read_b128 v[36:39], v114 offset:1024\n"
  "s_waitcnt lgkmcnt(8)\n"
  "v_mfma_f32_16x16x32_bf16 v[20:23], v[52:55], v[80:83], 0\n"
  "ds_read_b128 v[40:43], v114 offset:2048\n"
  "s_waitcnt lgkmcnt(8)\n"
  "v_mfma_f32_16x16x32_bf16 v[24:27], v[56:59], v[80:83], 0\n"
  "ds_read_b128 v[44:47], v114 offset:3072\n"
  "v_rcp_f32 v86, v76\n"
  "s_waitcnt lgkmcnt(8)\n"
  "v_mfma_f32_16x16x32_bf16 v[28:31], v[60:63], v[80:83], 0\n"
  "ds_read_b128 v[48:51], v114 offset:4096\n"
  "s_waitcnt lgkmcnt(8)\n"
  "v_mfma_f32_16x16x16_bf16 v[0:3], v[64:65], v[108:109], v[0:3]\n"
  "ds_read_b128 v[52:55], v114 offset:5120\n"
  "s_nop 0\n"
  "v_mfma_f32_16x16x16_bf16 v[4:7], v[64:65], v[110:111], v[4:7]\n"
  "ds_read_b128 v[56:59], v114 offset:6144\n"
  "s_waitcnt lgkmcnt(9)\n"
  "v_mfma_f32_16x16x16_bf16 v[8:11], v[66:67], v[108:109], v[8:11]\n"
  "ds_read_b128 v[60:63], v114 offset:7168\n"
  "s_nop 0\n"
  "v_mul_f32 v0, v0, v86\n"
  "v_mul_f32 v1, v1, v86\n"
  "v_mul_f32 v2, v2, v86\n"
  "v_mul_f32 v3, v3, v86\n"
  "v_cvt_pk_bf16_f32 v88, v0, v1\n"
  "v_cvt_pk_bf16_f32 v89, v2, v3\n"
  "v_mfma_f32_16x16x16_bf16 v[12:15], v[66:67], v[110:111], v[12:15]\n"
  "v_mul_f32 v4, v4, v86\n"
  "v_mul_f32 v5, v5, v86\n"
  "v_mul_f32 v6, v6, v86\n"
  "v_mul_f32 v7, v7, v86\n"
  "v_cvt_pk_bf16_f32 v90, v4, v5\n"
  "v_cvt_pk_bf16_f32 v91, v6, v7\n"
  "buffer_store_dwordx4 v[88:91], v113, s[36:39], s56 offen offset:0 sc1\n"
  "s_waitcnt lgkmcnt(9)\n"
  "v_mfma_f32_16x16x16_bf16 v[16:19], v[68:69], v[108:109], v[16:19]\n"
  "v_mul_f32 v8, v8, v86\n"
  "v_mul_f32 v9, v9, v86\n"
  "v_mul_f32 v10, v10, v86\n"
  "v_mul_f32 v11, v11, v86\n"
  "v_cvt_pk_bf16_f32 v92, v8, v9\n"
  "v_cvt_pk_bf16_f32 v93, v10, v11\n"
  "v_mfma_f32_16x16x16_bf16 v[20:23], v[68:69], v[110:111], v[20:23]\n"
  "v_mul_f32 v12, v12, v86\n"
  "v_mul_f32 v13, v13, v86\n"
  "v_mul_f32 v14, v14, v86\n"
  "v_mul_f32 v15, v15, v86\n"
  "v_cvt_pk_bf16_f32 v94, v12, v13\n"
  "v_cvt_pk_bf16_f32 v95, v14, v15\n"
  "buffer_store_dwordx4 v[92:95], v113, s[36:39], s56 offen offset:1024 sc1\n"
  "s_waitcnt lgkmcnt(8)\n"
  "v_mfma_f32_16x16x16_bf16 v[24:27], v[70:71], v[108:109], v[24:27]\n"
  "v_mul_f32 v16, v16, v86\n"
  "v_mul_f32 v17, v17, v86\n"
  "v_mul_f32 v18, v18, v86\n"
  "v_mul_f32 v19, v19, v86\n"
  "v_cvt_pk_bf16_f32 v96, v16, v17\n"
  "v_cvt_pk_bf16_f32 v97, v18, v19\n"
  "v_mfma_f32_16x16x16_bf16 v[28:31], v[70:71], v[110:111], v[28:31]\n"
  "v_mul_f32 v20, v20, v86\n"
  "v_mul_f32 v21, v21, v86\n"
  "v_mul_f32 v22, v22, v86\n"
  "v_mul_f32 v23, v23, v86\n"
  "v_cvt_pk_bf16_f32 v98, v20, v21\n"
  "v_cvt_pk_bf16_f32 v99, v22, v23\n"
  "buffer_store_dwordx4 v[96:99], v113, s[36:39], s56 offen offset:2048 sc1\n"
  "ds_read_b128 v[64:67], v114 offset:8192\n"
  "ds_read_b128 v[68:71], v114 offset:9216\n"
  "ds_read_b128 v[72:75], v114 offset:10240\n"
  "ds_read_b128 v[76:79], v114 offset:11264\n"
  "v_mul_f32 v24, v24, v86\n"
  "v_mul_f32 v25, v25, v86\n"
  "v_mul_f32 v26, v26, v86\n"
  "v_mul_f32 v27, v27, v86\n"
  "v_cvt_pk_bf16_f32 v100, v24, v25\n"
  "v_cvt_pk_bf16_f32 v101, v26, v27\n"
  "v_mul_f32 v28, v28, v86\n"
  "v_mul_f32 v29, v29, v86\n"
  "v_mul_f32 v30, v30, v86\n"
  "v_mul_f32 v31, v31, v86\n"
  "v_cvt_pk_bf16_f32 v102, v28, v29\n"
  "v_cvt_pk_bf16_f32 v103, v30, v31\n"
  "buffer_store_dwordx4 v[100:103], v113, s[36:39], s56 offen offset:3072 sc1\n"
  "s_cmp_eq_u32 s49, 0\n"
  "s_cbranch_scc0 .Lch_xb1\n"
  "s_barrier\n"
  ".Lch_xb1:\n"
  ".Lch_x4:\n"
  "ds_read_b32 v104, v113 offset:30912\n"
  "ds_read_b128 v[80:83], v114 offset:12288\n"
  "ds_read_b128 v[84:87], v114 offset:13312\n"
  "s_add_u32 s56, s56, 0x200000\n"
  "s_xor_b32 s56, s56, 8192\n"
  "s_bitset0_b32 s56, 26\n"
  "s_cmp_eq_u32 s58, 63\n"
  "s_cbranch_scc1 .Lch_fin\n"
  "s_add_u32 s84, s84, 52\n"
  "s_waitcnt lgkmcnt(2)\n"
  "v_readfirstlane_b32 s61, v104\n"
  "s_max_u32 s84, s84, s61\n"
  "s_waitcnt lgkmcnt(0)\n"
  "s_memrealtime s[82:83]\n"
  "s_waitcnt lgkmcnt(0)\n"
  ".Lch_u8a:\n"
  "s_sub_u32 s85, s82, s84\n"
  ".Lch_u8b:\n"
  "s_cmp_ge_i32 s85, 0\n"
  "s_cbranch_scc1 .Lch_u8c\n"
  "s_memrealtime s[82:83]\n"
  "s_waitcnt lgkmcnt(0)\n"
  "s_sub_u32 s85, s82, s84\n"
  "s_branch .Lch_u8b\n"
  ".Lch_u8c:\n"
  "buffer_load_dwordx4 v[0:3], v121, s[36:39], s55 offen offset:0 sc1\n"
  "buffer_load_dwordx4 v[4:7], v121, s[36:39], s55 offen offset:1024 sc1\n"
  "s_mov_b32 s43, 0\n"
  "v_mov_b64 v[88:89], 0\n"
  "v_mov_b64 v[90:91], 0\n"
  "v_mov_b64 v[92:93], 0\n"
  "v_mov_b64 v[94:95], 0\n"
  "v_mov_b64 v[96:97], 0\n"
  "v_mov_b64 v[98:99], 0\n"
  "v_mov_b64 v[100:101], 0\n"
  "v_mov_b64 v[102:103], 0\n"
  "v_mov_b64 v[104:105], 0\n"
  "v_mov_b64 v[106:107], 0\n"
  "v_mov_b64 v[108:109], 0\n"
  "v_mov_b64 v[110:111], 0\n"
  "s_branch .Lch_next\n"
  ".Lch_fin:\n"
  "s_waitcnt vmcnt(1)\n"
  "v_mov_b32 v0, 64\n"
  "s_waitcnt vmcnt(0)\n"
  "buffer_store_dword v0, v123, s[36:39], 0 offen sc1\n"
  ".Lch_end:\n"
  "s_endpgm\n"
      : "+{v96}"(g64), "+{v97}"(g128), "+{v98}"(ta), "+{v99}"(tv), "+{v100}"(to), "+{v101}"(m0o), "+{v103}"(fcp),
        "+{s[40:41]}"(pk_), "+{s[42:43]}"(pv_), "+{s[44:45]}"(pq_), "+{s[46:47]}"(po_),
        "+{s52}"(skv), "+{s55}"(sm), "+{s56}"(so), "+{s57}"(need), "+{s58}"(st), "+{s59}"(bud), "+{s64}"(dbg), "+{s66}"(r1), "+{s67}"(r2)
      : "{v112}"(idx), "{v113}"(ll), "{v114}"(lw), "{v115}"(qw), "{v116}"(qr), "{v117}"(ow), "{v118}"(pst), "{v119}"(fa), "{v121}"(w2), "{v122}"(vmw),
        "{v123}"(rx), "{v[124:127]}"(r4), "{s[36:39]}"(rs), "{s[48:49]}"((u64)mem0), "{s50}"(RFL(w)), "{s51}"(RFL(reg)), "{s53}"(RFL(cpbase)), "{s54}"(RFL(INBOX(h) + c * 4096)),
        "{s60}"(SC), "{s68}"(RFL(w == 0 ? fl(cl, FL_CLEAN + h) + 4 : OOR)), "{s90}"(RFL(master ? fl(cl, FL_CTL) : OOR)), "{s92}"(HS), "{s93}"(RFL(l * DL1 + DCH)),
        "{s91}"(RFL(FL_C0 + (l * 32 + h) * 128 + 4 + c * 4)), "{s94}"(RFL(l * DS1 - 56)), "{s95}"(RFL(FL_C0 + l * 4096 + 4)), "{s96}"(RFL(l ? E21 : E20)), "{s69}"(RFL(OFF_DBG + (6u << 20) + (BID * 4 + w) * 32 + 16))
      : "memory", "vcc", "scc", "s61", "s62", "s63", "s65",
        "s101", "v255", "a255"
  );
  __builtin_unreachable();
}

template <int N> DEV void pull(u64 p, u32 off, int w) { asm volatile("s_mov_b32 m0, %2\n\ts_nop 0\n\tglobal_load_lds_dword %0, %1\n\ts_waitcnt vmcnt(%3)" :: "v"(off), "s"(p), "s"(w * 32768), "i"(N) : "memory"); }
#define FHD HD
#define PD0 0
#define PW0 3
DEV void pull_w(u32 hd, int odd, int hf, int j, int w, int lane, const u16* Wq, const u16* Wk, const u16* Wv, const u16* Wo, const u16* Wg, const u16* Wu, const u16* Wd) {
  const u32 i = (w * 16 + j) * 2 + (lane >> 5);
  const u32 o = odd ? (w * 128 + j * 4 + (lane >> 4)) * 8192 + hf * 4096 + (lane & 15) * 256 + (w >> 1) * 128 : i * 8192 + hf * 4096 + (lane & 31) * 128;
  u32 ta = 0;
  for (int m = 0; m < 3 + odd; ++m)
#pragma unroll
    for (int q = 0; q < 4; ++q) {
      if (m * 4 + q > HW + 1) { ta += m == 3 ? OH3 : hd; while ((int)(rt() - ta) < 0) __builtin_amdgcn_s_sleep(1); }
      const u32 oq = !odd ? o + q * 128 * 8192 : ORD == 3 ? (o + (q & 1) * 64 * 8192) ^ ((q >> 1) * 128) : o + q * 32 * 8192;
      if (m == 0 && q < HW) pull<63>((u64)Wk, oq, w);
      else pull<HW>((u64)(m == 0 ? Wk : m == 1 ? Wv : m == 2 ? Wq : Wo), m < 3 ? oq : (hf * 16 + j) * 131072 + (w * 4 + q) * 8192 + lane * 128, w);
      if (m * 4 + q == HW) ta = rt();
    }
  if ((FPW & 1) && !odd) {
    const u32 t = (j * 4 + w) * 64 + lane;
#pragma unroll
    for (int q = 0; q < 6; ++q) {
      ta += FHD; while ((int)(rt() - ta) < 0) __builtin_amdgcn_s_sleep(1);
      pull<(FPW & 8) ? 63 : HW>((u64)(q < 2 ? Wg : q < 4 ? Wu : Wd), min(q < 4 ? ((t >> 4) + (q & 1) * 256) * 2752 + hf * 1280 + min(t & 15, 12u) * 128 : hf * 655360 + min(t + (q & 1) * 4096, 6143u) * 128, 1409020u), w);
    }
  }
}
__global__ void __launch_bounds__(256, 1) mega(const u16* x, const u16* mem0, const u16* Wq, const u16* Wk, const u16* Wv, const u16* Wo,
                                               const u16* Wg, const u16* Wu, const u16* Wd, u16* out, char* ws) {
  extern __shared__ __attribute__((aligned(16))) char smem[];
  LDS char* L = (LDS char*)smem;
  const int lane = threadIdx.x & 63, w = __builtin_amdgcn_readfirstlane(threadIdx.x >> 6);
  u64 pc; u32 len;
  asm volatile("s_getpc_b64 %0\n.Lk_pc:\n\ts_max_i32 %1, .Lq_end-.Lk_pc, 0\n\ts_max_i32 %1, %1, .Lch_end-.Lk_pc\n\ts_max_i32 %1, %1, .Lff_lim-.Lk_pc" : "=s"(pc), "=s"(len) :: "scc");
  ws += (0 - (u64)ws) & 0x1fffff;
  Ctx C;
  C.ws = __builtin_amdgcn_make_buffer_rsrc(ws, 0, WS_USED, 0x00020000);
  C.budget = BUDGET;
  const int l = (BID >> 2) & 1, hf = (BID >> 1) & 1, odd = BID & 1, k = __builtin_amdgcn_readfirstlane((BID >> 3) ^ (31 * l));
  if (k < 16) {
    if (odd) role_chain(C, ws, l, 16 * hf + k, w, lane, mem0, Wq, Wk, Wv, Wo);
    role_qkv(C, ws, l, 16 * hf + k, w, lane, Wq, Wk, Wv);
  }
  const u32 t0 = rt(), v0 = ld32(C.ws, HS);
  const bool rf = !odd && k >= 27, ra = rf && k < 31 && w == 3;
  if (rf && !ra) {
    if (l == 0) { if (HLP) pull_w(HD, 0, hf, k - 16, w, lane, Wq, Wk, Wv, Wo, Wg, Wu, Wd); else pause(t0, DRF); }
    role_rf(C, ws, l, v0, k < 31 ? 3 * (4 * hf + k - 27) + w : 24 + 4 * hf + w, w, lane, x, out);
  } else {
    if (BID == MB) {
#pragma unroll
      for (int j = 0; j < 5; ++j) {
        const u32 i = (w * 5 + j) * 64 + lane, n = i >> 1;
        __builtin_amdgcn_raw_buffer_store_b64(u2v{n == FL_CTL ? A21_0 : 0, n == FL_CTL ? A21_0 : n - FL_CLEAN < 32 ? PM_0 : 0}, C.ws, fl(i, n), 0, 16);
      }
#pragma unroll
      for (u32 j = F2_XN; j < F2_END; j += 128) st32(C.ws, fl(lane, j + (threadIdx.x >> 1)), 0);
      st32(C.ws, w == 0 ? FL_X0 + (lane >> 1) * 128 + (lane & 1) * 4 : w < 3 ? FL_C0 + lane * 128 + (w & 2) * 2 : lane == 0 ? HS + 4 : OOR, 0);
      VM0(); BAR();
      st32(C.ws, threadIdx.x == 0 ? HS : OOR, v0 + 1);
    } else if (BID == MB + 8) {
      if (w == 0) {
        const u32 a = OFF_HOP + (1u << 20) + 13 * 32768;
        st32(C.ws, a + lane * 512, 0);
        u4v t = {~0u, ~0u, ~0u, ~0u};
        int s0 = 0, d = 0;
        VM0();
        for (int i = 0; i < PRB; ++i) {
#pragma unroll
          for (int c = 0; c < 4; ++c) {
            const u32 c0 = now();
            MEMB();
            const u32 v = ld32(C.ws, a + c * 8192 + (lane >> 5) * 4096 + i * 512);
            asm volatile("s_waitcnt vmcnt(0)" :: "v"(v) : "memory");
            t[c] = min(t[c], now() - c0);
          }
          const int e = min(t.x, t.z) - min(t.y, t.w);
          s0 = e < 0;
          d = t.y + t.z - t.x - t.w;
          if (s0) d = -d;
          if (i + 1 >= PROBES && abs(d) >= PRD && abs(d) < 220 && abs(e) > 100) break;
        }
        if (hs_wait(C, v0, 1)) st32(C.ws, lane == 0 ? HS + 4 : OOR, 4 + s0 + (d < 0 ? 0 : 2));
      }
      BAR();
    } else if (PFC && !ra) { pull<63>(pc, min(threadIdx.x * 128, len), w); pull<63>(pc, min(threadIdx.x * 128 + 32768, len), w); }
    if ((FPW & 2) && l && k < 27) {
      const u32 o = min((((k - 16) * 4 + (BID & 3)) * 256 + threadIdx.x) * 128, 1409020u);
      pull<63>((u64)Wg, o, w); pull<63>((u64)Wu, o, w); pull<63>((u64)Wd, o, w);
    }
    if ((FPW & 4) && !l && !odd && w < 3) pull<63>((u64)(w == 0 ? Wg : w == 1 ? Wu : Wd), lane * 22016, w);
    if ((FPW & 512) && (BID & 7) == 7) {
#pragma unroll
      for (int q = 0; q < 9; ++q) pull<63>((u64)(q < 3 ? Wg : q < 6 ? Wu : Wd), min((((k - 16) * 3 + q % 3) * 256 + threadIdx.x) * 128, 1409020u), w);
    }
    if ((BID & 7) == 7) {
      const int a = (k - 16) * 4 + w;
      const u32 cb = OFF_HOP + (a >> 5) * (4u << 20);
      const u4v ones = {~0u, ~0u, ~0u, ~0u};
#pragma unroll
      for (int j = 0; j < 4; ++j) st128(C.ws, cb + (1u << 20) + j * 8192 + (lane >> 4) * 32768 + (a & 31) * 256 + (lane & 15) * 16, ones);
#pragma unroll 4
      for (int j = 0; j < 32; ++j) st128(C.ws, cb + (a & 31) * 32768 + j * 1024 + lane * 16, ones);
      VM0();
      if (!hs_wait(C, v0, 1)) return;
      st32(C.ws, lane == 0 ? FL_C0 + a * 128 : OOR, rt() | 1);
    }
    if ((FPW & 112) && l && odd) {
      pause(t0, PD0);
#pragma unroll
      for (int q = 0; q < 12; ++q) if (FPW & (16 << (q >> 2))) {
        const u32 t = (k - 16) * 256 + threadIdx.x + (q & 3) * 4096;
        pull<PW0>((u64)(q < 4 ? Wv : q < 8 ? Wq : Wo), q < 8 ? (t >> 5) * 8192 + hf * 4096 + (t & 31) * 128 : hf * 2097152 + t * 128, w);
      }
    }
    if ((FPW & 128) && l && !odd && k < 27) {
      pause(t0, PD0);
#pragma unroll
      for (int q = 0; q < 6; ++q) pull<63>((u64)(q < 2 ? Wg : q < 4 ? Wu : Wd), min(((((k - 16) * 2 + hf) * 2 + (q & 1)) * 256 + threadIdx.x) * 128, 1409020u), w);
    }
    if ((FPW & 256) && (BID & 7) == 7) {
#pragma unroll
      for (int q = 0; q < 8; ++q) pull<PW0>((u64)Wo, (((k - 16) * 2 + (q & 1)) * 1024 + (q >> 1) * 256 + threadIdx.x) * 128, w);
    }
    if (HLP) {
      if (l) pause(t0, DL1);
      pull_w(l ? HD1 : HD, odd, hf, k - 16, w, lane, Wq, Wk, Wv, Wo, Wg, Wu, Wd);
    }
    pause(t0, ra ? DRF : l * DL1 + (odd ? DOP : DFF));
    const u32 P = hs_wait(C, v0, 0, BID == MB && lane == 0 ? HS : OOR);
    if (!P) return;
    const u32 cl = RFL((P ^ l) & 1);
    const int n = k - 16;
    if (odd) role_op(C, ws, cl, v0, 2 * hf + (n >> 3), (n >> 1) & 3, n & 1, w, lane, Wo);
    if (ra) role_ra(C, ws, l, cl, 4 * hf + k - 27, w, lane, x);
    role_ffn(C, ws, cl, (11 * hf + n) >> 1, (11 * hf + n) & 1, w, lane, Wg, Wu, Wd);
  }
}

extern "C" void kernel_launch(void* const* d_in, const int* in_sizes, int n_in, void* d_out, int out_size, void* d_ws, size_t ws_size, hipStream_t stream) {
  hipFuncSetAttribute((const void*)mega, hipFuncAttributeMaxDynamicSharedMemorySize, 163840);
  mega<<<256, 256, 163840, stream>>>((const u16*)d_in[0], (const u16*)d_in[1], (const u16*)d_in[2], (const u16*)d_in[3], (const u16*)d_in[4], (const u16*)d_in[5],
                                     (const u16*)d_in[8], (const u16*)d_in[9], (const u16*)d_in[10], (u16*)d_out, (char*)d_ws);
}
```
